```python
import torch

def _softmax(x, axis=-1):
    return torch.softmax(x, dim=axis)

def _gelu(x):
    return torch.nn.functional.gelu(x, approximate='tanh')

def _silu(x):
    return torch.nn.functional.silu(x)

def _sigmoid(x):
    return torch.sigmoid(x)

def _layernorm(x, axis=-1, eps=1e-05):
    m = x.mean(dim=axis, keepdim=True)
    v = x.var(dim=axis, keepdim=True, unbiased=False)
    return (x - m) / torch.sqrt(v + eps)

def _scatter_add(tgt, idx, src, axis=0):
    return tgt.clone().index_add_(axis, idx, src)

def _segment_sum(src, seg, n):
    out = torch.zeros((n,) + src.shape[1:], dtype=src.dtype)
    return out.index_add_(0, seg, src)

def _pad(x, pw):
    flat = tuple((v for pair in reversed(pw) for v in pair))
    return torch.nn.functional.pad(x, flat)
kernel_name = 'sequential_preLN__sliding_block__mixer_gmlp__prompt'

def setup_inputs():

    _w = lambda a: (lambda t: t.to(torch.bfloat16) if t.is_floating_point() else t)(torch.as_tensor(a))

    def _A_setup():
        B, S, D = (1, 2048, 512)
        return {'x': _w((lambda *s: torch.randn(*s, dtype=torch.bfloat16) * 0.05)(B, S, D)), 'wqkv': _w((lambda *s: torch.randn(*s, dtype=torch.bfloat16) * 0.05)(D, 3 * D)), 'wo': _w((lambda *s: torch.randn(*s, dtype=torch.bfloat16) * 0.05)(D, D))}
    _A_in = _A_setup()

    def _B_setup():
        B, S, D, F = (1, 2048, 512, 1792)
        Fh = F // 2
        return {'x': _w((lambda *s: torch.randn(*s, dtype=torch.bfloat16) * 0.05)(B, S, D)), 'w_in': _w((lambda *s: torch.randn(*s, dtype=torch.bfloat16) * 0.4)(D, 2 * Fh)), 'w_sp': _w(0.01 * (lambda *s: torch.randn(*s, dtype=torch.bfloat16) * 0.4)(S, S)), 'b_sp': _w(1.0 + 0.01 * (lambda *s: torch.randn(*s, dtype=torch.bfloat16) * 0.05)(S)), 'w_out': _w((lambda *s: torch.randn(*s, dtype=torch.bfloat16) * 0.05)(Fh, D))}
    _B_in = _B_setup()
    return {'A_x': _A_in['x'], 'A_wqkv': _A_in['wqkv'], 'A_wo': _A_in['wo'], 'B_x': _B_in['x'], 'B_w_in': _B_in['w_in'], 'B_w_sp': _B_in['w_sp'], 'B_b_sp': _B_in['b_sp'], 'B_w_out': _B_in['w_out']}

def reference(A_x, A_wqkv, A_wo, B_x, B_w_in, B_w_sp, B_b_sp, B_w_out):

    def _A_ref(x, wqkv, wo):
        B, S, D = x.shape
        H = 32
        Dh = D // H
        W = 256
        qkv = (lambda x, *d: x.permute(*d))((x @ wqkv).reshape(B, S, 3, H, Dh), 2, 0, 3, 1, 4)
        q, k, v = (qkv[0], qkv[1], qkv[2])
        scores = torch.einsum('bhqd,bhkd->bhqk', q, k) * Dh ** (-0.5)
        idx = torch.arange(S)
        mask = (idx[:, None] - idx[None, :] >= 0) & (idx[:, None] - idx[None, :] < W)
        scores = torch.where(mask[None, None], scores, -1000000000.0)
        p = _softmax(scores, axis=-1)
        o = torch.einsum('bhqk,bhkd->bhqd', p, v)
        return (lambda x, *d: x.permute(*d))(o, 0, 2, 1, 3).reshape(B, S, D) @ wo

    def _B_ref(x, w_in, w_sp, b_sp, w_out):
        B, S, D = x.shape
        F = w_in.shape[-1]
        Fh = F // 2
        z = _gelu(torch.einsum('bsd,df->bsf', x, w_in))
        z1 = z[..., :Fh].clone()
        z2 = _layernorm(z[..., Fh:], axis=-1)
        g = torch.einsum('ts,bsf->btf', w_sp, z2) + b_sp[None, :, None]
        return torch.einsum('bsf,fd->bsd', z1 * g, w_out)
    x = A_x
    h = x + _A_ref(_layernorm(x, axis=-1), A_wqkv, A_wo)
    h2 = h + _B_ref(_layernorm(h, axis=-1), B_w_in, B_w_sp, B_b_sp, B_w_out)
    return h2
```

```cpp
#include <hip/hip_runtime.h>
#include <hip/hip_bf16.h>
#include <stdint.h>

typedef __bf16 bf16_t;
typedef __attribute__((ext_vector_type(8))) __bf16 bf16x8;
typedef __attribute__((ext_vector_type(4))) __bf16 bf16x4;
typedef __attribute__((ext_vector_type(2))) __bf16 bf16x2;
typedef __attribute__((ext_vector_type(4))) short s16x4;
typedef __attribute__((ext_vector_type(8))) short s16x8;
typedef __attribute__((ext_vector_type(4))) float f32x4;
typedef __attribute__((ext_vector_type(2))) float f32x2;
typedef __attribute__((ext_vector_type(16))) float f32x16;
typedef __attribute__((ext_vector_type(4))) unsigned u32x4;
typedef __attribute__((ext_vector_type(2))) unsigned u32x2;
typedef unsigned long long u64;

#define DI __device__ __forceinline__

constexpr int S_LEN = 2048;
constexpr int D_MODEL = 512;
constexpr int FH = 896;

DI float rbf(float x) { return (float)(__bf16)x; }
DI float bf2f(unsigned short u) { return __uint_as_float(((unsigned)u) << 16); }
DI unsigned pack2(float a, float b) {
  bf16x2 v = {(__bf16)a, (__bf16)b};
  return __builtin_bit_cast(unsigned, v);
}
DI float lo_f(unsigned u) { return __uint_as_float(u << 16); }
DI float hi_f(unsigned u) { return __uint_as_float(u & 0xffff0000u); }

DI void wait_vm_dyn(int n) {
#define WV(N) case N: asm volatile("s_waitcnt vmcnt(" #N ")" ::: "memory"); break;
  switch (n) {
    WV(0) WV(1) WV(2) WV(3) WV(4) WV(5) WV(6) WV(7) WV(8) WV(9)
    WV(10) WV(11) WV(12) WV(13) WV(14) WV(15) WV(16) WV(17) WV(18) WV(19)
    WV(20) WV(21) WV(22) WV(23) WV(24) WV(25) WV(26) WV(27) WV(28) WV(29)
    WV(30) WV(31) WV(32) WV(33) WV(34) WV(35) WV(36) WV(37) WV(38) WV(39)
    WV(40) WV(41) WV(42) WV(43) WV(44) WV(45) WV(46) WV(47) WV(48)
    default: asm volatile("s_waitcnt vmcnt(0)" ::: "memory"); break;
  }
#undef WV
}

DI void glds16(const void* g, char* l) {
  __builtin_amdgcn_global_load_lds((const __attribute__((address_space(1))) void*)g,
                                   (__attribute__((address_space(3))) void*)l, 16, 0, 0);
}
DI s16x4 tr_read(const char* l) {
  return __builtin_amdgcn_ds_read_tr16_b64_v4i16((__attribute__((address_space(3))) s16x4*)l);
}

template <int BM, int BN, int WM, int WN, int KT, int NST>
struct GemmCfg {
  static constexpr int NW = WM * WN;
  static constexpr int PA = BM / 8, PB = BN / 8, P = PA + PB;
  static constexpr int PPW = (P + NW - 1) / NW;
  static constexpr int STAGE = (BM + BN) * 128;
  static constexpr int MF = BM / WM / 16;
  static constexpr int NFRAG = BN / 16;
  static constexpr int NFW = (NFRAG + WN - 1) / WN;
  static constexpr int LDS = STAGE * NST;
};

struct NoHook {
  DI void operator()(const char*, int) const {}
};

template <int BM, int BN, int WM, int WN, int KT, int NST, bool CS, class Hook>
DI void gemm_main(char* smem, const bf16_t* __restrict__ A, int lda, const bf16_t* __restrict__ B, int ldb,
                  f32x4 (&acc)[GemmCfg<BM, BN, WM, WN, KT, NST>::MF][GemmCfg<BM, BN, WM, WN, KT, NST>::NFW],
                  f32x4 (&cs)[GemmCfg<BM, BN, WM, WN, KT, NST>::NFW], Hook& hook) {
  using C = GemmCfg<BM, BN, WM, WN, KT, NST>;
  constexpr int NW = C::NW, PA = C::PA, P = C::P, PPW = C::PPW, STAGE = C::STAGE, MF = C::MF, NFW = C::NFW;
  const int lane = threadIdx.x & 63;
  const int wave = __builtin_amdgcn_readfirstlane(threadIdx.x >> 6);
  const int wr = wave % WM, wc = wave / WM;
  const int nfc = (wc == WN - 1) ? (C::NFRAG - (WN - 1) * NFW) : NFW;
  const bool lastvalid = (wave + (PPW - 1) * NW) < P;
  const int pw = lastvalid ? PPW : PPW - 1;

  const char* src[PPW];
  int step[PPW];
#pragma unroll
  for (int i = 0; i < PPW; ++i) {
    int p = wave + i * NW;
    if (p >= P) p = 0;
    if (p < PA) {
      int row = 8 * p + (lane >> 3);
      int c = (lane & 7) ^ (lane >> 3);
      src[i] = (const char*)(A + (size_t)row * lda + c * 8);
      step[i] = 128;
    } else {
      int idx = (p - PA) * 64 + lane;
      int kb = idx / BN, rem = idx % BN;
      int c = rem >> 3, r = rem & 7;
      int k = kb * 8 + (r ^ ((kb & 1) << 2));
      src[i] = (const char*)(B + (size_t)k * ldb + c * 8);
      step[i] = 128 * ldb;
    }
  }
  auto issue = [&](int slot) {
    char* base = smem + slot * STAGE + wave * 1024;
#pragma unroll
    for (int i = 0; i < PPW; ++i) {
      if (i < PPW - 1 || lastvalid) glds16(src[i], base + i * NW * 1024);
      src[i] += step[i];
    }
  };

  const int g = lane >> 4, q = (lane & 15) >> 2, pp = lane & 3;
  int aoff[2], boff[2];
#pragma unroll
  for (int ks = 0; ks < 2; ++ks)
    aoff[ks] = (wr * (MF * 16) + (lane & 15)) * 128 + (((4 * ks + g) ^ (lane & 7)) << 4);
#pragma unroll
  for (int t = 0; t < 2; ++t)
    boff[t] = BM * 128 + (((g * (BN / 8) + (pp >> 1) + 2 * wc * NFW) * 8 + ((4 * t + q) ^ ((g & 1) << 2))) << 4) +
              8 * (pp & 1);

  bf16x8 ones;
#pragma unroll
  for (int j = 0; j < 8; ++j) ones[j] = (__bf16)1.0f;

  constexpr int PRE = (NST - 1 < KT) ? NST - 1 : KT;
#pragma unroll
  for (int s = 0; s < PRE; ++s) issue(s);

  int slot = 0, fill = PRE % NST;
  for (int kt = 0; kt < KT; ++kt) {
    int rem = KT - 1 - kt;
    if (rem > NST - 2) rem = NST - 2;
    wait_vm_dyn(rem * pw);
    asm volatile("s_waitcnt lgkmcnt(0)" ::: "memory");
    __builtin_amdgcn_s_barrier();
    if (kt + NST - 1 < KT) {
      issue(fill);
      fill = (fill + 1 == NST) ? 0 : fill + 1;
    }
    const char* sb = smem + slot * STAGE;
#pragma unroll
    for (int ks = 0; ks < 2; ++ks) {
      bf16x8 af[MF], bfr[NFW];
#pragma unroll
      for (int mf = 0; mf < MF; ++mf) af[mf] = *(const bf16x8*)(sb + aoff[ks] + mf * 2048);
#pragma unroll
      for (int nf = 0; nf < NFW; ++nf) {
        if (nf < NFW - 1 || nf < nfc) {
          s16x4 lo = tr_read(sb + boff[0] + ks * (4 * (BN / 8) * 128) + nf * 256);
          s16x4 hi = tr_read(sb + boff[1] + ks * (4 * (BN / 8) * 128) + nf * 256);
          s16x8 v = __builtin_shufflevector(lo, hi, 0, 1, 2, 3, 4, 5, 6, 7);
          bfr[nf] = __builtin_bit_cast(bf16x8, v);
        }
      }
#pragma unroll
      for (int nf = 0; nf < NFW; ++nf) {
        if (nf < NFW - 1 || nf < nfc) {
#pragma unroll
          for (int mf = 0; mf < MF; ++mf)
            acc[mf][nf] = __builtin_amdgcn_mfma_f32_16x16x32_bf16(bfr[nf], af[mf], acc[mf][nf], 0, 0, 0);
          if constexpr (CS) {
            if ((WM == 1) || (wr & 1) == ks)
              if (WM <= 2 || (wr >> 1) == (kt & 1))
                cs[nf] = __builtin_amdgcn_mfma_f32_16x16x32_bf16(bfr[nf], ones, cs[nf], 0, 0, 0);
          }
        }
      }
    }
    hook(sb, kt);
    slot = (slot + 1 == NST) ? 0 : slot + 1;
  }
}

DI void chan_eq(float& mean, float& m2, float mean_b, float m2_b, float n) {
  float d = mean_b - mean;
  m2 = m2 + m2_b + d * d * (0.5f * n);
  mean = 0.5f * (mean + mean_b);
}

DI void ln_consts(float mean, float var, float& mean_b, float& rinv) {
  mean_b = rbf(mean);
  float v = rbf(var);
  float t = rbf(v + 1e-5f);
  float sd = rbf(sqrtf(t));
  rinv = 1.0f / sd;
}

struct StatHook {
  float piv, s1, s2;
  int row, half;
  DI void operator()(const char* sb, int kt) {
    if (kt == 0) piv = bf2f(*(const unsigned short*)(sb + row * 128 + ((row & 7) << 4)));
#pragma unroll
    for (int j = 0; j < 4; ++j) {
      int c = half * 4 + j;
      u32x4 v = *(const u32x4*)(sb + row * 128 + ((c ^ (row & 7)) << 4));
#pragma unroll
      for (int e = 0; e < 4; ++e) {
        float a = lo_f(v[e]) - piv, b = hi_f(v[e]) - piv;
        s1 += a + b;
        s2 = fmaf(a, a, s2);
        s2 = fmaf(b, b, s2);
      }
    }
  }
};

__global__ __launch_bounds__(256, 1) void k1_qkv(const bf16_t* __restrict__ x, const bf16_t* __restrict__ w,
                                                 bf16_t* __restrict__ qk, bf16_t* __restrict__ vT,
                                                 unsigned* __restrict__ exz) {
  constexpr int BM = 128, BN = 96, WM = 2, WN = 2, KT = 8, NST = 5;
  using C = GemmCfg<BM, BN, WM, WN, KT, NST>;
  __shared__ __attribute__((aligned(16))) char smem[C::LDS + 2048];
  exz[blockIdx.x * 256 + threadIdx.x] = 0u;

  const int b = blockIdx.x, xcd = b & 7, idx = b >> 3;
  const int tm = (xcd >> 1) * 4 + (idx >> 3), tn = (xcd & 1) * 8 + (idx & 7);
  const int m0 = tm * BM, n0 = tn * BN;
  f32x4 acc[C::MF][C::NFW], cs[C::NFW];
#pragma unroll
  for (int i = 0; i < C::MF; ++i)
#pragma unroll
    for (int j = 0; j < C::NFW; ++j) acc[i][j] = f32x4{0.f, 0.f, 0.f, 0.f};
#pragma unroll
  for (int j = 0; j < C::NFW; ++j) cs[j] = f32x4{0.f, 0.f, 0.f, 0.f};
  StatHook hook;
  hook.s1 = 0.f; hook.s2 = 0.f; hook.piv = 0.f;
  hook.row = threadIdx.x >> 1; hook.half = threadIdx.x & 1;
  gemm_main<BM, BN, WM, WN, KT, NST, true>(smem, x + (size_t)m0 * D_MODEL, D_MODEL, w + n0, 3 * D_MODEL, acc, cs, hook);

  float* tmean = (float*)(smem + C::LDS);
  float* trinv = tmean + 128;
  float* tcs = trinv + 128;
  const int lane = threadIdx.x & 63, wave = threadIdx.x >> 6, wr = wave % WM, wc = wave / WM;
  {
    float s1 = hook.s1 + __shfl_xor(hook.s1, 1);
    float s2 = hook.s2 + __shfl_xor(hook.s2, 1);
    float d = s1 * (1.0f / 512.0f);
    float var = fmaxf(s2 * (1.0f / 512.0f) - d * d, 0.f);
    float mb, ri;
    ln_consts(hook.piv + d, var, mb, ri);
    if (hook.half == 0) { tmean[hook.row] = mb; trinv[hook.row] = ri; }
    if ((lane & 15) == 0) {
#pragma unroll
      for (int nf = 0; nf < C::NFW; ++nf)
#pragma unroll
        for (int r = 0; r < 4; ++r) tcs[wr * 96 + wc * 48 + nf * 16 + 4 * (lane >> 4) + r] = cs[nf][r];
    }
  }
  __syncthreads();
#pragma unroll
  for (int nf = 0; nf < C::NFW; ++nf) {
    const int nl = wc * 48 + nf * 16 + 4 * (lane >> 4);
    float c0 = tcs[nl] + tcs[96 + nl], c1 = tcs[nl + 1] + tcs[96 + nl + 1];
    float c2 = tcs[nl + 2] + tcs[96 + nl + 2], c3 = tcs[nl + 3] + tcs[96 + nl + 3];
    const int n = n0 + nl;
#pragma unroll
    for (int mf = 0; mf < C::MF; ++mf) {
      const int ml = wr * 64 + mf * 16 + (lane & 15);
      const float mb = tmean[ml], ri = trinv[ml];
      float v0 = (acc[mf][nf][0] - mb * c0) * ri, v1 = (acc[mf][nf][1] - mb * c1) * ri;
      float v2 = (acc[mf][nf][2] - mb * c2) * ri, v3 = (acc[mf][nf][3] - mb * c3) * ri;
      const int m = m0 + ml;
      if (n < 1024) {
        u32x2 o = {pack2(v0, v1), pack2(v2, v3)};
        *(u32x2*)(qk + (size_t)m * 1024 + n) = o;
      } else {
        bf16_t* p = vT + (size_t)(n - 1024) * S_LEN + m;
        p[0] = (__bf16)v0; p[S_LEN] = (__bf16)v1; p[2 * S_LEN] = (__bf16)v2; p[3 * S_LEN] = (__bf16)v3;
      }
    }
  }
}

__global__ __launch_bounds__(512) void k2_attn(const bf16_t* __restrict__ qk, const bf16_t* __restrict__ vT,
                                               bf16_t* __restrict__ o) {
  const int b = blockIdx.x, xcd = b & 7, idx = b >> 3;
  const int qt = xcd * 8 + (idx >> 2), hg = idx & 3;
  const int lane = threadIdx.x & 63, wave = threadIdx.x >> 6;
  const int h = hg * 8 + wave;
  const int r = lane & 31, hf = lane >> 5;
  const int q0 = qt * 32;
  const int t0 = (q0 >= 256) ? 0 : (256 - q0) / 32;

  const bf16x8 qf = *(const bf16x8*)(qk + (size_t)(q0 + r) * 1024 + h * 16 + 8 * hf);
  const int pr = 16 * ((r >> 2) & 1) + 4 * (r >> 3) + (r & 3);
  bf16x8 kf[9];
#pragma unroll
  for (int t = 0; t < 9; ++t) {
    int key = q0 - 256 + 32 * t + pr;
    if (key < 0) key = 0;
    kf[t] = *(const bf16x8*)(qk + (size_t)key * 1024 + 512 + h * 16 + 8 * hf);
  }
  f32x16 s[9];
  f32x16 zero;
#pragma unroll
  for (int i = 0; i < 16; ++i) zero[i] = 0.f;
#pragma unroll
  for (int t = 0; t < 9; ++t) s[t] = __builtin_amdgcn_mfma_f32_32x32x16_bf16(kf[t], qf, zero, 0, 0, 0);

  const int vr = (r < 16) ? r : 0;
  bf16x8 vf[9][2];
#pragma unroll
  for (int t = 0; t < 9; ++t) {
    int kb = q0 - 256 + 32 * t;
    if (kb < 0) kb = 0;
    const bf16_t* p = vT + (size_t)(h * 16 + vr) * S_LEN + kb + 16 * hf;
    vf[t][0] = *(const bf16x8*)(p);
    vf[t][1] = *(const bf16x8*)(p + 8);
  }
  const float NEG = -3.0e38f;
#pragma unroll
  for (int i = 0; i < 16; ++i) {
    if (!(16 * hf + i > r)) s[0][i] = NEG;
    if (!(16 * hf + i <= r)) s[8][i] = NEG;
  }
#pragma unroll
  for (int t = 0; t < 8; ++t) {
    if (t < t0) {
#pragma unroll
      for (int i = 0; i < 16; ++i) s[t][i] = NEG;
    }
  }
  float mx = NEG;
#pragma unroll
  for (int t = 0; t < 9; ++t)
#pragma unroll
    for (int i = 0; i < 16; ++i) mx = fmaxf(mx, s[t][i]);
  mx = fmaxf(mx, __shfl_xor(mx, 32));
  const float c = 0.25f * 1.4426950408889634f;
  const float mc = mx * c;
  bf16x8 special;
#pragma unroll
  for (int j = 0; j < 8; ++j) special[j] = (__bf16)((r == 16) ? 1.0f : 0.0f);
  f32x16 oacc = zero;
#pragma unroll
  for (int t = 0; t < 9; ++t) {
#pragma unroll
    for (int st = 0; st < 2; ++st) {
      bf16x8 pf;
#pragma unroll
      for (int j = 0; j < 8; ++j) pf[j] = (__bf16)__builtin_amdgcn_exp2f(fmaf(s[t][8 * st + j], c, -mc));
      bf16x8 a = (r < 16) ? vf[t][st] : special;
      oacc = __builtin_amdgcn_mfma_f32_32x32x16_bf16(a, pf, oacc, 0, 0, 0);
    }
  }
  const float l = __shfl(oacc[8], r);
  const float il = 1.0f / l;
  bf16_t* op = o + (size_t)(q0 + r) * D_MODEL + h * 16 + 4 * hf;
  u32x2 o0 = {pack2(oacc[0] * il, oacc[1] * il), pack2(oacc[2] * il, oacc[3] * il)};
  u32x2 o1 = {pack2(oacc[4] * il, oacc[5] * il), pack2(oacc[6] * il, oacc[7] * il)};
  *(u32x2*)(op) = o0;
  *(u32x2*)(op + 8) = o1;
}

template <int KT, int NST, bool STATS>
__global__ __launch_bounds__(256, 1) void k_proj(const bf16_t* __restrict__ a, const bf16_t* __restrict__ w,
                                                 const bf16_t* __restrict__ res, bf16_t* __restrict__ out,
                                                 f32x2* __restrict__ stats) {
  constexpr int BM = 64, BN = 64, WM = 2, WN = 2;
  using C = GemmCfg<BM, BN, WM, WN, KT, NST>;
  __shared__ __attribute__((aligned(16))) char smem[C::LDS + 1024];
  const int b = blockIdx.x, xcd = b & 7, idx = b >> 3;
  const int tm = xcd * 4 + (idx >> 3), tn = idx & 7;
  const int m0 = tm * BM, n0 = tn * BN;
  const int lane = threadIdx.x & 63, wave = threadIdx.x >> 6, wr = wave % WM, wc = wave / WM;
  f32x4 acc[C::MF][C::NFW], cs[C::NFW];
#pragma unroll
  for (int i = 0; i < C::MF; ++i)
#pragma unroll
    for (int j = 0; j < C::NFW; ++j) acc[i][j] = f32x4{0.f, 0.f, 0.f, 0.f};
  NoHook hook;
  gemm_main<BM, BN, WM, WN, KT, NST, false>(smem, a + (size_t)m0 * (KT * 64), KT * 64, w + n0, D_MODEL, acc, cs, hook);

  f32x2* tst = (f32x2*)(smem + C::LDS);
#pragma unroll
  for (int mf = 0; mf < 2; ++mf) {
    const int ml = wr * 32 + mf * 16 + (lane & 15);
    const int m = m0 + ml;
    float hv[8];
#pragma unroll
    for (int nf = 0; nf < 2; ++nf) {
      const int n = n0 + wc * 32 + nf * 16 + 4 * (lane >> 4);
      u32x2 rv = *(const u32x2*)(res + (size_t)m * D_MODEL + n);
      hv[nf * 4 + 0] = rbf(lo_f(rv[0]) + rbf(acc[mf][nf][0]));
      hv[nf * 4 + 1] = rbf(hi_f(rv[0]) + rbf(acc[mf][nf][1]));
      hv[nf * 4 + 2] = rbf(lo_f(rv[1]) + rbf(acc[mf][nf][2]));
      hv[nf * 4 + 3] = rbf(hi_f(rv[1]) + rbf(acc[mf][nf][3]));
      u32x2 ov = {pack2(hv[nf * 4], hv[nf * 4 + 1]), pack2(hv[nf * 4 + 2], hv[nf * 4 + 3])};
      *(u32x2*)(out + (size_t)m * D_MODEL + n) = ov;
    }
    if constexpr (STATS) {
      float mean = 0.f;
#pragma unroll
      for (int j = 0; j < 8; ++j) mean += hv[j];
      mean *= 0.125f;
      float m2 = 0.f;
#pragma unroll
      for (int j = 0; j < 8; ++j) { float d = hv[j] - mean; m2 = fmaf(d, d, m2); }
      chan_eq(mean, m2, __shfl_xor(mean, 16), __shfl_xor(m2, 16), 8.f);
      chan_eq(mean, m2, __shfl_xor(mean, 32), __shfl_xor(m2, 32), 16.f);
      if (wc == 1 && lane < 16) tst[ml] = f32x2{mean, m2};
      acc[mf][0][0] = mean; acc[mf][0][1] = m2;
    }
  }
  if constexpr (STATS) {
    __syncthreads();
    if (wc == 0 && lane < 16) {
#pragma unroll
      for (int mf = 0; mf < 2; ++mf) {
        const int ml = wr * 32 + mf * 16 + lane;
        float mean = acc[mf][0][0], m2 = acc[mf][0][1];
        f32x2 o = tst[ml];
        chan_eq(mean, m2, o[0], o[1], 32.f);
        stats[(size_t)(m0 + ml) * 8 + tn] = f32x2{mean, m2};
      }
    }
  }
}

DI float gelu_f(float x) {
  const float k0 = 0.7978845608028654f * 2.0f * 1.4426950408889634f;
  float y = k0 * fmaf(0.044715f * x * x, x, x);
  float e = __builtin_amdgcn_exp2f(-y);
  return x * __builtin_amdgcn_rcpf(1.0f + e);
}

constexpr unsigned EX_TAG = 0x5a17c3e1u;

__global__ __launch_bounds__(256, 1) void k4_in(const bf16_t* __restrict__ h, const bf16_t* __restrict__ w,
                                                const f32x2* __restrict__ hstats, bf16_t* __restrict__ z1,
                                                bf16_t* __restrict__ z2n, u64* __restrict__ ex) {
  constexpr int BM = 128, BN = 112, WM = 4, WN = 1, KT = 8, NST = 5;
  using C = GemmCfg<BM, BN, WM, WN, KT, NST>;
  __shared__ __attribute__((aligned(16))) char smem[C::LDS + 4096];
  const int b = blockIdx.x, xcd = b & 7, idx = b >> 3;
  const int tm = (xcd >> 1) * 4 + (idx >> 3), tn = (xcd & 1) * 8 + (idx & 7);
  const int m0 = tm * BM, n0 = tn * BN;
  const int lane = threadIdx.x & 63, wave = threadIdx.x >> 6, wr = wave;
  float* tmean = (float*)(smem + C::LDS);
  float* trinv = tmean + 128;
  float* tcs = trinv + 128;
  float* tmean2 = tcs + 448;
  float* trinv2 = tmean2 + 128;
  if (threadIdx.x < 128) {
    const f32x2* p = hstats + (size_t)(m0 + threadIdx.x) * 8;
    f32x2 v[8];
#pragma unroll
    for (int j = 0; j < 8; ++j) v[j] = p[j];
    float mean = 0.f;
#pragma unroll
    for (int j = 0; j < 8; ++j) mean += v[j][0];
    mean *= 0.125f;
    float m2 = 0.f;
#pragma unroll
    for (int j = 0; j < 8; ++j) { float d = v[j][0] - mean; m2 += v[j][1] + 64.f * d * d; }
    float mb, ri;
    ln_consts(mean, m2 * (1.0f / 512.0f), mb, ri);
    tmean[threadIdx.x] = mb;
    trinv[threadIdx.x] = ri;
  }
  f32x4 acc[C::MF][C::NFW], cs[C::NFW];
#pragma unroll
  for (int i = 0; i < C::MF; ++i)
#pragma unroll
    for (int j = 0; j < C::NFW; ++j) acc[i][j] = f32x4{0.f, 0.f, 0.f, 0.f};
#pragma unroll
  for (int j = 0; j < C::NFW; ++j) cs[j] = f32x4{0.f, 0.f, 0.f, 0.f};
  NoHook hook;
  gemm_main<BM, BN, WM, WN, KT, NST, true>(smem, h + (size_t)m0 * D_MODEL, D_MODEL, w + n0, 2 * FH, acc, cs, hook);

  if ((lane & 15) == 0) {
#pragma unroll
    for (int nf = 0; nf < C::NFW; ++nf)
#pragma unroll
      for (int r = 0; r < 4; ++r) tcs[wr * 112 + nf * 16 + 4 * (lane >> 4) + r] = cs[nf][r];
  }
  __syncthreads();
#pragma unroll
  for (int nf = 0; nf < C::NFW; ++nf) {
    const int nl = nf * 16 + 4 * (lane >> 4);
    float c[4];
#pragma unroll
    for (int r = 0; r < 4; ++r) c[r] = (tcs[nl + r] + tcs[112 + nl + r]) + (tcs[224 + nl + r] + tcs[336 + nl + r]);
#pragma unroll
    for (int mf = 0; mf < C::MF; ++mf) {
      const int ml = wr * 32 + mf * 16 + (lane & 15);
      const float mb = tmean[ml], ri = trinv[ml];
#pragma unroll
      for (int r = 0; r < 4; ++r) acc[mf][nf][r] = rbf(gelu_f(rbf((acc[mf][nf][r] - mb * c[r]) * ri)));
    }
  }
  if (tn < 8) {
#pragma unroll
    for (int mf = 0; mf < C::MF; ++mf) {
      const int m = m0 + wr * 32 + mf * 16 + (lane & 15);
#pragma unroll
      for (int nf = 0; nf < C::NFW; ++nf) {
        const int n = n0 + nf * 16 + 4 * (lane >> 4);
        u32x2 ov = {pack2(acc[mf][nf][0], acc[mf][nf][1]), pack2(acc[mf][nf][2], acc[mf][nf][3])};
        *(u32x2*)(z1 + (size_t)m * FH + n) = ov;
      }
    }
    return;
  }
  const int j0 = tn - 8;
  u64* exp_ = ex + (size_t)tm * (8 * 256);
#pragma unroll
  for (int mf = 0; mf < C::MF; ++mf) {
    float mean = 0.f;
#pragma unroll
    for (int nf = 0; nf < C::NFW; ++nf)
#pragma unroll
      for (int r = 0; r < 4; ++r) mean += acc[mf][nf][r];
    mean *= (1.0f / 28.0f);
    float m2 = 0.f;
#pragma unroll
    for (int nf = 0; nf < C::NFW; ++nf)
#pragma unroll
      for (int r = 0; r < 4; ++r) { float d = acc[mf][nf][r] - mean; m2 = fmaf(d, d, m2); }
    chan_eq(mean, m2, __shfl_xor(mean, 16), __shfl_xor(m2, 16), 28.f);
    chan_eq(mean, m2, __shfl_xor(mean, 32), __shfl_xor(m2, 32), 56.f);
    const int ml = wr * 32 + mf * 16 + (lane & 15);
    if (lane < 32) {
      float v = (lane < 16) ? mean : m2;
      __hip_atomic_store(exp_ + j0 * 256 + ml * 2 + (lane >> 4), ((u64)EX_TAG << 32) | (u64)__float_as_uint(v),
                         __ATOMIC_RELAXED, __HIP_MEMORY_SCOPE_AGENT);
    }
  }
  {
    const int t = threadIdx.x;
    float v[8];
    for (unsigned spins = 0;; ++spins) {
      bool ok = true;
#pragma unroll
      for (int j = 0; j < 8; ++j) {
        u64 gg = __hip_atomic_load(exp_ + j * 256 + t, __ATOMIC_RELAXED, __HIP_MEMORY_SCOPE_AGENT);
        v[j] = __uint_as_float((unsigned)gg);
        ok &= ((unsigned)(gg >> 32) == EX_TAG);
      }
      if (__all(ok)) break;
      if (spins > 4000000u) break;
      __builtin_amdgcn_s_sleep(1);
    }
    float sum = 0.f;
#pragma unroll
    for (int j = 0; j < 8; ++j) sum += v[j];
    float other = __shfl_xor(sum, 1);
    float mean = ((t & 1) ? other : sum) * 0.125f;
    float dd = 0.f;
#pragma unroll
    for (int j = 0; j < 8; ++j) { float d = v[j] - mean; dd = fmaf(d, d, dd); }
    float m2tot = other + 112.f * dd;
    if ((t & 1) == 0) {
      float mb, ri;
      ln_consts(mean, m2tot * (1.0f / 896.0f), mb, ri);
      tmean2[t >> 1] = mb;
      trinv2[t >> 1] = ri;
    }
  }
  __syncthreads();
#pragma unroll
  for (int mf = 0; mf < C::MF; ++mf) {
    const int ml = wr * 32 + mf * 16 + (lane & 15);
    const int m = m0 + ml;
    const float mb = tmean2[ml], ri = trinv2[ml];
#pragma unroll
    for (int nf = 0; nf < C::NFW; ++nf) {
      const int n = n0 - FH + nf * 16 + 4 * (lane >> 4);
      float v0 = rbf(acc[mf][nf][0] - mb) * ri, v1 = rbf(acc[mf][nf][1] - mb) * ri;
      float v2 = rbf(acc[mf][nf][2] - mb) * ri, v3 = rbf(acc[mf][nf][3] - mb) * ri;
      u32x2 ov = {pack2(v0, v1), pack2(v2, v3)};
      *(u32x2*)(z2n + (size_t)m * FH + n) = ov;
    }
  }
}

__global__ __launch_bounds__(256, 1) void k5_sp(const bf16_t* __restrict__ wsp, const bf16_t* __restrict__ z2n,
                                                const bf16_t* __restrict__ bsp, const bf16_t* __restrict__ z1,
                                                bf16_t* __restrict__ y) {
  constexpr int BM = 64, BN = 112, WM = 2, WN = 2, KT = 32, NST = 7;
  using C = GemmCfg<BM, BN, WM, WN, KT, NST>;
  __shared__ __attribute__((aligned(16))) char smem[C::LDS];
  const int b = blockIdx.x, xcd = b & 7, idx = b >> 3;
  const int tm = (xcd >> 1) * 8 + (idx >> 2), tn = (xcd & 1) * 4 + (idx & 3);
  const int m0 = tm * BM, n0 = tn * BN;
  const int lane = threadIdx.x & 63, wave = threadIdx.x >> 6, wr = wave % WM, wc = wave / WM;
  f32x4 acc[C::MF][C::NFW], cs[C::NFW];
#pragma unroll
  for (int i = 0; i < C::MF; ++i)
#pragma unroll
    for (int j = 0; j < C::NFW; ++j) acc[i][j] = f32x4{0.f, 0.f, 0.f, 0.f};
  NoHook hook;
  gemm_main<BM, BN, WM, WN, KT, NST, false>(smem, wsp + (size_t)m0 * S_LEN, S_LEN, z2n + n0, FH, acc, cs, hook);
  const int nfc = (wc == 1) ? 3 : 4;
#pragma unroll
  for (int mf = 0; mf < C::MF; ++mf) {
    const int m = m0 + wr * 32 + mf * 16 + (lane & 15);
    const float bb = (float)bsp[m];
#pragma unroll
    for (int nf = 0; nf < C::NFW; ++nf) {
      if (nf < nfc) {
        const int n = n0 + wc * 64 + nf * 16 + 4 * (lane >> 4);
        u32x2 zv = *(const u32x2*)(z1 + (size_t)m * FH + n);
        float v0 = lo_f(zv[0]) * rbf(rbf(acc[mf][nf][0]) + bb);
        float v1 = hi_f(zv[0]) * rbf(rbf(acc[mf][nf][1]) + bb);
        float v2 = lo_f(zv[1]) * rbf(rbf(acc[mf][nf][2]) + bb);
        float v3 = hi_f(zv[1]) * rbf(rbf(acc[mf][nf][3]) + bb);
        u32x2 ov = {pack2(v0, v1), pack2(v2, v3)};
        *(u32x2*)(y + (size_t)m * FH + n) = ov;
      }
    }
  }
}

extern "C" void kernel_launch(void* const* d_in, const int* in_sizes, int n_in, void* d_out, int out_size,
                              void* d_ws, size_t ws_size, hipStream_t stream) {
  const bf16_t* x = (const bf16_t*)d_in[0];
  const bf16_t* wqkv = (const bf16_t*)d_in[1];
  const bf16_t* wo = (const bf16_t*)d_in[2];
  const bf16_t* w_in = (const bf16_t*)d_in[4];
  const bf16_t* w_sp = (const bf16_t*)d_in[5];
  const bf16_t* b_sp = (const bf16_t*)d_in[6];
  const bf16_t* w_out = (const bf16_t*)d_in[7];
  bf16_t* out = (bf16_t*)d_out;

  char* ws = (char*)d_ws;
  size_t off = 0;
  auto take = [&](size_t bytes) { char* p = ws + off; off += (bytes + 255) & ~(size_t)255; return p; };
  bf16_t* qk = (bf16_t*)take((size_t)S_LEN * 1024 * 2);
  bf16_t* vT = (bf16_t*)take((size_t)512 * S_LEN * 2);
  bf16_t* o = (bf16_t*)take((size_t)S_LEN * D_MODEL * 2);
  bf16_t* h = (bf16_t*)take((size_t)S_LEN * D_MODEL * 2);
  f32x2* hstats = (f32x2*)take((size_t)S_LEN * 8 * 8);
  bf16_t* z1 = (bf16_t*)take((size_t)S_LEN * FH * 2);
  bf16_t* z2n = (bf16_t*)take((size_t)S_LEN * FH * 2);
  bf16_t* y = (bf16_t*)take((size_t)S_LEN * FH * 2);
  u64* ex = (u64*)take((size_t)16 * 8 * 256 * 8);

  k1_qkv<<<256, 256, 0, stream>>>(x, wqkv, qk, vT, (unsigned*)ex);
  k2_attn<<<256, 512, 0, stream>>>(qk, vT, o);
  k_proj<8, 8, true><<<256, 256, 0, stream>>>(o, wo, x, h, hstats);
  k4_in<<<256, 256, 0, stream>>>(h, w_in, hstats, z1, z2n, ex);
  k5_sp<<<256, 256, 0, stream>>>(w_sp, z2n, b_sp, z1, y);
  k_proj<14, 9, false><<<256, 256, 0, stream>>>(y, w_out, h, out, nullptr);
}
```

```cpp
#include <hip/hip_runtime.h>
#include <hip/hip_bf16.h>
#include <stdint.h>

typedef __bf16 bf16_t;
typedef __attribute__((ext_vector_type(8))) __bf16 bf16x8;
typedef __attribute__((ext_vector_type(2))) __bf16 bf16x2;
typedef __attribute__((ext_vector_type(4))) short s16x4;
typedef __attribute__((ext_vector_type(8))) short s16x8;
typedef __attribute__((ext_vector_type(4))) float f32x4;
typedef __attribute__((ext_vector_type(2))) float f32x2;
typedef __attribute__((ext_vector_type(16))) float f32x16;
typedef __attribute__((ext_vector_type(4))) unsigned u32x4;
typedef __attribute__((ext_vector_type(2))) unsigned u32x2;
typedef unsigned long long u64;

#define DI __device__ __forceinline__

constexpr int S_LEN = 2048;
constexpr int D_MODEL = 512;
constexpr int FH = 896;

DI float rbf(float x) { return (float)(__bf16)x; }
DI unsigned pack2(float a, float b) {
  bf16x2 v = {(__bf16)a, (__bf16)b};
  return __builtin_bit_cast(unsigned, v);
}
DI float lo_f(unsigned u) { return __uint_as_float(u << 16); }
DI float hi_f(unsigned u) { return __uint_as_float(u & 0xffff0000u); }

template <int N> DI void wait_vm() { asm volatile("s_waitcnt vmcnt(%0)" ::"n"(N) : "memory"); }

DI void glds16(const void* g, char* l) {
  __builtin_amdgcn_global_load_lds((const __attribute__((address_space(1))) void*)g,
                                   (__attribute__((address_space(3))) void*)l, 16, 0, 0);
}
DI s16x4 tr_read(const char* l) {
  return __builtin_amdgcn_ds_read_tr16_b64_v4i16((__attribute__((address_space(3))) s16x4*)l);
}

template <int V> struct IC { static constexpr int value = V; };
constexpr int cmin3(int a, int b, int c) { return a < b ? (a < c ? a : c) : (b < c ? b : c); }
template <int N, int I = 0, class F> DI void static_for(F& f) {
  if constexpr (I < N) {
    f(IC<I>{});
    static_for<N, I + 1>(f);
  }
}

DI int fragaddr(int m, int k, int K32) {
  return ((m >> 4) * K32 + (k >> 5)) * 512 + ((m & 15) + 16 * ((k & 31) >> 3)) * 8 + (k & 7);
}

DI int bimg(int k, int cc) {
  return k * 128 + ((((cc >> 2) & 1) ^ ((k >> 1) & 1)) << 6) + (((cc & 3) ^ (((k >> 3) & 1) << 1)) << 4);
}

template <int BM, int BN, int WM, int WK, int WN, int KT, int NST>
struct GemmCfg {
  static constexpr int NW = WM * WN * WK;
  static constexpr int NL = 4;
  static constexpr int PA = BM / 8, PB = BN / 8, P = PA + PB;
  static constexpr int PPL = (P + NL - 1) / NL;
  static constexpr int STAGE = (BM + BN) * 128;
  static constexpr int MF = BM / WM / 16;
  static constexpr int NFRAG = BN / 16;
  static constexpr int NFW = (NFRAG + WN - 1) / WN;
  static constexpr int LDS = STAGE * NST;
  static constexpr bool RESIDENT = NST >= KT;
  static constexpr int PRE = RESIDENT ? KT : NST - 1;
};

struct NoHook {
  DI void operator()(const char*) const {}
};

template <int BM, int BN, int WM, int WK, int WN, int KT, int NST, bool AFRAG, bool BFRAG, int PW, class Hook>
DI void gemm_loader(char* smem, const bf16_t* __restrict__ A, int lda, const bf16_t* __restrict__ B, int ldb,
                    int pstride, int lw, int rot, Hook& hook) {
  using C = GemmCfg<BM, BN, WM, WK, WN, KT, NST>;
  constexpr int NL = C::NL, PA = C::PA, STAGE = C::STAGE, PRE = C::PRE;
  static_assert(PA % NL == 0, "A pieces must be a multiple of the loader count");
  static_assert(BFRAG || (BN % 64) == 0, "row-major B needs 64-column panels");
  const int lane = threadIdx.x & 63;
  unsigned off[PW];
#pragma unroll
  for (int i = 0; i < PW; ++i) {
    if (i < PA / NL) {
      const int p = lw + i * NL;
      if constexpr (AFRAG) {
        off[i] = (unsigned)(((p >> 1) * lda + (p & 1)) * 1024 + lane * 16);
      } else {
        off[i] = (unsigned)((8 * p + (lane >> 3)) * lda * 2 + (((lane & 7) ^ (lane >> 3)) << 4));
      }
    } else {
      const int p = lw + i * NL - PA;
      if constexpr (BFRAG) {
        off[i] = (unsigned)(((p >> 1) * ldb + (p & 1)) * 1024 + lane * 16);
      } else {
        int k = (p & 7) * 8 + (lane >> 3);
        int cc = (lane & 7) ^ (((lane >> 4) & 1) << 2) ^ ((p & 1) << 1);
        off[i] = (unsigned)(k * ldb * 2 + (p >> 3) * pstride + cc * 16);
      }
    }
  }
  rot = 0;
  int kr = __builtin_amdgcn_readfirstlane(rot), kw = 0;
  auto issue = [&](int slot) {
    char* base = smem + slot * STAGE + lw * 1024;
    const int kk = kw + kr;
    const char* pa = (const char*)A + kk * (AFRAG ? 2048 : 128);
    const char* pb = (const char*)B + (size_t)kk * (BFRAG ? 2048 : 128 * ldb);
#pragma unroll
    for (int i = 0; i < PW; ++i) {
      unsigned o = off[i];
      asm volatile("" : "+v"(o));
      glds16((i < PA / NL ? pa : pb) + o, base + i * NL * 1024);
    }
    if constexpr (KT % 8 == 0) {
      kr = (kr + 1) & 7;
      if (kr == rot) kw += 8;
    } else {
      kr = (kr + 1 == KT) ? 0 : kr + 1;
    }
  };
  auto body = [&](auto KTC) {
    constexpr int kt = decltype(KTC)::value;
    constexpr int I0 = (kt == 0) ? 0 : cmin3(KT, kt - 1 + PRE, 2 * kt);
    constexpr int I1 = cmin3(KT, kt + PRE, 2 * kt + 2);
    if constexpr (I1 > I0) issue(I0 % NST);
    if constexpr (I1 > I0 + 1) issue((I0 + 1) % NST);
    wait_vm<(I1 - kt - 1) * PW>();
    __builtin_amdgcn_s_barrier();
    asm volatile("" ::: "memory");
    hook(smem + (kt % NST) * STAGE);
    __builtin_amdgcn_s_waitcnt(0xC07F);
  };
  static_for<KT>(body);
}

template <int BM, int BN, int WM, int WK, int WN, int KT, int NST, bool AFRAG, bool BFRAG, bool CS, bool FULLN,
          bool SPLITN, int PLAIN>
DI void gemm_compute(char* smem, int wave,
                     f32x4 (&acc)[GemmCfg<BM, BN, WM, WK, WN, KT, NST>::MF][GemmCfg<BM, BN, WM, WK, WN, KT, NST>::NFW],
                     f32x4 (&cs)[GemmCfg<BM, BN, WM, WK, WN, KT, NST>::NFW]) {
  using C = GemmCfg<BM, BN, WM, WK, WN, KT, NST>;
  constexpr int STAGE = C::STAGE, MF = C::MF, NFW = C::NFW;
  constexpr int NF = FULLN ? NFW : NFW - 1;
  const int lane = threadIdx.x & 63;
  const int wr = wave % WM, wk = (wave / WM) % WK, wc = wave / (WM * WK);
  const int g = lane >> 4, q = (lane & 15) >> 2, pp = lane & 3;
  constexpr int NKS = (WK == 2) ? 1 : 2;
  int aoff[NKS];
#pragma unroll
  for (int i = 0; i < NKS; ++i) {
    const int ks = (WK == 2) ? wk : i;
    if constexpr (AFRAG) aoff[i] = (wr * MF * 2 + ks) * 1024 + lane * 16;
    else aoff[i] = (wr * (MF * 16) + (lane & 15)) * 128 + (((4 * ks + g) ^ (lane & 7)) << 4);
  }
  int boff[NF];
#pragma unroll
  for (int nf = 0; nf < NF; ++nf) {
    const int f = SPLITN ? ((nf >> 1) * 4 + 2 * wc + (nf & 1)) : (wc * NFW + nf);
    if constexpr (BFRAG) {
      boff[nf] = BM * 128 + (f * 2 + ((WK == 2) ? wk : 0)) * 1024 + lane * 16;
    } else {
      const int kk = 8 * g + q + ((WK == 2) ? wk * 32 : 0);
      boff[nf] = BM * 128 + (f >> 2) * 8192 + bimg(kk, 2 * (f & 3) + (pp >> 1)) + 8 * (pp & 1);
    }
  }
  bf16x8 ones;
#pragma unroll
  for (int j = 0; j < 8; ++j) ones[j] = (__bf16)1.0f;

  bf16x8 af[2][MF], bfr[2][NF];
  auto step = [&](auto JC) {
    constexpr int j = decltype(JC)::value;
    constexpr int NSTEP = KT * NKS;
    if constexpr (j < NSTEP) {
      constexpr int kt = j / NKS, i = j % NKS;
      if constexpr (i == 0) {
        __builtin_amdgcn_s_barrier();
        asm volatile("" ::: "memory");
      }
      const char* sb = smem + (kt % NST) * STAGE;
#pragma unroll
      for (int mf = 0; mf < MF; ++mf) af[j & 1][mf] = *(const bf16x8*)(sb + aoff[i] + mf * 2048);
#pragma unroll
      for (int nf = 0; nf < NF; ++nf) {
        if constexpr (BFRAG) {
          bfr[j & 1][nf] = *(const bf16x8*)(sb + boff[nf] + i * 1024);
        } else {
          s16x4 lo = tr_read(sb + boff[nf] + i * 4096);
          s16x4 hi = tr_read(sb + boff[nf] + i * 4096 + 512);
          s16x8 v = __builtin_shufflevector(lo, hi, 0, 1, 2, 3, 4, 5, 6, 7);
          bfr[j & 1][nf] = __builtin_bit_cast(bf16x8, v);
        }
      }
    }
    if constexpr (j > 0) {
      constexpr int jp = j - 1;
#pragma unroll
      for (int nf = 0; nf < NF; ++nf) {
#pragma unroll
        for (int mf = 0; mf < MF; ++mf) {
          if ((PLAIN >> nf) & 1)
            acc[mf][nf] = __builtin_amdgcn_mfma_f32_16x16x32_bf16(af[jp & 1][mf], bfr[jp & 1][nf], acc[mf][nf], 0, 0, 0);
          else
            acc[mf][nf] = __builtin_amdgcn_mfma_f32_16x16x32_bf16(bfr[jp & 1][nf], af[jp & 1][mf], acc[mf][nf], 0, 0, 0);
        }
      }
      if constexpr (j < NSTEP) {
        constexpr int NRD = MF + (BFRAG ? NF : 2 * NF), NMM = MF * NF;
        constexpr int RPM = (NRD + NMM - 1) / NMM;
#pragma unroll
        for (int u = 0; u < NMM; ++u) {
          __builtin_amdgcn_sched_group_barrier(0x8, 1, 0);
          __builtin_amdgcn_sched_group_barrier(0x100, RPM, 0);
        }
      }
      __builtin_amdgcn_sched_barrier(0);
      if constexpr (CS) {
        if (wr == (jp % WM)) {
#pragma unroll
          for (int nf = 0; nf < NF; ++nf) {
            if ((PLAIN >> nf) & 1) cs[nf] = __builtin_amdgcn_mfma_f32_16x16x32_bf16(ones, bfr[jp & 1][nf], cs[nf], 0, 0, 0);
            else cs[nf] = __builtin_amdgcn_mfma_f32_16x16x32_bf16(bfr[jp & 1][nf], ones, cs[nf], 0, 0, 0);
          }
        }
      }
    }
    __builtin_amdgcn_sched_barrier(0);
    __builtin_amdgcn_s_waitcnt(0xC07F);
  };
  static_for<KT * NKS + 1>(step);
}

template <int BM, int BN, int WM, int WK, int WN, int KT, int NST, bool AFRAG, bool BFRAG, bool CS, bool SPLITN,
          int PLAIN, class Hook>
DI bool gemm_main(char* smem, const bf16_t* __restrict__ A, int lda, const bf16_t* __restrict__ B, int ldb, int pstride,
                  int rot,
                  f32x4 (&acc)[GemmCfg<BM, BN, WM, WK, WN, KT, NST>::MF][GemmCfg<BM, BN, WM, WK, WN, KT, NST>::NFW],
                  f32x4 (&cs)[GemmCfg<BM, BN, WM, WK, WN, KT, NST>::NFW], Hook& hook) {
  using C = GemmCfg<BM, BN, WM, WK, WN, KT, NST>;
  const int wave = __builtin_amdgcn_readfirstlane(threadIdx.x >> 6);
  if (wave >= C::NW) {
    const int lw = wave - C::NW;
    __builtin_amdgcn_s_setprio(3);
    constexpr bool EVEN = (C::P % C::NL) == 0;
    if (EVEN || lw < (C::P % C::NL))
      gemm_loader<BM, BN, WM, WK, WN, KT, NST, AFRAG, BFRAG, C::PPL>(smem, A, lda, B, ldb, pstride, lw, rot, hook);
    else if constexpr (!EVEN)
      gemm_loader<BM, BN, WM, WK, WN, KT, NST, AFRAG, BFRAG, C::PPL - 1>(smem, A, lda, B, ldb, pstride, lw, rot, hook);
    return false;
  }
  const int wc = wave / (WM * WK);
  constexpr bool ALLFULL = (C::NFRAG == WN * C::NFW);
  if (ALLFULL || wc != WN - 1)
    gemm_compute<BM, BN, WM, WK, WN, KT, NST, AFRAG, BFRAG, CS, true, SPLITN, PLAIN>(smem, wave, acc, cs);
  else if constexpr (!ALLFULL)
    gemm_compute<BM, BN, WM, WK, WN, KT, NST, AFRAG, BFRAG, CS, false, SPLITN, PLAIN>(smem, wave, acc, cs);
  return true;
}

template <int NFW>
DI void splitk_combine(char* smem, f32x4 (&acc)[2][NFW], f32x4 (&res)[NFW], int pairid, int wk, int lane, bool fulln) {
  __syncthreads();
  char* mine = smem + (pairid * 2 + wk) * (NFW * 1024) + lane * 16;
  char* theirs = smem + (pairid * 2 + (wk ^ 1)) * (NFW * 1024) + lane * 16;
#pragma unroll
  for (int nf = 0; nf < NFW; ++nf)
    if (nf < NFW - 1 || fulln) *(f32x4*)(mine + nf * 1024) = wk ? acc[0][nf] : acc[1][nf];
  __syncthreads();
#pragma unroll
  for (int nf = 0; nf < NFW; ++nf)
    if (nf < NFW - 1 || fulln) res[nf] = (wk ? acc[1][nf] : acc[0][nf]) + *(const f32x4*)(theirs + nf * 1024);
}

DI void chan_eq(float& mean, float& m2, float mean_b, float m2_b, float n) {
  float d = mean_b - mean;
  m2 = m2 + m2_b + d * d * (0.5f * n);
  mean = 0.5f * (mean + mean_b);
}

DI void ln_consts(float mean, float var, float& mean_b, float& rinv) {
  mean_b = rbf(mean);
  float v = rbf(var);
  float t = rbf(v + 1e-5f);
  float sd = rbf(sqrtf(t));
  rinv = 1.0f / sd;
}

DI float row16_sum(float v) {
  v += __builtin_bit_cast(float, __builtin_amdgcn_update_dpp(0, __builtin_bit_cast(int, v), 0x128, 0xf, 0xf, false));
  v += __builtin_bit_cast(float, __builtin_amdgcn_update_dpp(0, __builtin_bit_cast(int, v), 0x124, 0xf, 0xf, false));
  v += __builtin_bit_cast(float, __builtin_amdgcn_update_dpp(0, __builtin_bit_cast(int, v), 0x122, 0xf, 0xf, false));
  v += __builtin_bit_cast(float, __builtin_amdgcn_update_dpp(0, __builtin_bit_cast(int, v), 0x121, 0xf, 0xf, false));
  return v;
}

struct StatHook {
  float s1, s2;
  int o0;
  DI void operator()(const char* sb) {
    bf16x2 one2 = {(__bf16)1.0f, (__bf16)1.0f};
    u32x4 v0 = *(const u32x4*)(sb + o0);
    u32x4 v1 = *(const u32x4*)(sb + o0 + 16);
#pragma unroll
    for (int e = 0; e < 4; ++e) {
      const unsigned ua = v0[e], ub = v1[e];
      bf16x2 a = __builtin_bit_cast(bf16x2, ua), b = __builtin_bit_cast(bf16x2, ub);
      s1 = __builtin_amdgcn_fdot2_f32_bf16(a, one2, s1, false);
      s2 = __builtin_amdgcn_fdot2_f32_bf16(a, a, s2, false);
      s1 = __builtin_amdgcn_fdot2_f32_bf16(b, one2, s1, false);
      s2 = __builtin_amdgcn_fdot2_f32_bf16(b, b, s2, false);
    }
  }
};

__global__ __launch_bounds__(768, 3) void k1_qkv(const bf16_t* __restrict__ x, const bf16_t* __restrict__ w,
                                                 bf16_t* __restrict__ qkf, bf16_t* __restrict__ v2,
                                                 u32x4* __restrict__ exz) {
  constexpr int BM = 64, BN = 192, WM = 2, WK = 1, WN = 4, KT = 8, NST = 4;
  using C = GemmCfg<BM, BN, WM, WK, WN, KT, NST>;
  __shared__ __attribute__((aligned(16))) char smem[C::LDS + 2048];
  if (threadIdx.x < 256) exz[blockIdx.x * 256 + threadIdx.x] = u32x4{0u, 0u, 0u, 0u};

  const int b = blockIdx.x, xcd = b & 7, idx = b >> 3;
  const int tm = xcd * 4 + (idx >> 3), tn = idx & 7;
  const int m0 = tm * BM, n0 = tn * BN;
  f32x4 acc[C::MF][C::NFW], cs[C::NFW];
#pragma unroll
  for (int i = 0; i < C::MF; ++i)
#pragma unroll
    for (int j = 0; j < C::NFW; ++j) acc[i][j] = f32x4{0.f, 0.f, 0.f, 0.f};
#pragma unroll
  for (int j = 0; j < C::NFW; ++j) cs[j] = f32x4{0.f, 0.f, 0.f, 0.f};
  StatHook hook;
  const int srow = (threadIdx.x >> 2) & 63, sq = threadIdx.x & 3;
  hook.s1 = 0.f; hook.s2 = 0.f;
  hook.o0 = srow * 128 + (sq << 5);
  const int lane = threadIdx.x & 63, wave = __builtin_amdgcn_readfirstlane(threadIdx.x >> 6), wr = wave & 1,
            wc = (wave >> 1) & 3;
  const int nw0 = n0 + wc * 48;
  const int plain = (nw0 >= 1024) ? 7 : ((nw0 + 16 >= 1024) ? 6 : 0);
  bool comp;
  if (plain == 7)
    comp = gemm_main<BM, BN, WM, WK, WN, KT, NST, false, false, true, false, 7>(
        smem, x + (size_t)m0 * D_MODEL, D_MODEL, w + n0, 3 * D_MODEL, 128, 0, acc, cs, hook);
  else if (plain == 6)
    comp = gemm_main<BM, BN, WM, WK, WN, KT, NST, false, false, true, false, 6>(
        smem, x + (size_t)m0 * D_MODEL, D_MODEL, w + n0, 3 * D_MODEL, 128, 0, acc, cs, hook);
  else
    comp = gemm_main<BM, BN, WM, WK, WN, KT, NST, false, false, true, false, 0>(
        smem, x + (size_t)m0 * D_MODEL, D_MODEL, w + n0, 3 * D_MODEL, 128, 0, acc, cs, hook);

  float* tmean = (float*)(smem + C::LDS);
  float* trinv = tmean + 64;
  float* tcs = trinv + 64;
  if (!comp) {
    float s1 = hook.s1 + __shfl_xor(hook.s1, 1);
    float s2 = hook.s2 + __shfl_xor(hook.s2, 1);
    s1 += __shfl_xor(s1, 2);
    s2 += __shfl_xor(s2, 2);
    const float mean = s1 * (1.0f / 512.0f), ex2 = s2 * (1.0f / 512.0f);
    float var = ex2 - mean * mean;
    if (var < ex2 * (1.0f / 1024.0f)) {
      const bf16_t* xr = x + (size_t)(m0 + srow) * D_MODEL + sq * 128;
      float a2 = 0.f;
      for (int j = 0; j < 16; ++j) {
        u32x4 v = *(const u32x4*)(xr + j * 8);
#pragma unroll
        for (int e = 0; e < 4; ++e) {
          float d0 = lo_f(v[e]) - mean, d1 = hi_f(v[e]) - mean;
          a2 = fmaf(d0, d0, a2);
          a2 = fmaf(d1, d1, a2);
        }
      }
      a2 += __shfl_xor(a2, 1);
      a2 += __shfl_xor(a2, 2);
      var = a2 * (1.0f / 512.0f);
    }
    float mb, ri;
    ln_consts(mean, var, mb, ri);
    if (sq == 0) { tmean[srow] = mb; trinv[srow] = ri; }
    __syncthreads();
    return;
  }
  const int g = lane >> 4;
#pragma unroll
  for (int nf = 0; nf < C::NFW; ++nf) {
    if ((plain >> nf) & 1) {
      if (lane < 16) tcs[wr * 192 + wc * 48 + nf * 16 + lane] = cs[nf][0];
    } else if ((lane & 15) == 0) {
      *(f32x4*)(tcs + wr * 192 + wc * 48 + nf * 16 + 4 * g) = cs[nf];
    }
  }
  __syncthreads();
#pragma unroll
  for (int nf = 0; nf < C::NFW; ++nf) {
    if ((plain >> nf) & 1) {
      const int nl = wc * 48 + nf * 16 + (lane & 15);
      const float c = tcs[nl] + tcs[192 + nl];
      const int nv = n0 + nl - 1024;
#pragma unroll
      for (int mf = 0; mf < C::MF; ++mf) {
        const int ml = wr * 32 + mf * 16 + 4 * g;
        const f32x4 mb = *(const f32x4*)(tmean + ml), ri = *(const f32x4*)(trinv + ml);
        u32x2 o = {pack2((acc[mf][nf][0] - mb[0] * c) * ri[0], (acc[mf][nf][1] - mb[1] * c) * ri[1]),
                   pack2((acc[mf][nf][2] - mb[2] * c) * ri[2], (acc[mf][nf][3] - mb[3] * c) * ri[3])};
        const int m = m0 + ml;
        *(u32x2*)(v2 + (((size_t)(nv >> 4) * 256 + (m >> 3)) * 16 + (nv & 15)) * 8 + (m & 7)) = o;
      }
    } else {
      const int nl = wc * 48 + nf * 16 + 4 * g;
      f32x4 c = *(const f32x4*)(tcs + nl) + *(const f32x4*)(tcs + 192 + nl);
      const int n = n0 + nl;
#pragma unroll
      for (int mf = 0; mf < C::MF; ++mf) {
        const int ml = wr * 32 + mf * 16 + (lane & 15);
        const float mb = tmean[ml], ri = trinv[ml];
        u32x2 o = {pack2((acc[mf][nf][0] - mb * c[0]) * ri, (acc[mf][nf][1] - mb * c[1]) * ri),
                   pack2((acc[mf][nf][2] - mb * c[2]) * ri, (acc[mf][nf][3] - mb * c[3]) * ri)};
        *(u32x2*)(qkf + ((size_t)(n >> 3) * S_LEN + m0 + ml) * 8 + (n & 7)) = o;
      }
    }
  }
}

__global__ __launch_bounds__(512) void k2_attn(const bf16_t* __restrict__ qkf, const bf16_t* __restrict__ v2,
                                               bf16_t* __restrict__ o) {
  const int b = blockIdx.x, xcd = b & 7, idx = b >> 3;
  const int qt = xcd * 8 + (idx >> 2), hg = idx & 3;
  const int lane = threadIdx.x & 63, wave = threadIdx.x >> 6;
  const int h = hg * 8 + wave;
  const int r = lane & 31, hf = lane >> 5;
  const int q0 = qt * 32;
  const int t0 = (q0 >= 256) ? 0 : (256 - q0) / 32;

  const bf16x8 qf = *(const bf16x8*)(qkf + ((size_t)(h * 2 + hf) * S_LEN + q0 + r) * 8);
  const int pr = 16 * ((r >> 2) & 1) + 4 * (r >> 3) + (r & 3);
  const bf16_t* kp = qkf + ((size_t)(64 + h * 2 + hf) * S_LEN) * 8;
  bf16x8 kf[9];
#pragma unroll
  for (int t = 0; t < 9; ++t) {
    int key = q0 - 256 + 32 * t + pr;
    if (key < 0) key = 0;
    kf[t] = *(const bf16x8*)(kp + key * 8);
  }
  bf16x8 vf[9][2];
  const bf16_t* vp = v2 + ((size_t)h * 256 * 16 + (r & 15)) * 8;
#pragma unroll
  for (int t = 0; t < 9; ++t) {
    int kb = q0 - 256 + 32 * t;
    if (kb < 0) kb = 0;
    const int s8 = (kb >> 3) + 2 * hf;
    vf[t][0] = *(const bf16x8*)(vp + s8 * 128);
    vf[t][1] = *(const bf16x8*)(vp + s8 * 128 + 128);
  }
  f32x16 s[9];
  f32x16 zero;
#pragma unroll
  for (int i = 0; i < 16; ++i) zero[i] = 0.f;
#pragma unroll
  for (int t = 0; t < 9; ++t) s[t] = __builtin_amdgcn_mfma_f32_32x32x16_bf16(kf[t], qf, zero, 0, 0, 0);

  const float NEG = -3.0e38f;
#pragma unroll
  for (int i = 0; i < 16; ++i) {
    if (!(16 * hf + i > r)) s[0][i] = NEG;
    if (!(16 * hf + i <= r)) s[8][i] = NEG;
  }
  if (t0 > 0) {
#pragma unroll
    for (int t = 0; t < 8; ++t) {
      if (t < t0) {
#pragma unroll
        for (int i = 0; i < 16; ++i) s[t][i] = NEG;
      }
    }
  }
  float mx = NEG;
#pragma unroll
  for (int t = 0; t < 9; ++t)
#pragma unroll
    for (int i = 0; i < 16; ++i) mx = fmaxf(mx, s[t][i]);
  mx = fmaxf(mx, __shfl_xor(mx, 32));
  const float c = 0.25f * 1.4426950408889634f;
  const float mc = mx * c;
  bf16x8 special;
#pragma unroll
  for (int j = 0; j < 8; ++j) special[j] = (__bf16)((r == 16) ? 1.0f : 0.0f);
  f32x16 oacc = zero;
#pragma unroll
  for (int t = 0; t < 9; ++t) {
#pragma unroll
    for (int st = 0; st < 2; ++st) {
      bf16x8 pf;
#pragma unroll
      for (int j = 0; j < 8; ++j) pf[j] = (__bf16)__builtin_amdgcn_exp2f(fmaf(s[t][8 * st + j], c, -mc));
      bf16x8 a = (r < 16) ? vf[t][st] : special;
      oacc = __builtin_amdgcn_mfma_f32_32x32x16_bf16(a, pf, oacc, 0, 0, 0);
    }
  }
  const float l = __shfl(oacc[8], r);
  const float il = 1.0f / l;
  bf16_t* op = o + fragaddr(q0 + r, h * 16 + 4 * hf, 16);
  u32x2 o0 = {pack2(oacc[0] * il, oacc[1] * il), pack2(oacc[2] * il, oacc[3] * il)};
  u32x2 o1 = {pack2(oacc[4] * il, oacc[5] * il), pack2(oacc[6] * il, oacc[7] * il)};
  *(u32x2*)(op) = o0;
  *(u32x2*)(op + 128) = o1;
}

__global__ __launch_bounds__(768, 3) void k3_wo(const bf16_t* __restrict__ o, const bf16_t* __restrict__ w,
                                                const bf16_t* __restrict__ x, bf16_t* __restrict__ h,
                                                f32x2* __restrict__ stats) {
  constexpr int BM = 64, BN = 64, WM = 2, WK = 2, WN = 2, KT = 8, NST = 8;
  using C = GemmCfg<BM, BN, WM, WK, WN, KT, NST>;
  __shared__ __attribute__((aligned(16))) char smem[C::LDS + 8192];
  const int b = blockIdx.x, xcd = b & 7, idx = b >> 3;
  const int tm = xcd * 4 + (idx >> 3), tn = idx & 7;
  const int m0 = tm * BM, n0 = tn * BN;
  const int lane = threadIdx.x & 63, wave = threadIdx.x >> 6, wr = wave & 1, wk = (wave >> 1) & 1, wc = (wave >> 2) & 1;
  char* xs = smem + C::LDS;
  if (wave >= 8) {
    const int lw = __builtin_amdgcn_readfirstlane(wave) - 8;
#pragma unroll
    for (int i = 0; i < 2; ++i) {
      const int p = lw + 4 * i;
      glds16((const char*)(x + (size_t)(m0 + 8 * p + (lane >> 3)) * D_MODEL + n0) + (((lane & 7) ^ (lane >> 3)) << 4),
             xs + p * 1024);
    }
  }
  f32x4 acc[C::MF][C::NFW], cs[C::NFW], r[C::NFW];
#pragma unroll
  for (int i = 0; i < C::MF; ++i)
#pragma unroll
    for (int j = 0; j < C::NFW; ++j) acc[i][j] = f32x4{0.f, 0.f, 0.f, 0.f};
  NoHook hook;
  if (!gemm_main<BM, BN, WM, WK, WN, KT, NST, true, false, false, false, 0>(
          smem, o + (size_t)m0 * D_MODEL, 16, w + n0, D_MODEL, 128, (2 * (idx >> 3) + (idx & 7)) & 7, acc, cs, hook))
    return;
  splitk_combine<2>(smem, acc, r, wr + 2 * wc, wk, lane, true);
  const int ml = wr * 32 + wk * 16 + (lane & 15);
  const int nl = wc * 32 + 4 * (lane >> 4);
  const u32x2 rv0 = *(const u32x2*)(xs + ml * 128 + (((nl >> 3) ^ (ml & 7)) << 4) + (nl & 4) * 2);
  const u32x2 rv1 = *(const u32x2*)(xs + ml * 128 + ((((nl + 16) >> 3) ^ (ml & 7)) << 4) + (nl & 4) * 2);
  float hv[8];
  hv[0] = rbf(lo_f(rv0[0]) + rbf(r[0][0])); hv[1] = rbf(hi_f(rv0[0]) + rbf(r[0][1]));
  hv[2] = rbf(lo_f(rv0[1]) + rbf(r[0][2])); hv[3] = rbf(hi_f(rv0[1]) + rbf(r[0][3]));
  hv[4] = rbf(lo_f(rv1[0]) + rbf(r[1][0])); hv[5] = rbf(hi_f(rv1[0]) + rbf(r[1][1]));
  hv[6] = rbf(lo_f(rv1[1]) + rbf(r[1][2])); hv[7] = rbf(hi_f(rv1[1]) + rbf(r[1][3]));
  u32x2 o0 = {pack2(hv[0], hv[1]), pack2(hv[2], hv[3])};
  u32x2 o1 = {pack2(hv[4], hv[5]), pack2(hv[6], hv[7])};
  bf16_t* hp = h + fragaddr(m0 + ml, n0 + nl, 16);
  *(u32x2*)(hp) = o0;
  *(u32x2*)(hp + 256) = o1;
  float mean = 0.f;
#pragma unroll
  for (int j = 0; j < 8; ++j) mean += hv[j];
  mean *= 0.125f;
  float m2 = 0.f;
#pragma unroll
  for (int j = 0; j < 8; ++j) { float d = hv[j] - mean; m2 = fmaf(d, d, m2); }
  chan_eq(mean, m2, __shfl_xor(mean, 16), __shfl_xor(m2, 16), 8.f);
  chan_eq(mean, m2, __shfl_xor(mean, 32), __shfl_xor(m2, 32), 16.f);
  if (lane < 16) stats[(size_t)(tn * 2 + wc) * S_LEN + m0 + ml] = f32x2{mean, m2};
}

DI float gelu_f(float x) {
  const float k0 = 0.7978845608028654f * 2.0f * 1.4426950408889634f;
  float y = k0 * fmaf(0.044715f * x * x, x, x);
  float e = __builtin_amdgcn_exp2f(-y);
  return x * __builtin_amdgcn_rcpf(1.0f + e);
}

struct HStatHook {
  const f32x2* hp;
  float* dst;
  int cnt;
  DI void operator()(const char*) {
    if (cnt++ == 5) {
      f32x2 v[8];
#pragma unroll
      for (int j = 0; j < 8; ++j) v[j] = hp[(size_t)j * S_LEN];
      float mean = 0.f;
#pragma unroll
      for (int j = 0; j < 8; ++j) mean += v[j][0];
      mean *= 0.125f;
      float m2 = 0.f;
#pragma unroll
      for (int j = 0; j < 8; ++j) { float d = v[j][0] - mean; m2 += v[j][1] + 32.f * d * d; }
      dst[0] = mean;
      dst[1] = m2;
    }
  }
};

__global__ __launch_bounds__(768, 3) void k4_in(const bf16_t* __restrict__ h, const bf16_t* __restrict__ w,
                                                const f32x2* __restrict__ hstats, bf16_t* __restrict__ z1c,
                                                bf16_t* __restrict__ z2f, u64* __restrict__ ex) {
  constexpr int BM = 128, BN = 128, WM = 4, WK = 1, WN = 2, KT = 8, NST = 4;
  using C = GemmCfg<BM, BN, WM, WK, WN, KT, NST>;
  __shared__ __attribute__((aligned(16))) char smem[C::LDS + 8192];
  const int b = blockIdx.x, xcd = b & 7, idx = b >> 3;
  const int tm = xcd * 2 + (idx >= 14 ? 1 : 0), tn = (idx >= 14) ? idx - 14 : idx;
  const int m0 = tm * BM, n0 = tn * 64;
  const int lane = threadIdx.x & 63, wave = threadIdx.x >> 6, wr = wave & 3, wc = (wave >> 2) & 1;
  const int g = lane >> 4;
  float* tmean = (float*)(smem + C::LDS);
  float* trinv = tmean + 128;
  float* tcs = trinv + 128;
  float* tmean2 = tcs + 512;
  float* trinv2 = tmean2 + 128;
  float* tpart = trinv2 + 128;
  f32x2* tpart2 = (f32x2*)(tpart + 512);
  f32x4 acc[C::MF][C::NFW], cs[C::NFW];
#pragma unroll
  for (int i = 0; i < C::MF; ++i)
#pragma unroll
    for (int j = 0; j < C::NFW; ++j) acc[i][j] = f32x4{0.f, 0.f, 0.f, 0.f};
#pragma unroll
  for (int j = 0; j < C::NFW; ++j) cs[j] = f32x4{0.f, 0.f, 0.f, 0.f};
  HStatHook hook;
  hook.hp = hstats + (size_t)((threadIdx.x >> 7) & 1) * 8 * S_LEN + m0 + (threadIdx.x & 127);
  hook.dst = tpart + ((threadIdx.x >> 7) & 1) * 256 + (threadIdx.x & 127) * 2;
  hook.cnt = 0;
  const bool comp = gemm_main<BM, BN, WM, WK, WN, KT, NST, true, false, true, true, 12>(
      smem, h + (size_t)m0 * D_MODEL, 16, w + n0, 2 * FH, FH * 2, ((idx >= 14 ? 4 : 0) + tn) & 7, acc, cs, hook);
  if (!comp) {
    const int lt = threadIdx.x - 512;
    if (lt < 128) {
      float mean = tpart[lt * 2], m2 = tpart[lt * 2 + 1];
      chan_eq(mean, m2, tpart[256 + lt * 2], tpart[256 + lt * 2 + 1], 256.f);
      float mb, ri;
      ln_consts(mean, m2 * (1.0f / 512.0f), mb, ri);
      tmean[lt] = mb;
      trinv[lt] = ri;
    }
    __syncthreads();
    return;
  }
  if ((lane & 15) == 0) {
#pragma unroll
    for (int nf = 0; nf < 2; ++nf) *(f32x4*)(tcs + wr * 128 + wc * 32 + nf * 16 + 4 * g) = cs[nf];
  }
  if (lane < 16) {
#pragma unroll
    for (int nf = 2; nf < 4; ++nf) tcs[wr * 128 + 64 + wc * 32 + (nf & 1) * 16 + lane] = cs[nf][0];
  }
  __syncthreads();
  u64* exp_ = ex + (size_t)tm * (14 * 128);
  {
    float c2[2];
#pragma unroll
    for (int nf = 0; nf < 2; ++nf) {
      const int nl = 64 + wc * 32 + nf * 16 + (lane & 15);
      c2[nf] = (tcs[nl] + tcs[128 + nl]) + (tcs[256 + nl] + tcs[384 + nl]);
    }
#pragma unroll
    for (int mf = 0; mf < C::MF; ++mf) {
      const int ml = wr * 32 + mf * 16 + 4 * g;
      const f32x4 mb = *(const f32x4*)(tmean + ml), ri = *(const f32x4*)(trinv + ml);
      float pm = 0.f, pq = 0.f;
#pragma unroll
      for (int r = 0; r < 4; ++r) {
        const float a = rbf(gelu_f(rbf((acc[mf][2][r] - mb[r] * c2[0]) * ri[r])));
        const float bq = rbf(gelu_f(rbf((acc[mf][3][r] - mb[r] * c2[1]) * ri[r])));
        acc[mf][2][r] = a;
        acc[mf][3][r] = bq;
        const float mean = row16_sum(a + bq) * (1.0f / 32.0f);
        const float da = a - mean, db = bq - mean;
        const float m2 = row16_sum(fmaf(da, da, db * db));
        if ((lane & 3) == r) { pm = mean; pq = m2; }
      }
      if ((lane & 15) < 4) ((f32x2*)tpart)[wc * 128 + ml + (lane & 3)] = f32x2{pm, pq};
    }
  }
  __syncthreads();
  if (threadIdx.x < 128) {
    f32x2 a = ((const f32x2*)tpart)[threadIdx.x], bq = ((const f32x2*)tpart)[128 + threadIdx.x];
    float mean = a[0], m2 = a[1];
    chan_eq(mean, m2, bq[0], bq[1], 32.f);
    __hip_atomic_store(exp_ + tn * 128 + threadIdx.x,
                       ((u64)(__float_as_uint(m2) | 1u) << 32) | (u64)__float_as_uint(mean), __ATOMIC_RELAXED,
                       __HIP_MEMORY_SCOPE_AGENT);
  }
#pragma unroll
  for (int nf = 0; nf < 2; ++nf) {
    const int nl = wc * 32 + nf * 16 + 4 * g;
    f32x4 c = (*(const f32x4*)(tcs + nl) + *(const f32x4*)(tcs + 128 + nl)) +
              (*(const f32x4*)(tcs + 256 + nl) + *(const f32x4*)(tcs + 384 + nl));
#pragma unroll
    for (int mf = 0; mf < C::MF; ++mf) {
      const int ml = wr * 32 + mf * 16 + (lane & 15);
      const float mb = tmean[ml], ri = trinv[ml];
      float v[4];
#pragma unroll
      for (int r = 0; r < 4; ++r) v[r] = gelu_f(rbf((acc[mf][nf][r] - mb * c[r]) * ri));
      u32x2 ov = {pack2(v[0], v[1]), pack2(v[2], v[3])};
      *(u32x2*)(z1c + ((size_t)((m0 >> 4) + wr * 2 + mf) * 56 + (n0 >> 4) + wc * 2 + nf) * 256 + lane * 4) = ov;
    }
  }
  if (threadIdx.x < 256) {
    const int t = threadIdx.x, row = t & 127, half = t >> 7;
    const u64* gp = exp_ + half * (7 * 128) + row;
    float vm[7], vq[7];
    for (unsigned spins = 0;; ++spins) {
      bool ok = true;
#pragma unroll
      for (int j = 0; j < 7; ++j) {
        u64 gg = __hip_atomic_load(gp + j * 128, __ATOMIC_RELAXED, __HIP_MEMORY_SCOPE_AGENT);
        vm[j] = __uint_as_float((unsigned)gg);
        vq[j] = __uint_as_float((unsigned)(gg >> 32));
        ok &= ((unsigned)(gg >> 32) != 0u);
      }
      if (__all(ok)) break;
      if (spins > 4000000u) break;
      __builtin_amdgcn_s_sleep(1);
    }
    float hm = 0.f, hq = 0.f;
#pragma unroll
    for (int j = 0; j < 7; ++j) { hm += vm[j]; hq += vq[j]; }
    hm *= (1.0f / 7.0f);
#pragma unroll
    for (int j = 0; j < 7; ++j) { float d = vm[j] - hm; hq = fmaf(64.f * d, d, hq); }
    tpart2[t] = f32x2{hm, hq};
  }
  __syncthreads();
  if (threadIdx.x < 128) {
    f32x2 a = tpart2[threadIdx.x], bq = tpart2[128 + threadIdx.x];
    float mean = a[0], m2 = a[1];
    chan_eq(mean, m2, bq[0], bq[1], 448.f);
    float mb, ri;
    ln_consts(mean, m2 * (1.0f / 896.0f), mb, ri);
    tmean2[threadIdx.x] = mb;
    trinv2[threadIdx.x] = ri;
  }
  __syncthreads();
#pragma unroll
  for (int mf = 0; mf < C::MF; ++mf) {
    const int ml = wr * 32 + mf * 16 + 4 * g;
    const f32x4 mb = *(const f32x4*)(tmean2 + ml), ri = *(const f32x4*)(trinv2 + ml);
#pragma unroll
    for (int nf = 2; nf < 4; ++nf) {
      const int n = n0 + wc * 32 + (nf & 1) * 16 + (lane & 15);
      float v0 = rbf(acc[mf][nf][0] - mb[0]) * ri[0], v1 = rbf(acc[mf][nf][1] - mb[1]) * ri[1];
      float v2 = rbf(acc[mf][nf][2] - mb[2]) * ri[2], v3 = rbf(acc[mf][nf][3] - mb[3]) * ri[3];
      u32x2 ov = {pack2(v0, v1), pack2(v2, v3)};
      *(u32x2*)(z2f + fragaddr(n, m0 + ml, 64)) = ov;
    }
  }
}

__global__ __launch_bounds__(768, 3) void k5_sp(const bf16_t* __restrict__ wsp, const bf16_t* __restrict__ z2f,
                                                const bf16_t* __restrict__ bsp, const bf16_t* __restrict__ z1c,
                                                bf16_t* __restrict__ y) {
  constexpr int BM = 64, BN = 112, WM = 2, WK = 2, WN = 2, KT = 32, NST = 7;
  using C = GemmCfg<BM, BN, WM, WK, WN, KT, NST>;
  __shared__ __attribute__((aligned(16))) char smem[C::LDS];
  const int b = blockIdx.x, xcd = b & 7, idx = b >> 3;
  const int tm = (xcd >> 2) * 16 + (idx >> 1), tn = (xcd & 3) * 2 + (idx & 1);
  const int m0 = tm * BM, n0 = tn * BN;
  const int lane = threadIdx.x & 63, wave = threadIdx.x >> 6, wr = wave & 1, wk = (wave >> 1) & 1, wc = (wave >> 2) & 1;
  const bool fulln = (wc == 0);
  const int m = m0 + wr * 32 + wk * 16 + (lane & 15);
  const int nb = n0 + wc * 64;
  float bb = 0.f;
  u32x2 zv[4];
  if (wave < 8) {
    bb = (float)bsp[m];
#pragma unroll
    for (int nf = 0; nf < 4; ++nf)
      if (nf < 3 || fulln) zv[nf] = *(const u32x2*)(z1c + ((size_t)(m >> 4) * 56 + (nb >> 4) + nf) * 256 + lane * 4);
  }
  f32x4 acc[C::MF][C::NFW], cs[C::NFW], r[C::NFW];
#pragma unroll
  for (int i = 0; i < C::MF; ++i)
#pragma unroll
    for (int j = 0; j < C::NFW; ++j) acc[i][j] = f32x4{0.f, 0.f, 0.f, 0.f};
  NoHook hook;
  if (!gemm_main<BM, BN, WM, WK, WN, KT, NST, false, true, false, false, 0>(
          smem, wsp + (size_t)m0 * S_LEN, S_LEN, z2f + (size_t)(n0 >> 4) * 64 * 512, 64, 0,
          (idx >> 1) & 7, acc, cs, hook))
    return;
  splitk_combine<4>(smem, acc, r, wr + 2 * wc, wk, lane, fulln);
#pragma unroll
  for (int nf = 0; nf < C::NFW; ++nf) {
    if (nf < 3 || fulln) {
      float v0 = lo_f(zv[nf][0]) * rbf(rbf(r[nf][0]) + bb);
      float v1 = hi_f(zv[nf][0]) * rbf(rbf(r[nf][1]) + bb);
      float v2 = lo_f(zv[nf][1]) * rbf(rbf(r[nf][2]) + bb);
      float v3 = hi_f(zv[nf][1]) * rbf(rbf(r[nf][3]) + bb);
      u32x2 ov = {pack2(v0, v1), pack2(v2, v3)};
      *(u32x2*)(y + fragaddr(m, nb + nf * 16 + 4 * (lane >> 4), 28)) = ov;
    }
  }
}

__global__ __launch_bounds__(768, 3) void k6_out(const bf16_t* __restrict__ y, const bf16_t* __restrict__ w,
                                                 const bf16_t* __restrict__ h, bf16_t* __restrict__ out) {
  constexpr int BM = 64, BN = 64, WM = 2, WK = 2, WN = 2, KT = 14, NST = 9;
  using C = GemmCfg<BM, BN, WM, WK, WN, KT, NST>;
  __shared__ __attribute__((aligned(16))) char smem[C::LDS + 8192];
  const int b = blockIdx.x, xcd = b & 7, idx = b >> 3;
  const int tm = xcd * 4 + (idx >> 3), tn = idx & 7;
  const int m0 = tm * BM, n0 = tn * BN;
  const int lane = threadIdx.x & 63, wave = threadIdx.x >> 6, wr = wave & 1, wk = (wave >> 1) & 1, wc = (wave >> 2) & 1;
  const int ml = wr * 32 + wk * 16 + (lane & 15);
  const int nl = wc * 32 + 4 * (lane >> 4);
  u32x2 rv0 = {0u, 0u}, rv1 = {0u, 0u};
  if (wave < 8) {
    const bf16_t* hp = h + fragaddr(m0 + ml, n0 + nl, 16);
    rv0 = *(const u32x2*)(hp);
    rv1 = *(const u32x2*)(hp + 256);
  }
  f32x4 acc[C::MF][C::NFW], cs[C::NFW], r[C::NFW];
#pragma unroll
  for (int i = 0; i < C::MF; ++i)
#pragma unroll
    for (int j = 0; j < C::NFW; ++j) acc[i][j] = f32x4{0.f, 0.f, 0.f, 0.f};
  NoHook hook;
  if (!gemm_main<BM, BN, WM, WK, WN, KT, NST, true, false, false, false, 0>(
          smem, y + (size_t)(m0 >> 4) * 28 * 512, 28, w + n0, D_MODEL, 128, ((idx >> 3) * 3 + (idx & 7) * 2) % KT, acc, cs,
          hook))
    return;
  splitk_combine<2>(smem, acc, r, wr + 2 * wc, wk, lane, true);
  u32x2 o0 = {pack2(lo_f(rv0[0]) + rbf(r[0][0]), hi_f(rv0[0]) + rbf(r[0][1])),
              pack2(lo_f(rv0[1]) + rbf(r[0][2]), hi_f(rv0[1]) + rbf(r[0][3]))};
  u32x2 o1 = {pack2(lo_f(rv1[0]) + rbf(r[1][0]), hi_f(rv1[0]) + rbf(r[1][1])),
              pack2(lo_f(rv1[1]) + rbf(r[1][2]), hi_f(rv1[1]) + rbf(r[1][3]))};
  char* os = smem + C::LDS;
  *(u32x2*)(os + ml * 128 + (((nl >> 3) ^ (ml & 7)) << 4) + (nl & 4) * 2) = o0;
  *(u32x2*)(os + ml * 128 + ((((nl + 16) >> 3) ^ (ml & 7)) << 4) + (nl & 4) * 2) = o1;
  __syncthreads();
  const int row = threadIdx.x >> 3, ch = threadIdx.x & 7;
  const u32x4 ov = *(const u32x4*)(os + row * 128 + ((ch ^ (row & 7)) << 4));
  *(u32x4*)(out + (size_t)(m0 + row) * D_MODEL + n0 + ch * 8) = ov;
}

#define RUNK(i)
extern "C" void kernel_launch(void* const* d_in, const int* in_sizes, int n_in, void* d_out, int out_size,
                              void* d_ws, size_t ws_size, hipStream_t stream) {
  const bf16_t* x = (const bf16_t*)d_in[0];
  const bf16_t* wqkv = (const bf16_t*)d_in[1];
  const bf16_t* wo = (const bf16_t*)d_in[2];
  const bf16_t* w_in = (const bf16_t*)d_in[4];
  const bf16_t* w_sp = (const bf16_t*)d_in[5];
  const bf16_t* b_sp = (const bf16_t*)d_in[6];
  const bf16_t* w_out = (const bf16_t*)d_in[7];
  bf16_t* out = (bf16_t*)d_out;

  char* ws = (char*)d_ws;
  size_t off = 0;
  auto take = [&](size_t bytes) { char* p = ws + off; off += (bytes + 255) & ~(size_t)255; return p; };
  bf16_t* qkf = (bf16_t*)take((size_t)S_LEN * 1024 * 2);
  bf16_t* v2 = (bf16_t*)take((size_t)512 * S_LEN * 2);
  bf16_t* o = (bf16_t*)take((size_t)S_LEN * D_MODEL * 2);
  bf16_t* h = (bf16_t*)take((size_t)S_LEN * D_MODEL * 2);
  f32x2* hstats = (f32x2*)take((size_t)S_LEN * 16 * 8);
  bf16_t* z1c = (bf16_t*)take((size_t)S_LEN * FH * 2);
  bf16_t* z2f = (bf16_t*)take((size_t)S_LEN * FH * 2);
  bf16_t* y = (bf16_t*)take((size_t)S_LEN * FH * 2);
  u64* ex = (u64*)take((size_t)1024 * 1024);

  RUNK(1) k1_qkv<<<256, 768, 0, stream>>>(x, wqkv, qkf, v2, (u32x4*)ex);
  RUNK(2) k2_attn<<<256, 512, 0, stream>>>(qkf, v2, o);
  RUNK(3) k3_wo<<<256, 768, 0, stream>>>(o, wo, x, h, hstats);
  RUNK(4) k4_in<<<224, 768, 0, stream>>>(h, w_in, hstats, z1c, z2f, ex);
  RUNK(5) k5_sp<<<256, 768, 0, stream>>>(w_sp, z2f, b_sp, z1c, y);
  RUNK(6) k6_out<<<256, 768, 0, stream>>>(y, w_out, h, out);
}
```

```cpp
#include <hip/hip_runtime.h>
#include <hip/hip_bf16.h>
#include <stdint.h>

typedef __bf16 bf16_t;
typedef __attribute__((ext_vector_type(8))) __bf16 bf16x8;
typedef __attribute__((ext_vector_type(2))) __bf16 bf16x2;
typedef __attribute__((ext_vector_type(4))) short s16x4;
typedef __attribute__((ext_vector_type(8))) short s16x8;
typedef __attribute__((ext_vector_type(4))) float f32x4;
typedef __attribute__((ext_vector_type(2))) float f32x2;
typedef __attribute__((ext_vector_type(16))) float f32x16;
typedef __attribute__((ext_vector_type(4))) unsigned u32x4;
typedef __attribute__((ext_vector_type(2))) unsigned u32x2;
typedef unsigned long long u64;

#define DI __device__ __forceinline__

constexpr int S_LEN = 2048;
constexpr int D_MODEL = 512;
constexpr int FH = 896;

DI float rbf(float x) { return (float)(__bf16)x; }
DI unsigned pack2(float a, float b) {
  bf16x2 v = {(__bf16)a, (__bf16)b};
  return __builtin_bit_cast(unsigned, v);
}
DI float lo_f(unsigned u) { return __uint_as_float(u << 16); }
DI float hi_f(unsigned u) { return __uint_as_float(u & 0xffff0000u); }

#define ST2(p, v) (*(u32x2*)(p) = (v))
template <int N> DI void wait_vm() { asm volatile("s_waitcnt vmcnt(%0)" ::"n"(N) : "memory"); }

DI void glds16(const void* g, char* l) {
  __builtin_amdgcn_global_load_lds((const __attribute__((address_space(1))) void*)g,
                                   (__attribute__((address_space(3))) void*)l, 16, 0, 0);
}
DI s16x4 tr_read(const char* l) {
  return __builtin_amdgcn_ds_read_tr16_b64_v4i16((__attribute__((address_space(3))) s16x4*)l);
}

template <int V> struct IC { static constexpr int value = V; };
constexpr int cmin3(int a, int b, int c) { return a < b ? (a < c ? a : c) : (b < c ? b : c); }
template <int N, int I = 0, class F> DI void static_for(F& f) {
  if constexpr (I < N) {
    f(IC<I>{});
    static_for<N, I + 1>(f);
  }
}

DI int fragaddr(int m, int k, int K32) {
  return ((m >> 4) * K32 + (k >> 5)) * 512 + ((m & 15) + 16 * ((k & 31) >> 3)) * 8 + (k & 7);
}

DI int bimg(int k, int cc) {
  return k * 128 + ((((cc >> 2) & 1) ^ ((k >> 1) & 1)) << 6) + (((cc & 3) ^ (((k >> 3) & 1) << 1)) << 4);
}

#define PF4 0
#define SPB5 2
#define SPB6 2
#define SPB3 2
template <int BM, int BN, int WM, int WK, int WN, int KT, int NST>
struct GemmCfg {
  static constexpr int NW = WM * WN * WK;
  static constexpr int NL = 4;
  static constexpr int PA = BM / 8, PB = BN / 8, P = PA + PB;
  static constexpr int PPL = (P + NL - 1) / NL;
  static constexpr int STAGE = (BM + BN) * 128;
  static constexpr int MF = BM / WM / 16;
  static constexpr int NFRAG = BN / 16;
  static constexpr int NFW = (NFRAG + WN - 1) / WN;
  static constexpr int LDS = STAGE * NST;
  static constexpr bool RESIDENT = NST >= KT;
  static constexpr int SPB = (KT == 32) ? SPB5 : (KT == 14) ? SPB6 : (BN == 64) ? SPB3 : 1;
  static constexpr int KS = KT / SPB;
  static constexpr int PRE = RESIDENT ? KS : NST / SPB - 1;
  static_assert(KT % SPB == 0 && NST % SPB == 0, "");
};

struct NoHook {
  DI void operator()(const char*) const {}
};

template <int BM, int BN, int WM, int WK, int WN, int KT, int NST, bool AFRAG, bool BFRAG, int PW, class Hook>
DI void gemm_loader(char* smem, const bf16_t* __restrict__ A, int lda, const bf16_t* __restrict__ B, int ldb,
                    int pstride, int lw, int rot, Hook& hook) {
  using C = GemmCfg<BM, BN, WM, WK, WN, KT, NST>;
  constexpr int NL = C::NL, PA = C::PA, STAGE = C::STAGE, PRE = C::PRE;
  static_assert(PA % NL == 0, "A pieces must be a multiple of the loader count");
  static_assert(BFRAG || (BN % 64) == 0, "row-major B needs 64-column panels");
  const int lane = threadIdx.x & 63;
  unsigned off[PW];
#pragma unroll
  for (int i = 0; i < PW; ++i) {
    if (i < PA / NL) {
      const int p = lw + i * NL;
      if constexpr (AFRAG) {
        off[i] = (unsigned)(((p >> 1) * lda + (p & 1)) * 1024 + lane * 16);
      } else {
        off[i] = (unsigned)((8 * p + (lane >> 3)) * lda * 2 + (((lane & 7) ^ (lane >> 3)) << 4));
      }
    } else {
      const int p = lw + i * NL - PA;
      if constexpr (BFRAG) {
        off[i] = (unsigned)(((p >> 1) * ldb + (p & 1)) * 1024 + lane * 16);
      } else {
        int k = (p & 7) * 8 + (lane >> 3);
        int cc = (lane & 7) ^ (((lane >> 4) & 1) << 2) ^ ((p & 1) << 1);
        off[i] = (unsigned)(k * ldb * 2 + (p >> 3) * pstride + cc * 16);
      }
    }
  }
  rot = 0;
  int kr = __builtin_amdgcn_readfirstlane(rot), kw = 0;
  auto issue = [&](int slot) {
    char* base = smem + slot * STAGE + lw * 1024;
    const int kk = kw + kr;
    const char* pa = (const char*)A + kk * (AFRAG ? 2048 : 128);
    const char* pb = (const char*)B + (size_t)kk * (BFRAG ? 2048 : 128 * ldb);
#pragma unroll
    for (int i = 0; i < PW; ++i) {
      unsigned o = off[i];
      asm volatile("" : "+v"(o));
      glds16((i < PA / NL ? pa : pb) + o, base + i * NL * 1024);
    }
    if constexpr (KT % 8 == 0) {
      kr = (kr + 1) & 7;
      if (kr == rot) kw += 8;
    } else {
      kr = (kr + 1 == KT) ? 0 : kr + 1;
    }
  };
  constexpr int SPB = C::SPB, KS = C::KS;
  auto body = [&](auto KTC) {
    constexpr int kt = decltype(KTC)::value;
    constexpr int I0 = (kt == 0) ? 0 : cmin3(KS, kt - 1 + PRE, 2 * kt);
    constexpr int I1 = cmin3(KS, kt + PRE, 2 * kt + 2);
#pragma unroll
    for (int u = I0 * SPB; u < I1 * SPB; ++u) issue(u % NST);
    wait_vm<(I1 - kt - 1) * PW * SPB>();
    __builtin_amdgcn_s_barrier();
    asm volatile("" ::: "memory");
#pragma unroll
    for (int u = 0; u < SPB; ++u) hook(smem + ((kt * SPB + u) % NST) * STAGE);
    __builtin_amdgcn_s_waitcnt(0xC07F);
  };
  static_for<KS>(body);
}

template <int BM, int BN, int WM, int WK, int WN, int KT, int NST, bool AFRAG, bool BFRAG, bool CS, bool FULLN,
          bool SPLITN, int PLAIN>
DI void gemm_compute(char* smem, int wave,
                     f32x4 (&acc)[GemmCfg<BM, BN, WM, WK, WN, KT, NST>::MF][GemmCfg<BM, BN, WM, WK, WN, KT, NST>::NFW],
                     f32x4 (&cs)[GemmCfg<BM, BN, WM, WK, WN, KT, NST>::NFW]) {
  using C = GemmCfg<BM, BN, WM, WK, WN, KT, NST>;
  constexpr int STAGE = C::STAGE, MF = C::MF, NFW = C::NFW;
  constexpr int NF = FULLN ? NFW : NFW - 1;
  const int lane = threadIdx.x & 63;
  const int wr = wave % WM, wk = (wave / WM) % WK, wc = wave / (WM * WK);
  const int g = lane >> 4, q = (lane & 15) >> 2, pp = lane & 3;
  constexpr int NKS = (WK == 2) ? 1 : 2;
  int aoff[NKS];
#pragma unroll
  for (int i = 0; i < NKS; ++i) {
    const int ks = (WK == 2) ? wk : i;
    if constexpr (AFRAG) aoff[i] = (wr * MF * 2 + ks) * 1024 + lane * 16;
    else aoff[i] = (wr * (MF * 16) + (lane & 15)) * 128 + (((4 * ks + g) ^ (lane & 7)) << 4);
  }
  int boff[NF];
#pragma unroll
  for (int nf = 0; nf < NF; ++nf) {
    const int f = SPLITN ? ((nf >> 1) * 4 + 2 * wc + (nf & 1)) : (wc * NFW + nf);
    if constexpr (BFRAG) {
      boff[nf] = BM * 128 + (f * 2 + ((WK == 2) ? wk : 0)) * 1024 + lane * 16;
    } else {
      const int kk = 8 * g + q + ((WK == 2) ? wk * 32 : 0);
      boff[nf] = BM * 128 + (f >> 2) * 8192 + bimg(kk, 2 * (f & 3) + (pp >> 1)) + 8 * (pp & 1);
    }
  }
  bf16x8 ones;
#pragma unroll
  for (int j = 0; j < 8; ++j) ones[j] = (__bf16)1.0f;

  bf16x8 af[2][MF], bfr[2][NF];
  auto step = [&](auto JC) {
    constexpr int j = decltype(JC)::value;
    constexpr int NSTEP = KT * NKS;
    if constexpr (j < NSTEP) {
      constexpr int kt = j / NKS, i = j % NKS;
      if constexpr (i == 0 && kt % C::SPB == 0) {
        __builtin_amdgcn_s_barrier();
        asm volatile("" ::: "memory");
      }
      const char* sb = smem + (kt % NST) * STAGE;
#pragma unroll
      for (int mf = 0; mf < MF; ++mf) af[j & 1][mf] = *(const bf16x8*)(sb + aoff[i] + mf * 2048);
#pragma unroll
      for (int nf = 0; nf < NF; ++nf) {
        if constexpr (BFRAG) {
          bfr[j & 1][nf] = *(const bf16x8*)(sb + boff[nf] + i * 1024);
        } else {
          s16x4 lo = tr_read(sb + boff[nf] + i * 4096);
          s16x4 hi = tr_read(sb + boff[nf] + i * 4096 + 512);
          s16x8 v = __builtin_shufflevector(lo, hi, 0, 1, 2, 3, 4, 5, 6, 7);
          bfr[j & 1][nf] = __builtin_bit_cast(bf16x8, v);
        }
      }
    }
    if constexpr (j > 0) {
      constexpr int jp = j - 1;
#pragma unroll
      for (int nf = 0; nf < NF; ++nf) {
#pragma unroll
        for (int mf = 0; mf < MF; ++mf) {
          if ((PLAIN >> nf) & 1)
            acc[mf][nf] = __builtin_amdgcn_mfma_f32_16x16x32_bf16(af[jp & 1][mf], bfr[jp & 1][nf], acc[mf][nf], 0, 0, 0);
          else
            acc[mf][nf] = __builtin_amdgcn_mfma_f32_16x16x32_bf16(bfr[jp & 1][nf], af[jp & 1][mf], acc[mf][nf], 0, 0, 0);
        }
      }
      if constexpr (j < NSTEP) {
        constexpr int NRD = MF + (BFRAG ? NF : 2 * NF), NMM = MF * NF;
        constexpr int RPM = (NRD + NMM - 1) / NMM;
#pragma unroll
        for (int u = 0; u < NMM; ++u) {
          __builtin_amdgcn_sched_group_barrier(0x8, 1, 0);
          __builtin_amdgcn_sched_group_barrier(0x100, RPM, 0);
        }
      }
      __builtin_amdgcn_sched_barrier(0);
      if constexpr (CS) {
        if (wr == (jp % WM)) {
#pragma unroll
          for (int nf = 0; nf < NF; ++nf) {
            if ((PLAIN >> nf) & 1) cs[nf] = __builtin_amdgcn_mfma_f32_16x16x32_bf16(ones, bfr[jp & 1][nf], cs[nf], 0, 0, 0);
            else cs[nf] = __builtin_amdgcn_mfma_f32_16x16x32_bf16(bfr[jp & 1][nf], ones, cs[nf], 0, 0, 0);
          }
        }
      }
    }
    __builtin_amdgcn_sched_barrier(0);
    __builtin_amdgcn_s_waitcnt(0xC07F);
  };
  static_for<KT * NKS + 1>(step);
}

template <int BM, int BN, int WM, int WK, int WN, int KT, int NST, bool AFRAG, bool BFRAG, bool CS, bool SPLITN,
          int PLAIN, class Hook>
DI bool gemm_main(char* smem, const bf16_t* __restrict__ A, int lda, const bf16_t* __restrict__ B, int ldb, int pstride,
                  int rot,
                  f32x4 (&acc)[GemmCfg<BM, BN, WM, WK, WN, KT, NST>::MF][GemmCfg<BM, BN, WM, WK, WN, KT, NST>::NFW],
                  f32x4 (&cs)[GemmCfg<BM, BN, WM, WK, WN, KT, NST>::NFW], Hook& hook) {
  using C = GemmCfg<BM, BN, WM, WK, WN, KT, NST>;
  const int wave = __builtin_amdgcn_readfirstlane(threadIdx.x >> 6);
  if (wave >= C::NW) {
    const int lw = wave - C::NW;
    __builtin_amdgcn_s_setprio(3);
    constexpr bool EVEN = (C::P % C::NL) == 0;
    if (EVEN || lw < (C::P % C::NL))
      gemm_loader<BM, BN, WM, WK, WN, KT, NST, AFRAG, BFRAG, C::PPL>(smem, A, lda, B, ldb, pstride, lw, rot, hook);
    else if constexpr (!EVEN)
      gemm_loader<BM, BN, WM, WK, WN, KT, NST, AFRAG, BFRAG, C::PPL - 1>(smem, A, lda, B, ldb, pstride, lw, rot, hook);
    return false;
  }
  const int wc = wave / (WM * WK);
  constexpr bool ALLFULL = (C::NFRAG == WN * C::NFW);
  if (ALLFULL || wc != WN - 1)
    gemm_compute<BM, BN, WM, WK, WN, KT, NST, AFRAG, BFRAG, CS, true, SPLITN, PLAIN>(smem, wave, acc, cs);
  else if constexpr (!ALLFULL)
    gemm_compute<BM, BN, WM, WK, WN, KT, NST, AFRAG, BFRAG, CS, false, SPLITN, PLAIN>(smem, wave, acc, cs);
  return true;
}

template <int NFW>
DI void splitk_combine(char* smem, f32x4 (&acc)[2][NFW], f32x4 (&res)[NFW], int pairid, int wk, int lane, bool fulln) {
  __syncthreads();
  char* mine = smem + (pairid * 2 + wk) * (NFW * 1024) + lane * 16;
  char* theirs = smem + (pairid * 2 + (wk ^ 1)) * (NFW * 1024) + lane * 16;
#pragma unroll
  for (int nf = 0; nf < NFW; ++nf)
    if (nf < NFW - 1 || fulln) *(f32x4*)(mine + nf * 1024) = wk ? acc[0][nf] : acc[1][nf];
  __syncthreads();
#pragma unroll
  for (int nf = 0; nf < NFW; ++nf)
    if (nf < NFW - 1 || fulln) res[nf] = (wk ? acc[1][nf] : acc[0][nf]) + *(const f32x4*)(theirs + nf * 1024);
}

DI void chan_eq(float& mean, float& m2, float mean_b, float m2_b, float n) {
  float d = mean_b - mean;
  m2 = m2 + m2_b + d * d * (0.5f * n);
  mean = 0.5f * (mean + mean_b);
}

DI void ln_consts(float mean, float var, float& mean_b, float& rinv) {
  mean_b = rbf(mean);
  float v = rbf(var);
  float t = rbf(v + 1e-5f);
  float sd = rbf(sqrtf(t));
  rinv = 1.0f / sd;
}

DI void l2_prefetch(const char* base, int nlines, int t, int nt) {
  unsigned acc = 0u;
  for (int i = t; i < nlines; i += 4 * nt) {
    unsigned v0 = *(const unsigned*)(base + (size_t)i * 128);
    unsigned v1 = (i + nt < nlines) ? *(const unsigned*)(base + (size_t)(i + nt) * 128) : 0u;
    unsigned v2 = (i + 2 * nt < nlines) ? *(const unsigned*)(base + (size_t)(i + 2 * nt) * 128) : 0u;
    unsigned v3 = (i + 3 * nt < nlines) ? *(const unsigned*)(base + (size_t)(i + 3 * nt) * 128) : 0u;
    acc ^= v0 ^ v1 ^ v2 ^ v3;
  }
  asm volatile("" ::"v"(acc));
}

DI float row16_sum(float v) {
  v += __builtin_bit_cast(float, __builtin_amdgcn_update_dpp(0, __builtin_bit_cast(int, v), 0x128, 0xf, 0xf, false));
  v += __builtin_bit_cast(float, __builtin_amdgcn_update_dpp(0, __builtin_bit_cast(int, v), 0x124, 0xf, 0xf, false));
  v += __builtin_bit_cast(float, __builtin_amdgcn_update_dpp(0, __builtin_bit_cast(int, v), 0x122, 0xf, 0xf, false));
  v += __builtin_bit_cast(float, __builtin_amdgcn_update_dpp(0, __builtin_bit_cast(int, v), 0x121, 0xf, 0xf, false));
  return v;
}

struct StatHook {
  float s1, s2;
  int o0;
  DI void operator()(const char* sb) {
    bf16x2 one2 = {(__bf16)1.0f, (__bf16)1.0f};
    u32x4 v0 = *(const u32x4*)(sb + o0);
    u32x4 v1 = *(const u32x4*)(sb + o0 + 16);
#pragma unroll
    for (int e = 0; e < 4; ++e) {
      const unsigned ua = v0[e], ub = v1[e];
      bf16x2 a = __builtin_bit_cast(bf16x2, ua), b = __builtin_bit_cast(bf16x2, ub);
      s1 = __builtin_amdgcn_fdot2_f32_bf16(a, one2, s1, false);
      s2 = __builtin_amdgcn_fdot2_f32_bf16(a, a, s2, false);
      s1 = __builtin_amdgcn_fdot2_f32_bf16(b, one2, s1, false);
      s2 = __builtin_amdgcn_fdot2_f32_bf16(b, b, s2, false);
    }
  }
};

__global__ __launch_bounds__(768, 3) void k1_qkv(const bf16_t* __restrict__ x, const bf16_t* __restrict__ w,
                                                 bf16_t* __restrict__ qkf, bf16_t* __restrict__ v2,
                                                 u32x4* __restrict__ exz, const bf16_t* __restrict__ wo,
                                                 const bf16_t* __restrict__ win) {
  constexpr int BM = 64, BN = 192, WM = 2, WK = 1, WN = 4, KT = 8, NST = 4;
  using C = GemmCfg<BM, BN, WM, WK, WN, KT, NST>;
  __shared__ __attribute__((aligned(16))) char smem[C::LDS + 2048];
  if (threadIdx.x < 256) exz[blockIdx.x * 256 + threadIdx.x] = u32x4{0u, 0u, 0u, 0u};

  const int b = blockIdx.x, xcd = b & 7, idx = b >> 3;
  const int tm = xcd * 4 + (idx >> 3), tn = idx & 7;
  const int m0 = tm * BM, n0 = tn * BN;
  f32x4 acc[C::MF][C::NFW], cs[C::NFW];
#pragma unroll
  for (int i = 0; i < C::MF; ++i)
#pragma unroll
    for (int j = 0; j < C::NFW; ++j) acc[i][j] = f32x4{0.f, 0.f, 0.f, 0.f};
#pragma unroll
  for (int j = 0; j < C::NFW; ++j) cs[j] = f32x4{0.f, 0.f, 0.f, 0.f};
  StatHook hook;
  const int srow = (threadIdx.x >> 2) & 63, sq = threadIdx.x & 3;
  hook.s1 = 0.f; hook.s2 = 0.f;
  hook.o0 = srow * 128 + (sq << 5);
  const int lane = threadIdx.x & 63, wave = __builtin_amdgcn_readfirstlane(threadIdx.x >> 6), wr = wave & 1,
            wc = (wave >> 1) & 3;
  const int nw0 = n0 + wc * 48;
  const int plain = (nw0 >= 1024) ? 7 : ((nw0 + 16 >= 1024) ? 6 : 0);
  bool comp;
  if (plain == 7)
    comp = gemm_main<BM, BN, WM, WK, WN, KT, NST, false, false, true, false, 7>(
        smem, x + (size_t)m0 * D_MODEL, D_MODEL, w + n0, 3 * D_MODEL, 128, 0, acc, cs, hook);
  else if (plain == 6)
    comp = gemm_main<BM, BN, WM, WK, WN, KT, NST, false, false, true, false, 6>(
        smem, x + (size_t)m0 * D_MODEL, D_MODEL, w + n0, 3 * D_MODEL, 128, 0, acc, cs, hook);
  else
    comp = gemm_main<BM, BN, WM, WK, WN, KT, NST, false, false, true, false, 0>(
        smem, x + (size_t)m0 * D_MODEL, D_MODEL, w + n0, 3 * D_MODEL, 128, 0, acc, cs, hook);

  float* tmean = (float*)(smem + C::LDS);
  float* trinv = tmean + 64;
  float* tcs = trinv + 64;
  if (!comp) {
    float s1 = hook.s1 + __shfl_xor(hook.s1, 1);
    float s2 = hook.s2 + __shfl_xor(hook.s2, 1);
    s1 += __shfl_xor(s1, 2);
    s2 += __shfl_xor(s2, 2);
    const float mean = s1 * (1.0f / 512.0f), ex2 = s2 * (1.0f / 512.0f);
    float var = ex2 - mean * mean;
    if (var < ex2 * (1.0f / 1024.0f)) {
      const bf16_t* xr = x + (size_t)(m0 + srow) * D_MODEL + sq * 128;
      float a2 = 0.f;
      for (int j = 0; j < 16; ++j) {
        u32x4 v = *(const u32x4*)(xr + j * 8);
#pragma unroll
        for (int e = 0; e < 4; ++e) {
          float d0 = lo_f(v[e]) - mean, d1 = hi_f(v[e]) - mean;
          a2 = fmaf(d0, d0, a2);
          a2 = fmaf(d1, d1, a2);
        }
      }
      a2 += __shfl_xor(a2, 1);
      a2 += __shfl_xor(a2, 2);
      var = a2 * (1.0f / 512.0f);
    }
    float mb, ri;
    ln_consts(mean, var, mb, ri);
    if (sq == 0) { tmean[srow] = mb; trinv[srow] = ri; }
    __syncthreads();
    {
      const int lt = threadIdx.x - 512;
      if (lt < 72) {
        const char* p0 = (lt < 16) ? (const char*)wo + (size_t)(b * 16 + lt) * 128
                                   : (const char*)win + (size_t)(b * 56 + lt - 16) * 128;
        unsigned v0 = *(const unsigned*)p0;
        asm volatile("" ::"v"(v0));
      } else if (lt < 74) {
        unsigned v0 = *(const unsigned*)((const char*)qkf + (size_t)(b * 2 + lt - 72) * 49152);
        asm volatile("" ::"v"(v0));
      }
    }
    return;
  }
  const int g = lane >> 4;
#pragma unroll
  for (int nf = 0; nf < C::NFW; ++nf) {
    if ((plain >> nf) & 1) {
      if (lane < 16) tcs[wr * 192 + wc * 48 + nf * 16 + lane] = cs[nf][0];
    } else if ((lane & 15) == 0) {
      *(f32x4*)(tcs + wr * 192 + wc * 48 + nf * 16 + 4 * g) = cs[nf];
    }
  }
  __syncthreads();
#pragma unroll
  for (int nf = 0; nf < C::NFW; ++nf) {
    if ((plain >> nf) & 1) {
      const int nl = wc * 48 + nf * 16 + (lane & 15);
      const float c = tcs[nl] + tcs[192 + nl];
      const int nv = n0 + nl - 1024;
#pragma unroll
      for (int mf = 0; mf < C::MF; ++mf) {
        const int ml = wr * 32 + mf * 16 + 4 * g;
        const f32x4 mb = *(const f32x4*)(tmean + ml), ri = *(const f32x4*)(trinv + ml);
        u32x2 o = {pack2((acc[mf][nf][0] - mb[0] * c) * ri[0], (acc[mf][nf][1] - mb[1] * c) * ri[1]),
                   pack2((acc[mf][nf][2] - mb[2] * c) * ri[2], (acc[mf][nf][3] - mb[3] * c) * ri[3])};
        const int m = m0 + ml;
        ST2(v2 + (((size_t)(nv >> 4) * 256 + (m >> 3)) * 16 + (nv & 15)) * 8 + (m & 7), o);
      }
    } else {
      const int nl = wc * 48 + nf * 16 + 4 * g;
      f32x4 c = *(const f32x4*)(tcs + nl) + *(const f32x4*)(tcs + 192 + nl);
      const int n = n0 + nl;
#pragma unroll
      for (int mf = 0; mf < C::MF; ++mf) {
        const int ml = wr * 32 + mf * 16 + (lane & 15);
        const float mb = tmean[ml], ri = trinv[ml];
        u32x2 o = {pack2((acc[mf][nf][0] - mb * c[0]) * ri, (acc[mf][nf][1] - mb * c[1]) * ri),
                   pack2((acc[mf][nf][2] - mb * c[2]) * ri, (acc[mf][nf][3] - mb * c[3]) * ri)};
        ST2(qkf + ((size_t)(n >> 3) * S_LEN + m0 + ml) * 8 + (n & 7), o);
      }
    }
  }
}

__global__ __launch_bounds__(512) void k2_attn(const bf16_t* __restrict__ qkf, const bf16_t* __restrict__ v2,
                                               bf16_t* __restrict__ o) {
  const int b = blockIdx.x, xcd = b & 7, idx = b >> 3;
  const int qt = xcd * 8 + (idx >> 2), hg = idx & 3;
  const int lane = threadIdx.x & 63, wave = threadIdx.x >> 6;
  const int h = hg * 8 + wave;
  const int r = lane & 31, hf = lane >> 5;
  const int q0 = qt * 32;
  const int t0 = (q0 >= 256) ? 0 : (256 - q0) / 32;

  const bf16x8 qf = *(const bf16x8*)(qkf + ((size_t)(h * 2 + hf) * S_LEN + q0 + r) * 8);
  const int pr = 16 * ((r >> 2) & 1) + 4 * (r >> 3) + (r & 3);
  const bf16_t* kp = qkf + ((size_t)(64 + h * 2 + hf) * S_LEN) * 8;
  bf16x8 kf[9];
#pragma unroll
  for (int t = 0; t < 9; ++t) {
    int key = q0 - 256 + 32 * t + pr;
    if (key < 0) key = 0;
    kf[t] = *(const bf16x8*)(kp + key * 8);
  }
  bf16x8 vf[9][2];
  const bf16_t* vp = v2 + ((size_t)h * 256 * 16 + (r & 15)) * 8;
#pragma unroll
  for (int t = 0; t < 9; ++t) {
    int kb = q0 - 256 + 32 * t;
    if (kb < 0) kb = 0;
    const int s8 = (kb >> 3) + 2 * hf;
    vf[t][0] = *(const bf16x8*)(vp + s8 * 128);
    vf[t][1] = *(const bf16x8*)(vp + s8 * 128 + 128);
  }
  f32x16 s[9];
  f32x16 zero;
#pragma unroll
  for (int i = 0; i < 16; ++i) zero[i] = 0.f;
#pragma unroll
  for (int t = 0; t < 9; ++t) s[t] = __builtin_amdgcn_mfma_f32_32x32x16_bf16(kf[t], qf, zero, 0, 0, 0);

  const float NEG = -3.0e38f;
#pragma unroll
  for (int i = 0; i < 16; ++i) {
    if (!(16 * hf + i > r)) s[0][i] = NEG;
    if (!(16 * hf + i <= r)) s[8][i] = NEG;
  }
  if (t0 > 0) {
#pragma unroll
    for (int t = 0; t < 8; ++t) {
      if (t < t0) {
#pragma unroll
        for (int i = 0; i < 16; ++i) s[t][i] = NEG;
      }
    }
  }
  float mx = NEG;
#pragma unroll
  for (int t = 0; t < 9; ++t)
#pragma unroll
    for (int i = 0; i < 16; ++i) mx = fmaxf(mx, s[t][i]);
  mx = fmaxf(mx, __shfl_xor(mx, 32));
  const float c = 0.25f * 1.4426950408889634f;
  const float mc = mx * c;
  bf16x8 special;
#pragma unroll
  for (int j = 0; j < 8; ++j) special[j] = (__bf16)((r == 16) ? 1.0f : 0.0f);
  f32x16 oacc = zero;
#pragma unroll
  for (int t = 0; t < 9; ++t) {
#pragma unroll
    for (int st = 0; st < 2; ++st) {
      bf16x8 pf;
#pragma unroll
      for (int j = 0; j < 8; ++j) pf[j] = (__bf16)__builtin_amdgcn_exp2f(fmaf(s[t][8 * st + j], c, -mc));
      bf16x8 a = (r < 16) ? vf[t][st] : special;
      oacc = __builtin_amdgcn_mfma_f32_32x32x16_bf16(a, pf, oacc, 0, 0, 0);
    }
  }
  const float l = __shfl(oacc[8], r);
  const float il = 1.0f / l;
  bf16_t* op = o + fragaddr(q0 + r, h * 16 + 4 * hf, 16);
  u32x2 o0 = {pack2(oacc[0] * il, oacc[1] * il), pack2(oacc[2] * il, oacc[3] * il)};
  u32x2 o1 = {pack2(oacc[4] * il, oacc[5] * il), pack2(oacc[6] * il, oacc[7] * il)};
  ST2(op, o0);
  ST2(op + 128, o1);
}

__global__ __launch_bounds__(768, 3) void k3_wo(const bf16_t* __restrict__ o, const bf16_t* __restrict__ w,
                                                const bf16_t* __restrict__ x, bf16_t* __restrict__ h,
                                                f32x2* __restrict__ stats, const bf16_t* __restrict__ wsp,
                                                const bf16_t* __restrict__ wout) {
  constexpr int BM = 64, BN = 64, WM = 2, WK = 2, WN = 2, KT = 8, NST = 8;
  using C = GemmCfg<BM, BN, WM, WK, WN, KT, NST>;
  __shared__ __attribute__((aligned(16))) char smem[C::LDS + 8192];
  const int b = blockIdx.x, xcd = b & 7, idx = b >> 3;
  const int tm = xcd * 4 + (idx >> 3), tn = idx & 7;
  const int m0 = tm * BM, n0 = tn * BN;
  const int lane = threadIdx.x & 63, wave = threadIdx.x >> 6, wr = wave & 1, wk = (wave >> 1) & 1, wc = (wave >> 2) & 1;
  char* xs = smem + C::LDS;
  if (wave >= 8) {
    const int lw = __builtin_amdgcn_readfirstlane(wave) - 8;
#pragma unroll
    for (int i = 0; i < 2; ++i) {
      const int p = lw + 4 * i;
      glds16((const char*)(x + (size_t)(m0 + 8 * p + (lane >> 3)) * D_MODEL + n0) + (((lane & 7) ^ (lane >> 3)) << 4),
             xs + p * 1024);
    }
  }
  f32x4 acc[C::MF][C::NFW], cs[C::NFW], r[C::NFW];
#pragma unroll
  for (int i = 0; i < C::MF; ++i)
#pragma unroll
    for (int j = 0; j < C::NFW; ++j) acc[i][j] = f32x4{0.f, 0.f, 0.f, 0.f};
  NoHook hook;
  if (!gemm_main<BM, BN, WM, WK, WN, KT, NST, true, false, false, false, 0>(
          smem, o + (size_t)m0 * D_MODEL, 16, w + n0, D_MODEL, 128, (2 * (idx >> 3) + (idx & 7)) & 7, acc, cs, hook)) {
    const int lt = threadIdx.x - 512;
    unsigned v0 = *(const unsigned*)((const char*)wsp + (size_t)(b * 256 + lt) * 128);
    unsigned v1 = (lt < 28) ? *(const unsigned*)((const char*)wout + (size_t)(b * 28 + lt) * 128) : 0u;
    asm volatile("" ::"v"(v0 ^ v1));
    return;
  }
  splitk_combine<2>(smem, acc, r, wr + 2 * wc, wk, lane, true);
  const int ml = wr * 32 + wk * 16 + (lane & 15);
  const int nl = wc * 32 + 4 * (lane >> 4);
  const u32x2 rv0 = *(const u32x2*)(xs + ml * 128 + (((nl >> 3) ^ (ml & 7)) << 4) + (nl & 4) * 2);
  const u32x2 rv1 = *(const u32x2*)(xs + ml * 128 + ((((nl + 16) >> 3) ^ (ml & 7)) << 4) + (nl & 4) * 2);
  float hv[8];
  hv[0] = rbf(lo_f(rv0[0]) + rbf(r[0][0])); hv[1] = rbf(hi_f(rv0[0]) + rbf(r[0][1]));
  hv[2] = rbf(lo_f(rv0[1]) + rbf(r[0][2])); hv[3] = rbf(hi_f(rv0[1]) + rbf(r[0][3]));
  hv[4] = rbf(lo_f(rv1[0]) + rbf(r[1][0])); hv[5] = rbf(hi_f(rv1[0]) + rbf(r[1][1]));
  hv[6] = rbf(lo_f(rv1[1]) + rbf(r[1][2])); hv[7] = rbf(hi_f(rv1[1]) + rbf(r[1][3]));
  u32x2 o0 = {pack2(hv[0], hv[1]), pack2(hv[2], hv[3])};
  u32x2 o1 = {pack2(hv[4], hv[5]), pack2(hv[6], hv[7])};
  bf16_t* hp = h + fragaddr(m0 + ml, n0 + nl, 16);
  ST2(hp, o0);
  ST2(hp + 256, o1);
  float mean = 0.f;
#pragma unroll
  for (int j = 0; j < 8; ++j) mean += hv[j];
  mean *= 0.125f;
  float m2 = 0.f;
#pragma unroll
  for (int j = 0; j < 8; ++j) { float d = hv[j] - mean; m2 = fmaf(d, d, m2); }
  chan_eq(mean, m2, __shfl_xor(mean, 16), __shfl_xor(m2, 16), 8.f);
  chan_eq(mean, m2, __shfl_xor(mean, 32), __shfl_xor(m2, 32), 16.f);
  if (lane < 16) stats[(size_t)(tn * 2 + wc) * S_LEN + m0 + ml] = f32x2{mean, m2};
}

DI float gelu_f(float x) {
  const float k0 = 0.7978845608028654f * 2.0f * 1.4426950408889634f;
  float y = k0 * fmaf(0.044715f * x * x, x, x);
  float e = __builtin_amdgcn_exp2f(-y);
  return x * __builtin_amdgcn_rcpf(1.0f + e);
}

struct HStatHook {
  const f32x2* hp;
  float* dst;
  int cnt;
  DI void operator()(const char*) {
    if (cnt++ == 5) {
      f32x2 v[8];
#pragma unroll
      for (int j = 0; j < 8; ++j) v[j] = hp[(size_t)j * S_LEN];
      float mean = 0.f;
#pragma unroll
      for (int j = 0; j < 8; ++j) mean += v[j][0];
      mean *= 0.125f;
      float m2 = 0.f;
#pragma unroll
      for (int j = 0; j < 8; ++j) { float d = v[j][0] - mean; m2 += v[j][1] + 32.f * d * d; }
      dst[0] = mean;
      dst[1] = m2;
    }
  }
};

__global__ __launch_bounds__(768, 3) void k4_in(const bf16_t* __restrict__ h, const bf16_t* __restrict__ w,
                                                const f32x2* __restrict__ hstats, bf16_t* __restrict__ z1c,
                                                bf16_t* __restrict__ z2f, u64* __restrict__ ex,
                                                const bf16_t* __restrict__ wsp) {
  constexpr int BM = 128, BN = 128, WM = 4, WK = 1, WN = 2, KT = 8, NST = 4;
  using C = GemmCfg<BM, BN, WM, WK, WN, KT, NST>;
  __shared__ __attribute__((aligned(16))) char smem[C::LDS + 8192];
  const int b = blockIdx.x, xcd = b & 7, idx = b >> 3;
  const int tm = xcd * 2 + (idx >= 14 ? 1 : 0), tn = (idx >= 14) ? idx - 14 : idx;
  const int m0 = tm * BM, n0 = tn * 64;
  const int lane = threadIdx.x & 63, wave = threadIdx.x >> 6, wr = wave & 3, wc = (wave >> 2) & 1;
  const int g = lane >> 4;
  float* tmean = (float*)(smem + C::LDS);
  float* trinv = tmean + 128;
  float* tcs = trinv + 128;
  float* tmean2 = tcs + 512;
  float* trinv2 = tmean2 + 128;
  float* tpart = trinv2 + 128;
  f32x2* tpart2 = (f32x2*)(tpart + 512);
  f32x4 acc[C::MF][C::NFW], cs[C::NFW];
#pragma unroll
  for (int i = 0; i < C::MF; ++i)
#pragma unroll
    for (int j = 0; j < C::NFW; ++j) acc[i][j] = f32x4{0.f, 0.f, 0.f, 0.f};
#pragma unroll
  for (int j = 0; j < C::NFW; ++j) cs[j] = f32x4{0.f, 0.f, 0.f, 0.f};
  HStatHook hook;
  hook.hp = hstats + (size_t)((threadIdx.x >> 7) & 1) * 8 * S_LEN + m0 + (threadIdx.x & 127);
  hook.dst = tpart + ((threadIdx.x >> 7) & 1) * 256 + (threadIdx.x & 127) * 2;
  hook.cnt = 0;
  const bool comp = gemm_main<BM, BN, WM, WK, WN, KT, NST, true, false, true, true, 12>(
      smem, h + (size_t)m0 * D_MODEL, 16, w + n0, 2 * FH, FH * 2, ((idx >= 14 ? 4 : 0) + tn) & 7, acc, cs, hook);
  if (!comp) {
    const int lt = threadIdx.x - 512;
    if (lt < 128) {
      float mean = tpart[lt * 2], m2 = tpart[lt * 2 + 1];
      chan_eq(mean, m2, tpart[256 + lt * 2], tpart[256 + lt * 2 + 1], 256.f);
      float mb, ri;
      ln_consts(mean, m2 * (1.0f / 512.0f), mb, ri);
      tmean[lt] = mb;
      trinv[lt] = ri;
    }
    __syncthreads();
    return;
  }
  if ((lane & 15) == 0) {
#pragma unroll
    for (int nf = 0; nf < 2; ++nf) *(f32x4*)(tcs + wr * 128 + wc * 32 + nf * 16 + 4 * g) = cs[nf];
  }
  if (lane < 16) {
#pragma unroll
    for (int nf = 2; nf < 4; ++nf) tcs[wr * 128 + 64 + wc * 32 + (nf & 1) * 16 + lane] = cs[nf][0];
  }
  __syncthreads();
  u64* exp_ = ex + (size_t)tm * (14 * 128);
  {
    float c2[2];
#pragma unroll
    for (int nf = 0; nf < 2; ++nf) {
      const int nl = 64 + wc * 32 + nf * 16 + (lane & 15);
      c2[nf] = (tcs[nl] + tcs[128 + nl]) + (tcs[256 + nl] + tcs[384 + nl]);
    }
#pragma unroll
    for (int mf = 0; mf < C::MF; ++mf) {
      const int ml = wr * 32 + mf * 16 + 4 * g;
      const f32x4 mb = *(const f32x4*)(tmean + ml), ri = *(const f32x4*)(trinv + ml);
      float pm = 0.f, pq = 0.f;
#pragma unroll
      for (int r = 0; r < 4; ++r) {
        const float a = rbf(gelu_f(rbf((acc[mf][2][r] - mb[r] * c2[0]) * ri[r])));
        const float bq = rbf(gelu_f(rbf((acc[mf][3][r] - mb[r] * c2[1]) * ri[r])));
        acc[mf][2][r] = a;
        acc[mf][3][r] = bq;
        const float mean = row16_sum(a + bq) * (1.0f / 32.0f);
        const float da = a - mean, db = bq - mean;
        const float m2 = row16_sum(fmaf(da, da, db * db));
        if ((lane & 3) == r) { pm = mean; pq = m2; }
      }
      if ((lane & 15) < 4) ((f32x2*)tpart)[wc * 128 + ml + (lane & 3)] = f32x2{pm, pq};
    }
  }
  __syncthreads();
  if (threadIdx.x < 128) {
    f32x2 a = ((const f32x2*)tpart)[threadIdx.x], bq = ((const f32x2*)tpart)[128 + threadIdx.x];
    float mean = a[0], m2 = a[1];
    chan_eq(mean, m2, bq[0], bq[1], 32.f);
    __hip_atomic_store(exp_ + tn * 128 + threadIdx.x,
                       ((u64)(__float_as_uint(m2) | 1u) << 32) | (u64)__float_as_uint(mean), __ATOMIC_RELAXED,
                       __HIP_MEMORY_SCOPE_AGENT);
  }
#pragma unroll
  for (int nf = 0; nf < 2; ++nf) {
    const int nl = wc * 32 + nf * 16 + 4 * g;
    f32x4 c = (*(const f32x4*)(tcs + nl) + *(const f32x4*)(tcs + 128 + nl)) +
              (*(const f32x4*)(tcs + 256 + nl) + *(const f32x4*)(tcs + 384 + nl));
#pragma unroll
    for (int mf = 0; mf < C::MF; ++mf) {
      const int ml = wr * 32 + mf * 16 + (lane & 15);
      const float mb = tmean[ml], ri = trinv[ml];
      float v[4];
#pragma unroll
      for (int r = 0; r < 4; ++r) v[r] = gelu_f(rbf((acc[mf][nf][r] - mb * c[r]) * ri));
      u32x2 ov = {pack2(v[0], v[1]), pack2(v[2], v[3])};
      ST2(z1c + ((size_t)((m0 >> 4) + wr * 2 + mf) * 56 + (n0 >> 4) + wc * 2 + nf) * 256 + lane * 4, ov);
    }
  }
  if (threadIdx.x < 256) {
    const int t = threadIdx.x, row = t & 127, half = t >> 7;
    const u64* gp = exp_ + half * (7 * 128) + row;
    float vm[7], vq[7];
    for (unsigned spins = 0;; ++spins) {
      bool ok = true;
#pragma unroll
      for (int j = 0; j < 7; ++j) {
        u64 gg = __hip_atomic_load(gp + j * 128, __ATOMIC_RELAXED, __HIP_MEMORY_SCOPE_AGENT);
        vm[j] = __uint_as_float((unsigned)gg);
        vq[j] = __uint_as_float((unsigned)(gg >> 32));
        ok &= ((unsigned)(gg >> 32) != 0u);
      }
      if (__all(ok)) break;
      if (spins > 4000000u) break;
      __builtin_amdgcn_s_sleep(1);
    }
    float hm = 0.f, hq = 0.f;
#pragma unroll
    for (int j = 0; j < 7; ++j) { hm += vm[j]; hq += vq[j]; }
    hm *= (1.0f / 7.0f);
#pragma unroll
    for (int j = 0; j < 7; ++j) { float d = vm[j] - hm; hq = fmaf(64.f * d, d, hq); }
    tpart2[t] = f32x2{hm, hq};
  }
  __syncthreads();
  if (threadIdx.x < 128) {
    f32x2 a = tpart2[threadIdx.x], bq = tpart2[128 + threadIdx.x];
    float mean = a[0], m2 = a[1];
    chan_eq(mean, m2, bq[0], bq[1], 448.f);
    float mb, ri;
    ln_consts(mean, m2 * (1.0f / 896.0f), mb, ri);
    tmean2[threadIdx.x] = mb;
    trinv2[threadIdx.x] = ri;
  }
  __syncthreads();
#pragma unroll
  for (int mf = 0; mf < C::MF; ++mf) {
    const int ml = wr * 32 + mf * 16 + 4 * g;
    const f32x4 mb = *(const f32x4*)(tmean2 + ml), ri = *(const f32x4*)(trinv2 + ml);
#pragma unroll
    for (int nf = 2; nf < 4; ++nf) {
      const int n = n0 + wc * 32 + (nf & 1) * 16 + (lane & 15);
      float v0 = rbf(acc[mf][nf][0] - mb[0]) * ri[0], v1 = rbf(acc[mf][nf][1] - mb[1]) * ri[1];
      float v2 = rbf(acc[mf][nf][2] - mb[2]) * ri[2], v3 = rbf(acc[mf][nf][3] - mb[3]) * ri[3];
      u32x2 ov = {pack2(v0, v1), pack2(v2, v3)};
      ST2(z2f + fragaddr(n, m0 + ml, 64), ov);
    }
  }
}

__global__ __launch_bounds__(768, 3) void k5_sp(const bf16_t* __restrict__ wsp, const bf16_t* __restrict__ z2f,
                                                const bf16_t* __restrict__ bsp, const bf16_t* __restrict__ z1c,
                                                bf16_t* __restrict__ y) {
  constexpr int BM = 64, BN = 112, WM = 2, WK = 2, WN = 2, KT = 32, NST = (SPB5 == 2) ? 6 : 7;
  using C = GemmCfg<BM, BN, WM, WK, WN, KT, NST>;
  __shared__ __attribute__((aligned(16))) char smem[C::LDS];
  const int b = blockIdx.x, xcd = b & 7, idx = b >> 3;
  const int tm = (xcd >> 2) * 16 + (idx >> 1), tn = (xcd & 3) * 2 + (idx & 1);
  const int m0 = tm * BM, n0 = tn * BN;
  const int lane = threadIdx.x & 63, wave = threadIdx.x >> 6, wr = wave & 1, wk = (wave >> 1) & 1, wc = (wave >> 2) & 1;
  const bool fulln = (wc == 0);
  const int m = m0 + wr * 32 + wk * 16 + (lane & 15);
  const int nb = n0 + wc * 64;
  float bb = 0.f;
  u32x2 zv[4];
  if (wave < 8) {
    bb = (float)bsp[m];
#pragma unroll
    for (int nf = 0; nf < 4; ++nf)
      if (nf < 3 || fulln) zv[nf] = *(const u32x2*)(z1c + ((size_t)(m >> 4) * 56 + (nb >> 4) + nf) * 256 + lane * 4);
  }
  f32x4 acc[C::MF][C::NFW], cs[C::NFW], r[C::NFW];
#pragma unroll
  for (int i = 0; i < C::MF; ++i)
#pragma unroll
    for (int j = 0; j < C::NFW; ++j) acc[i][j] = f32x4{0.f, 0.f, 0.f, 0.f};
  NoHook hook;
  if (!gemm_main<BM, BN, WM, WK, WN, KT, NST, false, true, false, false, 0>(
          smem, wsp + (size_t)m0 * S_LEN, S_LEN, z2f + (size_t)(n0 >> 4) * 64 * 512, 64, 0,
          (idx >> 1) & 7, acc, cs, hook))
    return;
  splitk_combine<4>(smem, acc, r, wr + 2 * wc, wk, lane, fulln);
#pragma unroll
  for (int nf = 0; nf < C::NFW; ++nf) {
    if (nf < 3 || fulln) {
      float v0 = lo_f(zv[nf][0]) * rbf(rbf(r[nf][0]) + bb);
      float v1 = hi_f(zv[nf][0]) * rbf(rbf(r[nf][1]) + bb);
      float v2 = lo_f(zv[nf][1]) * rbf(rbf(r[nf][2]) + bb);
      float v3 = hi_f(zv[nf][1]) * rbf(rbf(r[nf][3]) + bb);
      u32x2 ov = {pack2(v0, v1), pack2(v2, v3)};
      ST2(y + fragaddr(m, nb + nf * 16 + 4 * (lane >> 4), 28), ov);
    }
  }
}

__global__ __launch_bounds__(768, 3) void k6_out(const bf16_t* __restrict__ y, const bf16_t* __restrict__ w,
                                                 const bf16_t* __restrict__ h, bf16_t* __restrict__ out) {
  constexpr int BM = 64, BN = 64, WM = 2, WK = 2, WN = 2, KT = 14, NST = (SPB6 == 2) ? 8 : 9;
  using C = GemmCfg<BM, BN, WM, WK, WN, KT, NST>;
  __shared__ __attribute__((aligned(16))) char smem[C::LDS + 8192];
  const int b = blockIdx.x, xcd = b & 7, idx = b >> 3;
  const int tm = xcd * 4 + (idx >> 3), tn = idx & 7;
  const int m0 = tm * BM, n0 = tn * BN;
  const int lane = threadIdx.x & 63, wave = threadIdx.x >> 6, wr = wave & 1, wk = (wave >> 1) & 1, wc = (wave >> 2) & 1;
  const int ml = wr * 32 + wk * 16 + (lane & 15);
  const int nl = wc * 32 + 4 * (lane >> 4);
  u32x2 rv0 = {0u, 0u}, rv1 = {0u, 0u};
  if (wave < 8) {
    const bf16_t* hp = h + fragaddr(m0 + ml, n0 + nl, 16);
    rv0 = *(const u32x2*)(hp);
    rv1 = *(const u32x2*)(hp + 256);
  }
  f32x4 acc[C::MF][C::NFW], cs[C::NFW], r[C::NFW];
#pragma unroll
  for (int i = 0; i < C::MF; ++i)
#pragma unroll
    for (int j = 0; j < C::NFW; ++j) acc[i][j] = f32x4{0.f, 0.f, 0.f, 0.f};
  NoHook hook;
  if (!gemm_main<BM, BN, WM, WK, WN, KT, NST, true, false, false, false, 0>(
          smem, y + (size_t)(m0 >> 4) * 28 * 512, 28, w + n0, D_MODEL, 128, ((idx >> 3) * 3 + (idx & 7) * 2) % KT, acc, cs,
          hook))
    return;
  splitk_combine<2>(smem, acc, r, wr + 2 * wc, wk, lane, true);
  u32x2 o0 = {pack2(lo_f(rv0[0]) + rbf(r[0][0]), hi_f(rv0[0]) + rbf(r[0][1])),
              pack2(lo_f(rv0[1]) + rbf(r[0][2]), hi_f(rv0[1]) + rbf(r[0][3]))};
  u32x2 o1 = {pack2(lo_f(rv1[0]) + rbf(r[1][0]), hi_f(rv1[0]) + rbf(r[1][1])),
              pack2(lo_f(rv1[1]) + rbf(r[1][2]), hi_f(rv1[1]) + rbf(r[1][3]))};
  char* os = smem + C::LDS;
  *(u32x2*)(os + ml * 128 + (((nl >> 3) ^ (ml & 7)) << 4) + (nl & 4) * 2) = o0;
  *(u32x2*)(os + ml * 128 + ((((nl + 16) >> 3) ^ (ml & 7)) << 4) + (nl & 4) * 2) = o1;
  __syncthreads();
  const int row = threadIdx.x >> 3, ch = threadIdx.x & 7;
  const u32x4 ov = *(const u32x4*)(os + row * 128 + ((ch ^ (row & 7)) << 4));
  *(u32x4*)(out + (size_t)(m0 + row) * D_MODEL + n0 + ch * 8) = ov;
}

#define RUNK(i)
extern "C" void kernel_launch(void* const* d_in, const int* in_sizes, int n_in, void* d_out, int out_size,
                              void* d_ws, size_t ws_size, hipStream_t stream) {
  const bf16_t* x = (const bf16_t*)d_in[0];
  const bf16_t* wqkv = (const bf16_t*)d_in[1];
  const bf16_t* wo = (const bf16_t*)d_in[2];
  const bf16_t* w_in = (const bf16_t*)d_in[4];
  const bf16_t* w_sp = (const bf16_t*)d_in[5];
  const bf16_t* b_sp = (const bf16_t*)d_in[6];
  const bf16_t* w_out = (const bf16_t*)d_in[7];
  bf16_t* out = (bf16_t*)d_out;

  char* ws = (char*)d_ws;
  size_t off = 0;
  auto take = [&](size_t bytes) { char* p = ws + off; off += (bytes + 255) & ~(size_t)255; return p; };
  bf16_t* qkf = (bf16_t*)take((size_t)S_LEN * 1024 * 2);
  bf16_t* v2 = (bf16_t*)take((size_t)512 * S_LEN * 2);
  bf16_t* o = (bf16_t*)take((size_t)S_LEN * D_MODEL * 2);
  bf16_t* h = (bf16_t*)take((size_t)S_LEN * D_MODEL * 2);
  f32x2* hstats = (f32x2*)take((size_t)S_LEN * 16 * 8);
  bf16_t* z1c = (bf16_t*)take((size_t)S_LEN * FH * 2);
  bf16_t* z2f = (bf16_t*)take((size_t)S_LEN * FH * 2);
  bf16_t* y = (bf16_t*)take((size_t)S_LEN * FH * 2);
  u64* ex = (u64*)take((size_t)1024 * 1024);

  RUNK(1) k1_qkv<<<256, 768, 0, stream>>>(x, wqkv, qkf, v2, (u32x4*)ex, wo, w_in);
  RUNK(2) k2_attn<<<256, 512, 0, stream>>>(qkf, v2, o);
  RUNK(3) k3_wo<<<256, 768, 0, stream>>>(o, wo, x, h, hstats, w_sp, w_out);
  RUNK(4) k4_in<<<224, 768, 0, stream>>>(h, w_in, hstats, z1c, z2f, ex, w_sp);
  RUNK(5) k5_sp<<<256, 768, 0, stream>>>(w_sp, z2f, b_sp, z1c, y);
  RUNK(6) k6_out<<<256, 768, 0, stream>>>(y, w_out, h, out);
}
```

```cpp
#include <hip/hip_runtime.h>
#include <hip/hip_bf16.h>
#include <stdint.h>

typedef __bf16 bf16_t;
typedef __attribute__((ext_vector_type(8))) __bf16 bf16x8;
typedef __attribute__((ext_vector_type(2))) __bf16 bf16x2;
typedef __attribute__((ext_vector_type(4))) short s16x4;
typedef __attribute__((ext_vector_type(8))) short s16x8;
typedef __attribute__((ext_vector_type(4))) float f32x4;
typedef __attribute__((ext_vector_type(2))) float f32x2;
typedef __attribute__((ext_vector_type(16))) float f32x16;
typedef __attribute__((ext_vector_type(4))) unsigned u32x4;
typedef __attribute__((ext_vector_type(2))) unsigned u32x2;
typedef unsigned long long u64;

#define DI __device__ __forceinline__

constexpr int S_LEN = 2048;
constexpr int D_MODEL = 512;
constexpr int FH = 896;

DI float rbf(float x) { return (float)(__bf16)x; }
DI unsigned pack2(float a, float b) {
  bf16x2 v = {(__bf16)a, (__bf16)b};
  return __builtin_bit_cast(unsigned, v);
}
DI float lo_f(unsigned u) { return __uint_as_float(u << 16); }
DI float hi_f(unsigned u) { return __uint_as_float(u & 0xffff0000u); }

#define ST2(p, v) (*(u32x2*)(p) = (v))
DI unsigned vtid() { return threadIdx.x < 256u ? threadIdx.x + 512u : threadIdx.x - 256u; }
template <int N> DI void wait_vm() { asm volatile("s_waitcnt vmcnt(%0)" ::"n"(N) : "memory"); }

DI void glds16(const void* g, char* l) {
  __builtin_amdgcn_global_load_lds((const __attribute__((address_space(1))) void*)g,
                                   (__attribute__((address_space(3))) void*)l, 16, 0, 0);
}
DI s16x4 tr_read(const char* l) {
  return __builtin_amdgcn_ds_read_tr16_b64_v4i16((__attribute__((address_space(3))) s16x4*)l);
}

template <int V> struct IC { static constexpr int value = V; };
constexpr int cmin3(int a, int b, int c) { return a < b ? (a < c ? a : c) : (b < c ? b : c); }
template <int N, int I = 0, class F> DI void static_for(F& f) {
  if constexpr (I < N) {
    f(IC<I>{});
    static_for<N, I + 1>(f);
  }
}

DI int fragaddr(int m, int k, int K32) {
  return ((m >> 4) * K32 + (k >> 5)) * 512 + ((m & 15) + 16 * ((k & 31) >> 3)) * 8 + (k & 7);
}

DI int bimg(int k, int cc) {
  return k * 128 + ((((cc >> 2) & 1) ^ ((k >> 1) & 1)) << 6) + (((cc & 3) ^ (((k >> 3) & 1) << 1)) << 4);
}

#define PF4 0
#define SPB5 2
#define SPB6 2
#define SPB3 2
template <int BM, int BN, int WM, int WK, int WN, int KT, int NST>
struct GemmCfg {
  static constexpr int NW = WM * WN * WK;
  static constexpr int NL = 4;
  static constexpr int PA = BM / 8, PB = BN / 8, P = PA + PB;
  static constexpr int PPL = (P + NL - 1) / NL;
  static constexpr int STAGE = (BM + BN) * 128;
  static constexpr int MF = BM / WM / 16;
  static constexpr int NFRAG = BN / 16;
  static constexpr int NFW = (NFRAG + WN - 1) / WN;
  static constexpr int LDS = STAGE * NST;
  static constexpr bool RESIDENT = NST >= KT;
  static constexpr int SPB = (KT == 32) ? SPB5 : (KT == 14) ? SPB6 : (BN == 64) ? SPB3 : 1;
  static constexpr int KS = KT / SPB;
  static constexpr int PRE = RESIDENT ? KS : NST / SPB - 1;
  static_assert(KT % SPB == 0 && NST % SPB == 0, "");
};

struct NoHook {
  DI void operator()(const char*) const {}
};

template <int BM, int BN, int WM, int WK, int WN, int KT, int NST, bool AFRAG, bool BFRAG, int PW, class Hook>
DI void gemm_loader(char* smem, const bf16_t* __restrict__ A, int lda, const bf16_t* __restrict__ B, int ldb,
                    int pstride, int lw, int rot, Hook& hook) {
  using C = GemmCfg<BM, BN, WM, WK, WN, KT, NST>;
  constexpr int NL = C::NL, PA = C::PA, STAGE = C::STAGE, PRE = C::PRE;
  static_assert(PA % NL == 0, "A pieces must be a multiple of the loader count");
  static_assert(BFRAG || (BN % 64) == 0, "row-major B needs 64-column panels");
  const int lane = vtid() & 63;
  unsigned off[PW];
#pragma unroll
  for (int i = 0; i < PW; ++i) {
    if (i < PA / NL) {
      const int p = lw + i * NL;
      if constexpr (AFRAG) {
        off[i] = (unsigned)(((p >> 1) * lda + (p & 1)) * 1024 + lane * 16);
      } else {
        off[i] = (unsigned)((8 * p + (lane >> 3)) * lda * 2 + (((lane & 7) ^ (lane >> 3)) << 4));
      }
    } else {
      const int p = lw + i * NL - PA;
      if constexpr (BFRAG) {
        off[i] = (unsigned)(((p >> 1) * ldb + (p & 1)) * 1024 + lane * 16);
      } else {
        int k = (p & 7) * 8 + (lane >> 3);
        int cc = (lane & 7) ^ (((lane >> 4) & 1) << 2) ^ ((p & 1) << 1);
        off[i] = (unsigned)(k * ldb * 2 + (p >> 3) * pstride + cc * 16);
      }
    }
  }
  rot = 0;
  int kr = __builtin_amdgcn_readfirstlane(rot), kw = 0;
  auto issue = [&](int slot) {
    char* base = smem + slot * STAGE + lw * 1024;
    const int kk = kw + kr;
    const char* pa = (const char*)A + kk * (AFRAG ? 2048 : 128);
    const char* pb = (const char*)B + (size_t)kk * (BFRAG ? 2048 : 128 * ldb);
#pragma unroll
    for (int i = 0; i < PW; ++i) {
      unsigned o = off[i];
      asm volatile("" : "+v"(o));
      glds16((i < PA / NL ? pa : pb) + o, base + i * NL * 1024);
    }
    if constexpr (KT % 8 == 0) {
      kr = (kr + 1) & 7;
      if (kr == rot) kw += 8;
    } else {
      kr = (kr + 1 == KT) ? 0 : kr + 1;
    }
  };
  constexpr int SPB = C::SPB, KS = C::KS;
  auto body = [&](auto KTC) {
    constexpr int kt = decltype(KTC)::value;
    constexpr int I0 = (kt == 0) ? 0 : cmin3(KS, kt - 1 + PRE, 2 * kt);
    constexpr int I1 = cmin3(KS, kt + PRE, 2 * kt + 2);
#pragma unroll
    for (int u = I0 * SPB; u < I1 * SPB; ++u) issue(u % NST);
    wait_vm<(I1 - kt - 1) * PW * SPB>();
    __builtin_amdgcn_s_barrier();
    asm volatile("" ::: "memory");
#pragma unroll
    for (int u = 0; u < SPB; ++u) hook(smem + ((kt * SPB + u) % NST) * STAGE);
    __builtin_amdgcn_s_waitcnt(0xC07F);
  };
  static_for<KS>(body);
}

template <int BM, int BN, int WM, int WK, int WN, int KT, int NST, bool AFRAG, bool BFRAG, bool CS, bool FULLN,
          bool SPLITN, int PLAIN>
DI void gemm_compute(char* smem, int wave,
                     f32x4 (&acc)[GemmCfg<BM, BN, WM, WK, WN, KT, NST>::MF][GemmCfg<BM, BN, WM, WK, WN, KT, NST>::NFW],
                     f32x4 (&cs)[GemmCfg<BM, BN, WM, WK, WN, KT, NST>::NFW]) {
  using C = GemmCfg<BM, BN, WM, WK, WN, KT, NST>;
  constexpr int STAGE = C::STAGE, MF = C::MF, NFW = C::NFW;
  constexpr int NF = FULLN ? NFW : NFW - 1;
  const int lane = vtid() & 63;
  const int wr = wave % WM, wk = (wave / WM) % WK, wc = wave / (WM * WK);
  const int g = lane >> 4, q = (lane & 15) >> 2, pp = lane & 3;
  constexpr int NKS = (WK == 2) ? 1 : 2;
  int aoff[NKS];
#pragma unroll
  for (int i = 0; i < NKS; ++i) {
    const int ks = (WK == 2) ? wk : i;
    if constexpr (AFRAG) aoff[i] = (wr * MF * 2 + ks) * 1024 + lane * 16;
    else aoff[i] = (wr * (MF * 16) + (lane & 15)) * 128 + (((4 * ks + g) ^ (lane & 7)) << 4);
  }
  int boff[NF];
#pragma unroll
  for (int nf = 0; nf < NF; ++nf) {
    const int f = SPLITN ? ((nf >> 1) * 4 + 2 * wc + (nf & 1)) : (wc * NFW + nf);
    if constexpr (BFRAG) {
      boff[nf] = BM * 128 + (f * 2 + ((WK == 2) ? wk : 0)) * 1024 + lane * 16;
    } else {
      const int kk = 8 * g + q + ((WK == 2) ? wk * 32 : 0);
      boff[nf] = BM * 128 + (f >> 2) * 8192 + bimg(kk, 2 * (f & 3) + (pp >> 1)) + 8 * (pp & 1);
    }
  }
  bf16x8 ones;
#pragma unroll
  for (int j = 0; j < 8; ++j) ones[j] = (__bf16)1.0f;

  bf16x8 af[2][MF], bfr[2][NF];
  auto step = [&](auto JC) {
    constexpr int j = decltype(JC)::value;
    constexpr int NSTEP = KT * NKS;
    if constexpr (j < NSTEP) {
      constexpr int kt = j / NKS, i = j % NKS;
      if constexpr (i == 0 && kt % C::SPB == 0) {
        __builtin_amdgcn_s_barrier();
        asm volatile("" ::: "memory");
      }
      const char* sb = smem + (kt % NST) * STAGE;
#pragma unroll
      for (int mf = 0; mf < MF; ++mf) af[j & 1][mf] = *(const bf16x8*)(sb + aoff[i] + mf * 2048);
#pragma unroll
      for (int nf = 0; nf < NF; ++nf) {
        if constexpr (BFRAG) {
          bfr[j & 1][nf] = *(const bf16x8*)(sb + boff[nf] + i * 1024);
        } else {
          s16x4 lo = tr_read(sb + boff[nf] + i * 4096);
          s16x4 hi = tr_read(sb + boff[nf] + i * 4096 + 512);
          s16x8 v = __builtin_shufflevector(lo, hi, 0, 1, 2, 3, 4, 5, 6, 7);
          bfr[j & 1][nf] = __builtin_bit_cast(bf16x8, v);
        }
      }
    }
    if constexpr (j > 0) {
      constexpr int jp = j - 1;
#pragma unroll
      for (int nf = 0; nf < NF; ++nf) {
#pragma unroll
        for (int mf = 0; mf < MF; ++mf) {
          if ((PLAIN >> nf) & 1)
            acc[mf][nf] = __builtin_amdgcn_mfma_f32_16x16x32_bf16(af[jp & 1][mf], bfr[jp & 1][nf], acc[mf][nf], 0, 0, 0);
          else
            acc[mf][nf] = __builtin_amdgcn_mfma_f32_16x16x32_bf16(bfr[jp & 1][nf], af[jp & 1][mf], acc[mf][nf], 0, 0, 0);
        }
      }
      if constexpr (j < NSTEP) {
        constexpr int NRD = MF + (BFRAG ? NF : 2 * NF), NMM = MF * NF;
        constexpr int RPM = (NRD + NMM - 1) / NMM;
#pragma unroll
        for (int u = 0; u < NMM; ++u) {
          __builtin_amdgcn_sched_group_barrier(0x8, 1, 0);
          __builtin_amdgcn_sched_group_barrier(0x100, RPM, 0);
        }
      }
      __builtin_amdgcn_sched_barrier(0);
      if constexpr (CS) {
        if (wr == (jp % WM)) {
#pragma unroll
          for (int nf = 0; nf < NF; ++nf) {
            if ((PLAIN >> nf) & 1) cs[nf] = __builtin_amdgcn_mfma_f32_16x16x32_bf16(ones, bfr[jp & 1][nf], cs[nf], 0, 0, 0);
            else cs[nf] = __builtin_amdgcn_mfma_f32_16x16x32_bf16(bfr[jp & 1][nf], ones, cs[nf], 0, 0, 0);
          }
        }
      }
    }
    __builtin_amdgcn_sched_barrier(0);
    __builtin_amdgcn_s_waitcnt(0xC07F);
  };
  static_for<KT * NKS + 1>(step);
}

template <int BM, int BN, int WM, int WK, int WN, int KT, int NST, bool AFRAG, bool BFRAG, bool CS, bool SPLITN,
          int PLAIN, class Hook>
DI bool gemm_main(char* smem, const bf16_t* __restrict__ A, int lda, const bf16_t* __restrict__ B, int ldb, int pstride,
                  int rot,
                  f32x4 (&acc)[GemmCfg<BM, BN, WM, WK, WN, KT, NST>::MF][GemmCfg<BM, BN, WM, WK, WN, KT, NST>::NFW],
                  f32x4 (&cs)[GemmCfg<BM, BN, WM, WK, WN, KT, NST>::NFW], Hook& hook) {
  using C = GemmCfg<BM, BN, WM, WK, WN, KT, NST>;
  const int wave = __builtin_amdgcn_readfirstlane(vtid() >> 6);
  if (wave >= C::NW) {
    const int lw = wave - C::NW;
    __builtin_amdgcn_s_setprio(3);
    constexpr bool EVEN = (C::P % C::NL) == 0;
    if (EVEN || lw < (C::P % C::NL))
      gemm_loader<BM, BN, WM, WK, WN, KT, NST, AFRAG, BFRAG, C::PPL>(smem, A, lda, B, ldb, pstride, lw, rot, hook);
    else if constexpr (!EVEN)
      gemm_loader<BM, BN, WM, WK, WN, KT, NST, AFRAG, BFRAG, C::PPL - 1>(smem, A, lda, B, ldb, pstride, lw, rot, hook);
    return false;
  }
  const int wc = wave / (WM * WK);
  constexpr bool ALLFULL = (C::NFRAG == WN * C::NFW);
  if (ALLFULL || wc != WN - 1)
    gemm_compute<BM, BN, WM, WK, WN, KT, NST, AFRAG, BFRAG, CS, true, SPLITN, PLAIN>(smem, wave, acc, cs);
  else if constexpr (!ALLFULL)
    gemm_compute<BM, BN, WM, WK, WN, KT, NST, AFRAG, BFRAG, CS, false, SPLITN, PLAIN>(smem, wave, acc, cs);
  return true;
}

template <int NFW>
DI void splitk_combine(char* smem, f32x4 (&acc)[2][NFW], f32x4 (&res)[NFW], int pairid, int wk, int lane, bool fulln) {
  __syncthreads();
  char* mine = smem + (pairid * 2 + wk) * (NFW * 1024) + lane * 16;
  char* theirs = smem + (pairid * 2 + (wk ^ 1)) * (NFW * 1024) + lane * 16;
#pragma unroll
  for (int nf = 0; nf < NFW; ++nf)
    if (nf < NFW - 1 || fulln) *(f32x4*)(mine + nf * 1024) = wk ? acc[0][nf] : acc[1][nf];
  __syncthreads();
#pragma unroll
  for (int nf = 0; nf < NFW; ++nf)
    if (nf < NFW - 1 || fulln) res[nf] = (wk ? acc[1][nf] : acc[0][nf]) + *(const f32x4*)(theirs + nf * 1024);
}

DI void chan_eq(float& mean, float& m2, float mean_b, float m2_b, float n) {
  float d = mean_b - mean;
  m2 = m2 + m2_b + d * d * (0.5f * n);
  mean = 0.5f * (mean + mean_b);
}

DI void ln_consts(float mean, float var, float& mean_b, float& rinv) {
  mean_b = rbf(mean);
  float v = rbf(var);
  float t = rbf(v + 1e-5f);
  float sd = rbf(sqrtf(t));
  rinv = 1.0f / sd;
}

DI void l2_prefetch(const char* base, int nlines, int t, int nt) {
  unsigned acc = 0u;
  for (int i = t; i < nlines; i += 4 * nt) {
    unsigned v0 = *(const unsigned*)(base + (size_t)i * 128);
    unsigned v1 = (i + nt < nlines) ? *(const unsigned*)(base + (size_t)(i + nt) * 128) : 0u;
    unsigned v2 = (i + 2 * nt < nlines) ? *(const unsigned*)(base + (size_t)(i + 2 * nt) * 128) : 0u;
    unsigned v3 = (i + 3 * nt < nlines) ? *(const unsigned*)(base + (size_t)(i + 3 * nt) * 128) : 0u;
    acc ^= v0 ^ v1 ^ v2 ^ v3;
  }
  asm volatile("" ::"v"(acc));
}

DI float row16_sum(float v) {
  v += __builtin_bit_cast(float, __builtin_amdgcn_update_dpp(0, __builtin_bit_cast(int, v), 0x128, 0xf, 0xf, false));
  v += __builtin_bit_cast(float, __builtin_amdgcn_update_dpp(0, __builtin_bit_cast(int, v), 0x124, 0xf, 0xf, false));
  v += __builtin_bit_cast(float, __builtin_amdgcn_update_dpp(0, __builtin_bit_cast(int, v), 0x122, 0xf, 0xf, false));
  v += __builtin_bit_cast(float, __builtin_amdgcn_update_dpp(0, __builtin_bit_cast(int, v), 0x121, 0xf, 0xf, false));
  return v;
}

struct StatHook {
  float s1, s2;
  int o0;
  DI void operator()(const char* sb) {
    bf16x2 one2 = {(__bf16)1.0f, (__bf16)1.0f};
    u32x4 v0 = *(const u32x4*)(sb + o0);
    u32x4 v1 = *(const u32x4*)(sb + o0 + 16);
#pragma unroll
    for (int e = 0; e < 4; ++e) {
      const unsigned ua = v0[e], ub = v1[e];
      bf16x2 a = __builtin_bit_cast(bf16x2, ua), b = __builtin_bit_cast(bf16x2, ub);
      s1 = __builtin_amdgcn_fdot2_f32_bf16(a, one2, s1, false);
      s2 = __builtin_amdgcn_fdot2_f32_bf16(a, a, s2, false);
      s1 = __builtin_amdgcn_fdot2_f32_bf16(b, one2, s1, false);
      s2 = __builtin_amdgcn_fdot2_f32_bf16(b, b, s2, false);
    }
  }
};

__global__ __launch_bounds__(768, 3) void k1_qkv(const bf16_t* __restrict__ x, const bf16_t* __restrict__ w,
                                                 bf16_t* __restrict__ qkf, bf16_t* __restrict__ v2,
                                                 u32x4* __restrict__ exz, const bf16_t* __restrict__ wo,
                                                 const bf16_t* __restrict__ win, float* __restrict__ wcs) {
  constexpr int BM = 64, BN = 192, WM = 2, WK = 1, WN = 4, KT = 8, NST = 4;
  using C = GemmCfg<BM, BN, WM, WK, WN, KT, NST>;
  __shared__ __attribute__((aligned(16))) char smem[C::LDS + 2048];
  if (vtid() < 256) exz[blockIdx.x * 256 + vtid()] = u32x4{0u, 0u, 0u, 0u};
  if (blockIdx.x == 0 && vtid() < 2) {
    const unsigned cv = vtid() ? 0u : 0x3f803f80u;
    exz[65536 + vtid()] = u32x4{cv, cv, cv, cv};
  }

  const int b = blockIdx.x, xcd = b & 7, idx = b >> 3;
  const int tm = xcd * 4 + (idx >> 3), tn = idx & 7;
  const int m0 = tm * BM, n0 = tn * BN;
  f32x4 acc[C::MF][C::NFW], cs[C::NFW];
#pragma unroll
  for (int i = 0; i < C::MF; ++i)
#pragma unroll
    for (int j = 0; j < C::NFW; ++j) acc[i][j] = f32x4{0.f, 0.f, 0.f, 0.f};
#pragma unroll
  for (int j = 0; j < C::NFW; ++j) cs[j] = f32x4{0.f, 0.f, 0.f, 0.f};
  StatHook hook;
  const int srow = (vtid() >> 2) & 63, sq = vtid() & 3;
  hook.s1 = 0.f; hook.s2 = 0.f;
  hook.o0 = srow * 128 + (sq << 5);
  const int lane = vtid() & 63, wave = __builtin_amdgcn_readfirstlane(vtid() >> 6), wr = wave & 1,
            wc = (wave >> 1) & 3;
  const int nw0 = n0 + wc * 48;
  const int plain = (nw0 >= 1024) ? 7 : ((nw0 + 16 >= 1024) ? 6 : 0);
  const bool comp = wave < 8;
  if (!comp) {
    __builtin_amdgcn_s_setprio(3);
    gemm_loader<BM, BN, WM, WK, WN, KT, NST, false, false, C::PPL>(smem, x + (size_t)m0 * D_MODEL, D_MODEL, w + n0,
                                                                   3 * D_MODEL, 128, wave - 8, 0, hook);
  } else if (plain == 7) {
    gemm_compute<BM, BN, WM, WK, WN, KT, NST, false, false, true, true, false, 7>(smem, wave, acc, cs);
  } else if (plain == 6) {
    gemm_compute<BM, BN, WM, WK, WN, KT, NST, false, false, true, true, false, 6>(smem, wave, acc, cs);
  } else {
    gemm_compute<BM, BN, WM, WK, WN, KT, NST, false, false, true, true, false, 0>(smem, wave, acc, cs);
  }

  float* tmean = (float*)(smem + C::LDS);
  float* trinv = tmean + 64;
  float* tcs = trinv + 64;
  if (!comp) {
    float s1 = hook.s1 + __shfl_xor(hook.s1, 1);
    float s2 = hook.s2 + __shfl_xor(hook.s2, 1);
    s1 += __shfl_xor(s1, 2);
    s2 += __shfl_xor(s2, 2);
    const float mean = s1 * (1.0f / 512.0f), ex2 = s2 * (1.0f / 512.0f);
    float var = ex2 - mean * mean;
    if (var < ex2 * (1.0f / 1024.0f)) {
      const bf16_t* xr = x + (size_t)(m0 + srow) * D_MODEL + sq * 128;
      float a2 = 0.f;
      for (int j = 0; j < 16; ++j) {
        u32x4 v = *(const u32x4*)(xr + j * 8);
#pragma unroll
        for (int e = 0; e < 4; ++e) {
          float d0 = lo_f(v[e]) - mean, d1 = hi_f(v[e]) - mean;
          a2 = fmaf(d0, d0, a2);
          a2 = fmaf(d1, d1, a2);
        }
      }
      a2 += __shfl_xor(a2, 1);
      a2 += __shfl_xor(a2, 2);
      var = a2 * (1.0f / 512.0f);
    }
    float mb, ri;
    ln_consts(mean, var, mb, ri);
    if (sq == 0) { tmean[srow] = mb; trinv[srow] = ri; }
    __syncthreads();
    {
      const int lt = vtid() - 512;
      if (lt < 72) {
        const char* p0 = (lt < 16) ? (const char*)wo + (size_t)(b * 16 + lt) * 128
                                   : (const char*)win + (size_t)(b * 56 + lt - 16) * 128;
        unsigned v0 = *(const unsigned*)p0;
        asm volatile("" ::"v"(v0));
      }
      if (b < 28) {
        const bf16_t* wp = win + (size_t)((lt >> 3) * 16) * (2 * FH) + b * 64 + (lt & 7) * 8;
        float a[8];
#pragma unroll
        for (int e = 0; e < 8; ++e) a[e] = 0.f;
#pragma unroll
        for (int i = 0; i < 16; ++i) {
          const u32x4 v = *(const u32x4*)(wp + (size_t)i * (2 * FH));
#pragma unroll
          for (int e = 0; e < 4; ++e) { a[2 * e] += lo_f(v[e]); a[2 * e + 1] += hi_f(v[e]); }
        }
#pragma unroll
        for (int e = 0; e < 8; ++e) {
          a[e] += __shfl_xor(a[e], 8);
          a[e] += __shfl_xor(a[e], 16);
          a[e] += __shfl_xor(a[e], 32);
        }
        if ((lt & 63) < 8) {
          float* dp = wcs + (lt >> 6) * (2 * FH) + b * 64 + (lt & 7) * 8;
          *(f32x4*)dp = f32x4{a[0], a[1], a[2], a[3]};
          *(f32x4*)(dp + 4) = f32x4{a[4], a[5], a[6], a[7]};
        }
      }
    }
    return;
  }
  const int g = lane >> 4;
#pragma unroll
  for (int nf = 0; nf < C::NFW; ++nf) {
    if ((plain >> nf) & 1) {
      if (lane < 16) tcs[wr * 192 + wc * 48 + nf * 16 + lane] = cs[nf][0];
    } else if ((lane & 15) == 0) {
      *(f32x4*)(tcs + wr * 192 + wc * 48 + nf * 16 + 4 * g) = cs[nf];
    }
  }
  __syncthreads();
#pragma unroll
  for (int nf = 0; nf < C::NFW; ++nf) {
    if ((plain >> nf) & 1) {
      const int nl = wc * 48 + nf * 16 + (lane & 15);
      const float c = tcs[nl] + tcs[192 + nl];
      const int nv = n0 + nl - 1024;
#pragma unroll
      for (int mf = 0; mf < C::MF; ++mf) {
        const int ml = wr * 32 + mf * 16 + 4 * g;
        const f32x4 mb = *(const f32x4*)(tmean + ml), ri = *(const f32x4*)(trinv + ml);
        u32x2 o = {pack2((acc[mf][nf][0] - mb[0] * c) * ri[0], (acc[mf][nf][1] - mb[1] * c) * ri[1]),
                   pack2((acc[mf][nf][2] - mb[2] * c) * ri[2], (acc[mf][nf][3] - mb[3] * c) * ri[3])};
        const int m = m0 + ml;
        ST2(v2 + (((size_t)(nv >> 4) * 256 + (m >> 3)) * 16 + (nv & 15)) * 8 + (m & 7), o);
      }
    } else {
      const int nl = wc * 48 + nf * 16 + 4 * g;
      f32x4 c = *(const f32x4*)(tcs + nl) + *(const f32x4*)(tcs + 192 + nl);
      const int n = n0 + nl;
#pragma unroll
      for (int mf = 0; mf < C::MF; ++mf) {
        const int ml = wr * 32 + mf * 16 + (lane & 15);
        const float mb = tmean[ml], ri = trinv[ml];
        u32x2 o = {pack2((acc[mf][nf][0] - mb * c[0]) * ri, (acc[mf][nf][1] - mb * c[1]) * ri),
                   pack2((acc[mf][nf][2] - mb * c[2]) * ri, (acc[mf][nf][3] - mb * c[3]) * ri)};
        ST2(qkf + ((size_t)(n >> 3) * S_LEN + m0 + ml) * 8 + (n & 7), o);
      }
    }
  }
}

template <bool EDGE>
DI void attn_body(const bf16_t* __restrict__ qkf, const bf16_t* __restrict__ v2, const bf16_t* __restrict__ cst,
                  bf16_t* __restrict__ o, int qt, int h) {
  const int lane = threadIdx.x & 63;
  const int r = lane & 31, hf = lane >> 5;
  const int q0 = qt * 32;
  const int t0 = EDGE ? 8 - qt : 0;

  const bf16x8 qf = *(const bf16x8*)(qkf + ((size_t)(h * 2 + hf) * S_LEN + q0 + r) * 8);
  const int pr = 16 * ((r >> 2) & 1) + 4 * (r >> 3) + (r & 3);
  const bf16_t* kp = qkf + ((size_t)(64 + h * 2 + hf) * S_LEN + pr) * 8;
  bf16x8 kf[9];
#pragma unroll
  for (int t = 0; t < 9; ++t) {
    int kb = q0 - 256 + 32 * t;
    if (EDGE && kb < 0) kb = 0;
    kf[t] = *(const bf16x8*)(kp + kb * 8);
  }
  bf16x8 vf[9][2];
  const bf16_t* vp = (r < 16) ? v2 + ((size_t)h * 256 * 16 + 2 * hf * 16 + r) * 8 : cst + ((r == 16) ? 0 : 8);
  const int vm = (r < 16) ? 16 : 0;
#pragma unroll
  for (int t = 0; t < 9; ++t) {
    int kb = q0 - 256 + 32 * t;
    if (EDGE && kb < 0) kb = 0;
    vf[t][0] = *(const bf16x8*)(vp + kb * vm);
    vf[t][1] = *(const bf16x8*)(vp + (kb + 8) * vm);
  }
  f32x16 s[9];
  f32x16 zero;
#pragma unroll
  for (int i = 0; i < 16; ++i) zero[i] = 0.f;
#pragma unroll
  for (int t = 0; t < 9; ++t) s[t] = __builtin_amdgcn_mfma_f32_32x32x16_bf16(kf[t], qf, zero, 0, 0, 0);

  const float NEG = -3.0e38f;
#pragma unroll
  for (int i = 0; i < 16; ++i) {
    if (!(16 * hf + i > r)) s[0][i] = NEG;
    if (!(16 * hf + i <= r)) s[8][i] = NEG;
  }
  if constexpr (EDGE) {
#pragma unroll
    for (int t = 0; t < 8; ++t) {
      if (t < t0) {
#pragma unroll
        for (int i = 0; i < 16; ++i) s[t][i] = NEG;
      }
    }
  }
  float mx = NEG;
#pragma unroll
  for (int t = 0; t < 9; ++t)
#pragma unroll
    for (int i = 0; i < 16; ++i) mx = fmaxf(mx, s[t][i]);
  mx = fmaxf(mx, __shfl_xor(mx, 32));
  const float c = 0.25f * 1.4426950408889634f;
  const f32x2 c2 = {c, c}, mc2 = {-mx * c, -mx * c};
  f32x16 oacc = zero;
#pragma unroll
  for (int t = 0; t < 9; ++t) {
#pragma unroll
    for (int st = 0; st < 2; ++st) {
      bf16x8 pf;
#pragma unroll
      for (int j = 0; j < 8; j += 2) {
        f32x2 e = {s[t][8 * st + j], s[t][8 * st + j + 1]};
        e = __builtin_elementwise_fma(e, c2, mc2);
        pf[j] = (__bf16)__builtin_amdgcn_exp2f(e[0]);
        pf[j + 1] = (__bf16)__builtin_amdgcn_exp2f(e[1]);
      }
      oacc = __builtin_amdgcn_mfma_f32_32x32x16_bf16(vf[t][st], pf, oacc, 0, 0, 0);
    }
  }
  const float l = __shfl(oacc[8], r);
  const float il = 1.0f / l;
  bf16_t* op = o + fragaddr(q0 + r, h * 16 + 4 * hf, 16);
  u32x2 o0 = {pack2(oacc[0] * il, oacc[1] * il), pack2(oacc[2] * il, oacc[3] * il)};
  u32x2 o1 = {pack2(oacc[4] * il, oacc[5] * il), pack2(oacc[6] * il, oacc[7] * il)};
  ST2(op, o0);
  ST2(op + 128, o1);
}

__global__ __launch_bounds__(512) void k2_attn(const bf16_t* __restrict__ qkf, const bf16_t* __restrict__ v2,
                                               const bf16_t* __restrict__ cst, bf16_t* __restrict__ o) {
  const int b = blockIdx.x, xcd = b & 7, idx = b >> 3;
  const int qt = xcd * 8 + (idx >> 2), hg = idx & 3;
  const int h = hg * 8 + (threadIdx.x >> 6);
  if (qt < 8) attn_body<true>(qkf, v2, cst, o, qt, h);
  else attn_body<false>(qkf, v2, cst, o, qt, h);
}

__global__ __launch_bounds__(768, 3) void k3_wo(const bf16_t* __restrict__ o, const bf16_t* __restrict__ w,
                                                const bf16_t* __restrict__ x, bf16_t* __restrict__ h,
                                                f32x2* __restrict__ stats, const bf16_t* __restrict__ wsp,
                                                const bf16_t* __restrict__ wout) {
  constexpr int BM = 64, BN = 64, WM = 2, WK = 2, WN = 2, KT = 8, NST = 8;
  using C = GemmCfg<BM, BN, WM, WK, WN, KT, NST>;
  __shared__ __attribute__((aligned(16))) char smem[C::LDS + 8192];
  const int b = blockIdx.x, xcd = b & 7, idx = b >> 3;
  const int tm = xcd * 4 + (idx >> 3), tn = idx & 7;
  const int m0 = tm * BM, n0 = tn * BN;
  const int lane = vtid() & 63, wave = vtid() >> 6, wr = wave & 1, wk = (wave >> 1) & 1, wc = (wave >> 2) & 1;
  char* xs = smem + C::LDS;
  if (wave >= 8) {
    const int lw = __builtin_amdgcn_readfirstlane(wave) - 8;
#pragma unroll
    for (int i = 0; i < 2; ++i) {
      const int p = lw + 4 * i;
      glds16((const char*)(x + (size_t)(m0 + 8 * p + (lane >> 3)) * D_MODEL + n0) + (((lane & 7) ^ (lane >> 3)) << 4),
             xs + p * 1024);
    }
  }
  f32x4 acc[C::MF][C::NFW], cs[C::NFW], r[C::NFW];
#pragma unroll
  for (int i = 0; i < C::MF; ++i)
#pragma unroll
    for (int j = 0; j < C::NFW; ++j) acc[i][j] = f32x4{0.f, 0.f, 0.f, 0.f};
  NoHook hook;
  if (!gemm_main<BM, BN, WM, WK, WN, KT, NST, true, false, false, false, 0>(
          smem, o + (size_t)m0 * D_MODEL, 16, w + n0, D_MODEL, 128, (2 * (idx >> 3) + (idx & 7)) & 7, acc, cs, hook)) {
    const int lt = vtid() - 512;
    unsigned v0 = *(const unsigned*)((const char*)wsp + (size_t)(b * 256 + lt) * 128);
    unsigned v1 = (lt < 28) ? *(const unsigned*)((const char*)wout + (size_t)(b * 28 + lt) * 128) : 0u;
    asm volatile("" ::"v"(v0 ^ v1));
    return;
  }
  splitk_combine<2>(smem, acc, r, wr + 2 * wc, wk, lane, true);
  const int ml = wr * 32 + wk * 16 + (lane & 15);
  const int nl = wc * 32 + 4 * (lane >> 4);
  const u32x2 rv0 = *(const u32x2*)(xs + ml * 128 + (((nl >> 3) ^ (ml & 7)) << 4) + (nl & 4) * 2);
  const u32x2 rv1 = *(const u32x2*)(xs + ml * 128 + ((((nl + 16) >> 3) ^ (ml & 7)) << 4) + (nl & 4) * 2);
  float hv[8];
  hv[0] = rbf(lo_f(rv0[0]) + rbf(r[0][0])); hv[1] = rbf(hi_f(rv0[0]) + rbf(r[0][1]));
  hv[2] = rbf(lo_f(rv0[1]) + rbf(r[0][2])); hv[3] = rbf(hi_f(rv0[1]) + rbf(r[0][3]));
  hv[4] = rbf(lo_f(rv1[0]) + rbf(r[1][0])); hv[5] = rbf(hi_f(rv1[0]) + rbf(r[1][1]));
  hv[6] = rbf(lo_f(rv1[1]) + rbf(r[1][2])); hv[7] = rbf(hi_f(rv1[1]) + rbf(r[1][3]));
  u32x2 o0 = {pack2(hv[0], hv[1]), pack2(hv[2], hv[3])};
  u32x2 o1 = {pack2(hv[4], hv[5]), pack2(hv[6], hv[7])};
  bf16_t* hp = h + fragaddr(m0 + ml, n0 + nl, 16);
  ST2(hp, o0);
  ST2(hp + 256, o1);
  float mean = 0.f;
#pragma unroll
  for (int j = 0; j < 8; ++j) mean += hv[j];
  mean *= 0.125f;
  float m2 = 0.f;
#pragma unroll
  for (int j = 0; j < 8; ++j) { float d = hv[j] - mean; m2 = fmaf(d, d, m2); }
  chan_eq(mean, m2, __shfl_xor(mean, 16), __shfl_xor(m2, 16), 8.f);
  chan_eq(mean, m2, __shfl_xor(mean, 32), __shfl_xor(m2, 32), 16.f);
  if (lane < 16) stats[(size_t)(tn * 2 + wc) * S_LEN + m0 + ml] = f32x2{mean, m2};
}

DI float gelu_f(float x) {
  const float k0 = 0.7978845608028654f * 2.0f * 1.4426950408889634f;
  float y = k0 * fmaf(0.044715f * x * x, x, x);
  float e = __builtin_amdgcn_exp2f(-y);
  return x * __builtin_amdgcn_rcpf(1.0f + e);
}

__global__ __launch_bounds__(768, 3) void k4_in(const bf16_t* __restrict__ h, const bf16_t* __restrict__ w,
                                                const f32x2* __restrict__ hstats, bf16_t* __restrict__ z1c,
                                                bf16_t* __restrict__ z2f, u64* __restrict__ ex,
                                                const float* __restrict__ wcs) {
  constexpr int BM = 128, BN = 128, WM = 4, WK = 1, WN = 2, KT = 8, NST = 4;
  using C = GemmCfg<BM, BN, WM, WK, WN, KT, NST>;
  __shared__ __attribute__((aligned(16))) char smem[C::LDS + 8192];
  const int b = blockIdx.x, xcd = b & 7, idx = b >> 3;
  const int tm = xcd * 2 + (idx >= 14 ? 1 : 0), tn = (idx >= 14) ? idx - 14 : idx;
  const int m0 = tm * BM, n0 = tn * 64;
  const int lane = vtid() & 63, wave = vtid() >> 6, wr = wave & 3, wc = (wave >> 2) & 1;
  const int g = lane >> 4;
  float* tmean = (float*)(smem + C::LDS);
  float* trinv = tmean + 128;
  float* tcs = trinv + 128;
  float* tmean2 = tcs + 512;
  float* trinv2 = tmean2 + 128;
  float* tpart = trinv2 + 128;
  f32x2* tpart2 = (f32x2*)(tpart + 512);
  f32x4 acc[C::MF][C::NFW], cs[C::NFW];
#pragma unroll
  for (int i = 0; i < C::MF; ++i)
#pragma unroll
    for (int j = 0; j < C::NFW; ++j) acc[i][j] = f32x4{0.f, 0.f, 0.f, 0.f};
#pragma unroll
  for (int j = 0; j < C::NFW; ++j) cs[j] = f32x4{0.f, 0.f, 0.f, 0.f};
  f32x2 hv[8];
  float cv[4];
  if (vtid() >= 512) {
    const f32x2* hp = hstats + (size_t)((vtid() >> 7) & 1) * 8 * S_LEN + m0 + (vtid() & 127);
#pragma unroll
    for (int j = 0; j < 8; ++j) hv[j] = hp[(size_t)j * S_LEN];
    const float* cp = wcs + n0 + (vtid() & 63) + ((vtid() >> 6) & 1) * FH;
#pragma unroll
    for (int j = 0; j < 4; ++j) cv[j] = cp[j * 2 * FH];
  }
  NoHook hook;
  const bool comp = gemm_main<BM, BN, WM, WK, WN, KT, NST, true, false, false, true, 12>(
      smem, h + (size_t)m0 * D_MODEL, 16, w + n0, 2 * FH, FH * 2, ((idx >= 14 ? 4 : 0) + tn) & 7, acc, cs, hook);
  if (!comp) {
    const int lt = vtid() - 512;
    float mean = 0.f;
#pragma unroll
    for (int j = 0; j < 8; ++j) mean += hv[j][0];
    mean *= 0.125f;
    float m2 = 0.f;
#pragma unroll
    for (int j = 0; j < 8; ++j) { float d = hv[j][0] - mean; m2 += hv[j][1] + 32.f * d * d; }
    if (lt >= 128) tpart2[lt - 128] = f32x2{mean, m2};
    else tcs[lt] = (cv[0] + cv[1]) + (cv[2] + cv[3]);
    __syncthreads();
    if (lt < 128) {
      const f32x2 o = tpart2[lt];
      chan_eq(mean, m2, o[0], o[1], 256.f);
      float mb, ri;
      ln_consts(mean, m2 * (1.0f / 512.0f), mb, ri);
      tmean[lt] = mb;
      trinv[lt] = ri;
    }
    __syncthreads();
    return;
  }
  __syncthreads();
  __syncthreads();
  u64* exp_ = ex + (size_t)tm * (14 * 128);
  {
    float c2[2];
#pragma unroll
    for (int nf = 0; nf < 2; ++nf) {
      const int nl = 64 + wc * 32 + nf * 16 + (lane & 15);
      c2[nf] = tcs[nl];
    }
#pragma unroll
    for (int mf = 0; mf < C::MF; ++mf) {
      const int ml = wr * 32 + mf * 16 + 4 * g;
      const f32x4 mb = *(const f32x4*)(tmean + ml), ri = *(const f32x4*)(trinv + ml);
      float pm = 0.f, pq = 0.f;
#pragma unroll
      for (int r = 0; r < 4; ++r) {
        const float a = rbf(gelu_f(rbf((acc[mf][2][r] - mb[r] * c2[0]) * ri[r])));
        const float bq = rbf(gelu_f(rbf((acc[mf][3][r] - mb[r] * c2[1]) * ri[r])));
        acc[mf][2][r] = a;
        acc[mf][3][r] = bq;
        const float mean = row16_sum(a + bq) * (1.0f / 32.0f);
        const float da = a - mean, db = bq - mean;
        const float m2 = row16_sum(fmaf(da, da, db * db));
        if ((lane & 3) == r) { pm = mean; pq = m2; }
      }
      if ((lane & 15) < 4) ((f32x2*)tpart)[wc * 128 + ml + (lane & 3)] = f32x2{pm, pq};
    }
  }
  __syncthreads();
  if (vtid() < 128) {
    f32x2 a = ((const f32x2*)tpart)[vtid()], bq = ((const f32x2*)tpart)[128 + vtid()];
    float mean = a[0], m2 = a[1];
    chan_eq(mean, m2, bq[0], bq[1], 32.f);
    __hip_atomic_store(exp_ + tn * 128 + vtid(),
                       ((u64)(__float_as_uint(m2) | 1u) << 32) | (u64)__float_as_uint(mean), __ATOMIC_RELAXED,
                       __HIP_MEMORY_SCOPE_AGENT);
  }
#pragma unroll
  for (int nf = 0; nf < 2; ++nf) {
    const int nl = wc * 32 + nf * 16 + 4 * g;
    const f32x4 c = *(const f32x4*)(tcs + nl);
#pragma unroll
    for (int mf = 0; mf < C::MF; ++mf) {
      const int ml = wr * 32 + mf * 16 + (lane & 15);
      const float mb = tmean[ml], ri = trinv[ml];
      float v[4];
#pragma unroll
      for (int r = 0; r < 4; ++r) v[r] = gelu_f(rbf((acc[mf][nf][r] - mb * c[r]) * ri));
      u32x2 ov = {pack2(v[0], v[1]), pack2(v[2], v[3])};
      ST2(z1c + ((size_t)((m0 >> 4) + wr * 2 + mf) * 56 + (n0 >> 4) + wc * 2 + nf) * 256 + lane * 4, ov);
    }
  }
  if (vtid() < 256) {
    const int t = vtid(), row = t & 127, half = t >> 7;
    const u64* gp = exp_ + half * (7 * 128) + row;
    float vm[7], vq[7];
    for (unsigned spins = 0;; ++spins) {
      bool ok = true;
#pragma unroll
      for (int j = 0; j < 7; ++j) {
        u64 gg = __hip_atomic_load(gp + j * 128, __ATOMIC_RELAXED, __HIP_MEMORY_SCOPE_AGENT);
        vm[j] = __uint_as_float((unsigned)gg);
        vq[j] = __uint_as_float((unsigned)(gg >> 32));
        ok &= ((unsigned)(gg >> 32) != 0u);
      }
      if (__all(ok)) break;
      if (spins > 4000000u) break;
      __builtin_amdgcn_s_sleep(1);
    }
    float hm = 0.f, hq = 0.f;
#pragma unroll
    for (int j = 0; j < 7; ++j) { hm += vm[j]; hq += vq[j]; }
    hm *= (1.0f / 7.0f);
#pragma unroll
    for (int j = 0; j < 7; ++j) { float d = vm[j] - hm; hq = fmaf(64.f * d, d, hq); }
    tpart2[t] = f32x2{hm, hq};
  }
  __syncthreads();
  if (vtid() < 128) {
    f32x2 a = tpart2[vtid()], bq = tpart2[128 + vtid()];
    float mean = a[0], m2 = a[1];
    chan_eq(mean, m2, bq[0], bq[1], 448.f);
    float mb, ri;
    ln_consts(mean, m2 * (1.0f / 896.0f), mb, ri);
    tmean2[vtid()] = mb;
    trinv2[vtid()] = ri;
  }
  __syncthreads();
#pragma unroll
  for (int mf = 0; mf < C::MF; ++mf) {
    const int ml = wr * 32 + mf * 16 + 4 * g;
    const f32x4 mb = *(const f32x4*)(tmean2 + ml), ri = *(const f32x4*)(trinv2 + ml);
#pragma unroll
    for (int nf = 2; nf < 4; ++nf) {
      const int n = n0 + wc * 32 + (nf & 1) * 16 + (lane & 15);
      float v0 = rbf(acc[mf][nf][0] - mb[0]) * ri[0], v1 = rbf(acc[mf][nf][1] - mb[1]) * ri[1];
      float v2 = rbf(acc[mf][nf][2] - mb[2]) * ri[2], v3 = rbf(acc[mf][nf][3] - mb[3]) * ri[3];
      u32x2 ov = {pack2(v0, v1), pack2(v2, v3)};
      ST2(z2f + fragaddr(n, m0 + ml, 64), ov);
    }
  }
}

__global__ __launch_bounds__(768, 3) void k5_sp(const bf16_t* __restrict__ wsp, const bf16_t* __restrict__ z2f,
                                                const bf16_t* __restrict__ bsp, const bf16_t* __restrict__ z1c,
                                                bf16_t* __restrict__ y) {
  constexpr int BM = 64, BN = 112, WM = 2, WK = 2, WN = 2, KT = 32, NST = (SPB5 == 2) ? 6 : 7;
  using C = GemmCfg<BM, BN, WM, WK, WN, KT, NST>;
  __shared__ __attribute__((aligned(16))) char smem[C::LDS];
  const int b = blockIdx.x, xcd = b & 7, idx = b >> 3;
  const int tm = (xcd >> 2) * 16 + (idx >> 1), tn = (xcd & 3) * 2 + (idx & 1);
  const int m0 = tm * BM, n0 = tn * BN;
  const int lane = vtid() & 63, wave = vtid() >> 6, wr = wave & 1, wk = (wave >> 1) & 1, wc = (wave >> 2) & 1;
  const bool fulln = (wc == 0);
  const int m = m0 + wr * 32 + wk * 16 + (lane & 15);
  const int nb = n0 + wc * 64;
  float bb = 0.f;
  u32x2 zv[4];
  if (wave < 8) {
    bb = (float)bsp[m];
#pragma unroll
    for (int nf = 0; nf < 4; ++nf)
      if (nf < 3 || fulln) zv[nf] = *(const u32x2*)(z1c + ((size_t)(m >> 4) * 56 + (nb >> 4) + nf) * 256 + lane * 4);
  }
  f32x4 acc[C::MF][C::NFW], cs[C::NFW], r[C::NFW];
#pragma unroll
  for (int i = 0; i < C::MF; ++i)
#pragma unroll
    for (int j = 0; j < C::NFW; ++j) acc[i][j] = f32x4{0.f, 0.f, 0.f, 0.f};
  NoHook hook;
  if (!gemm_main<BM, BN, WM, WK, WN, KT, NST, false, true, false, false, 0>(
          smem, wsp + (size_t)m0 * S_LEN, S_LEN, z2f + (size_t)(n0 >> 4) * 64 * 512, 64, 0,
          (idx >> 1) & 7, acc, cs, hook))
    return;
  splitk_combine<4>(smem, acc, r, wr + 2 * wc, wk, lane, fulln);
#pragma unroll
  for (int nf = 0; nf < C::NFW; ++nf) {
    if (nf < 3 || fulln) {
      float v0 = lo_f(zv[nf][0]) * rbf(rbf(r[nf][0]) + bb);
      float v1 = hi_f(zv[nf][0]) * rbf(rbf(r[nf][1]) + bb);
      float v2 = lo_f(zv[nf][1]) * rbf(rbf(r[nf][2]) + bb);
      float v3 = hi_f(zv[nf][1]) * rbf(rbf(r[nf][3]) + bb);
      u32x2 ov = {pack2(v0, v1), pack2(v2, v3)};
      ST2(y + fragaddr(m, nb + nf * 16 + 4 * (lane >> 4), 28), ov);
    }
  }
}

__global__ __launch_bounds__(768, 3) void k6_out(const bf16_t* __restrict__ y, const bf16_t* __restrict__ w,
                                                 const bf16_t* __restrict__ h, bf16_t* __restrict__ out) {
  constexpr int BM = 64, BN = 64, WM = 2, WK = 2, WN = 2, KT = 14, NST = (SPB6 == 2) ? 8 : 9;
  using C = GemmCfg<BM, BN, WM, WK, WN, KT, NST>;
  __shared__ __attribute__((aligned(16))) char smem[C::LDS + 8192];
  const int b = blockIdx.x, xcd = b & 7, idx = b >> 3;
  const int tm = xcd * 4 + (idx >> 3), tn = idx & 7;
  const int m0 = tm * BM, n0 = tn * BN;
  const int lane = vtid() & 63, wave = vtid() >> 6, wr = wave & 1, wk = (wave >> 1) & 1, wc = (wave >> 2) & 1;
  const int ml = wr * 32 + wk * 16 + (lane & 15);
  const int nl = wc * 32 + 4 * (lane >> 4);
  u32x2 rv0 = {0u, 0u}, rv1 = {0u, 0u};
  if (wave < 8) {
    const bf16_t* hp = h + fragaddr(m0 + ml, n0 + nl, 16);
    rv0 = *(const u32x2*)(hp);
    rv1 = *(const u32x2*)(hp + 256);
  }
  f32x4 acc[C::MF][C::NFW], cs[C::NFW], r[C::NFW];
#pragma unroll
  for (int i = 0; i < C::MF; ++i)
#pragma unroll
    for (int j = 0; j < C::NFW; ++j) acc[i][j] = f32x4{0.f, 0.f, 0.f, 0.f};
  NoHook hook;
  if (!gemm_main<BM, BN, WM, WK, WN, KT, NST, true, false, false, false, 0>(
          smem, y + (size_t)(m0 >> 4) * 28 * 512, 28, w + n0, D_MODEL, 128, ((idx >> 3) * 3 + (idx & 7) * 2) % KT, acc, cs,
          hook))
    return;
  splitk_combine<2>(smem, acc, r, wr + 2 * wc, wk, lane, true);
  u32x2 o0 = {pack2(lo_f(rv0[0]) + rbf(r[0][0]), hi_f(rv0[0]) + rbf(r[0][1])),
              pack2(lo_f(rv0[1]) + rbf(r[0][2]), hi_f(rv0[1]) + rbf(r[0][3]))};
  u32x2 o1 = {pack2(lo_f(rv1[0]) + rbf(r[1][0]), hi_f(rv1[0]) + rbf(r[1][1])),
              pack2(lo_f(rv1[1]) + rbf(r[1][2]), hi_f(rv1[1]) + rbf(r[1][3]))};
  char* os = smem + C::LDS;
  *(u32x2*)(os + ml * 128 + (((nl >> 3) ^ (ml & 7)) << 4) + (nl & 4) * 2) = o0;
  *(u32x2*)(os + ml * 128 + ((((nl + 16) >> 3) ^ (ml & 7)) << 4) + (nl & 4) * 2) = o1;
  __syncthreads();
  const int row = vtid() >> 3, ch = vtid() & 7;
  const u32x4 ov = *(const u32x4*)(os + row * 128 + ((ch ^ (row & 7)) << 4));
  *(u32x4*)(out + (size_t)(m0 + row) * D_MODEL + n0 + ch * 8) = ov;
}

#define RUNK(i)
extern "C" void kernel_launch(void* const* d_in, const int* in_sizes, int n_in, void* d_out, int out_size,
                              void* d_ws, size_t ws_size, hipStream_t stream) {
  const bf16_t* x = (const bf16_t*)d_in[0];
  const bf16_t* wqkv = (const bf16_t*)d_in[1];
  const bf16_t* wo = (const bf16_t*)d_in[2];
  const bf16_t* w_in = (const bf16_t*)d_in[4];
  const bf16_t* w_sp = (const bf16_t*)d_in[5];
  const bf16_t* b_sp = (const bf16_t*)d_in[6];
  const bf16_t* w_out = (const bf16_t*)d_in[7];
  bf16_t* out = (bf16_t*)d_out;

  char* ws = (char*)d_ws;
  size_t off = 0;
  auto take = [&](size_t bytes) { char* p = ws + off; off += (bytes + 255) & ~(size_t)255; return p; };
  bf16_t* qkf = (bf16_t*)take((size_t)S_LEN * 1024 * 2);
  bf16_t* v2 = (bf16_t*)take((size_t)512 * S_LEN * 2);
  bf16_t* o = (bf16_t*)take((size_t)S_LEN * D_MODEL * 2);
  bf16_t* h = (bf16_t*)take((size_t)S_LEN * D_MODEL * 2);
  f32x2* hstats = (f32x2*)take((size_t)S_LEN * 16 * 8);
  bf16_t* z1c = (bf16_t*)take((size_t)S_LEN * FH * 2);
  bf16_t* z2f = (bf16_t*)take((size_t)S_LEN * FH * 2);
  bf16_t* y = (bf16_t*)take((size_t)S_LEN * FH * 2);
  float* wcs = (float*)take((size_t)4 * 2 * FH * 4);
  u64* ex = (u64*)take((size_t)1024 * 1024 + 256);

  RUNK(1) k1_qkv<<<256, 768, 0, stream>>>(x, wqkv, qkf, v2, (u32x4*)ex, wo, w_in, wcs);
  RUNK(2) k2_attn<<<256, 512, 0, stream>>>(qkf, v2, (const bf16_t*)(ex + 131072), o);
  RUNK(3) k3_wo<<<256, 768, 0, stream>>>(o, wo, x, h, hstats, w_sp, w_out);
  RUNK(4) k4_in<<<224, 768, 0, stream>>>(h, w_in, hstats, z1c, z2f, ex, wcs);
  RUNK(5) k5_sp<<<256, 768, 0, stream>>>(w_sp, z2f, b_sp, z1c, y);
  RUNK(6) k6_out<<<256, 768, 0, stream>>>(y, w_out, h, out);
}
```

```cpp
#include <hip/hip_runtime.h>
#include <hip/hip_bf16.h>
#include <stdint.h>

typedef __bf16 bf16_t;
typedef __attribute__((ext_vector_type(8))) __bf16 bf16x8;
typedef __attribute__((ext_vector_type(2))) __bf16 bf16x2;
typedef __attribute__((ext_vector_type(4))) short s16x4;
typedef __attribute__((ext_vector_type(8))) short s16x8;
typedef __attribute__((ext_vector_type(4))) float f32x4;
typedef __attribute__((ext_vector_type(2))) float f32x2;
typedef __attribute__((ext_vector_type(16))) float f32x16;
typedef __attribute__((ext_vector_type(4))) unsigned u32x4;
typedef __attribute__((ext_vector_type(2))) unsigned u32x2;
typedef unsigned long long u64;

#define DI __device__ __forceinline__

constexpr int S_LEN = 2048;
constexpr int D_MODEL = 512;
constexpr int FH = 896;

DI float rbf(float x) { return (float)(__bf16)x; }
DI unsigned pack2(float a, float b) {
  bf16x2 v = {(__bf16)a, (__bf16)b};
  return __builtin_bit_cast(unsigned, v);
}
DI float lo_f(unsigned u) { return __uint_as_float(u << 16); }
DI float hi_f(unsigned u) { return __uint_as_float(u & 0xffff0000u); }

#define ST2(p, v) (*(u32x2*)(p) = (v))
DI unsigned vtid() {
  return threadIdx.x < 256u ? threadIdx.x + 512u : (threadIdx.x < 768u ? threadIdx.x - 256u : threadIdx.x);
}
template <int N> DI void wait_vm() { asm volatile("s_waitcnt vmcnt(%0)" ::"n"(N) : "memory"); }

DI void glds16(const void* g, char* l) {
  __builtin_amdgcn_global_load_lds((const __attribute__((address_space(1))) void*)g,
                                   (__attribute__((address_space(3))) void*)l, 16, 0, 0);
}
DI s16x4 tr_read(const char* l) {
  return __builtin_amdgcn_ds_read_tr16_b64_v4i16((__attribute__((address_space(3))) s16x4*)l);
}

template <int V> struct IC { static constexpr int value = V; };
constexpr int cmin3(int a, int b, int c) { return a < b ? (a < c ? a : c) : (b < c ? b : c); }
template <int N, int I = 0, class F> DI void static_for(F& f) {
  if constexpr (I < N) {
    f(IC<I>{});
    static_for<N, I + 1>(f);
  }
}

DI int fragaddr(int m, int k, int K32) {
  return ((m >> 4) * K32 + (k >> 5)) * 512 + ((m & 15) + 16 * ((k & 31) >> 3)) * 8 + (k & 7);
}

DI int bimg(int k, int cc) {
  return k * 128 + ((((cc >> 2) & 1) ^ ((k >> 1) & 1)) << 6) + (((cc & 3) ^ (((k >> 3) & 1) << 1)) << 4);
}

#define PF4 0
#define SPB5 2
#define SPB6 2
#define SPB3 2
template <int BM, int BN, int WM, int WK, int WN, int KT, int NST>
struct GemmCfg {
  static constexpr int NW = WM * WN * WK;
  static constexpr int NL = 4;
  static constexpr int PA = BM / 8, PB = BN / 8, P = PA + PB;
  static constexpr int PPL = (P + NL - 1) / NL;
  static constexpr int STAGE = (BM + BN) * 128;
  static constexpr int MF = BM / WM / 16;
  static constexpr int NFRAG = BN / 16;
  static constexpr int NFW = (NFRAG + WN - 1) / WN;
  static constexpr int LDS = STAGE * NST;
  static constexpr bool RESIDENT = NST >= KT;
  static constexpr int SPB = (KT == 32) ? SPB5 : (KT == 14) ? SPB6 : (BN == 64) ? SPB3 : 1;
  static constexpr int KS = KT / SPB;
  static constexpr int PRE = RESIDENT ? KS : NST / SPB - 1;
  static_assert(KT % SPB == 0 && NST % SPB == 0, "");
};

struct NoHook {
  DI void operator()(const char*) const {}
};

template <int BM, int BN, int WM, int WK, int WN, int KT, int NST, bool AFRAG, bool BFRAG, int PW, class Hook>
DI void gemm_loader(char* smem, const bf16_t* __restrict__ A, int lda, const bf16_t* __restrict__ B, int ldb,
                    int pstride, int lw, int rot, Hook& hook) {
  using C = GemmCfg<BM, BN, WM, WK, WN, KT, NST>;
  constexpr int NL = C::NL, PA = C::PA, STAGE = C::STAGE, PRE = C::PRE;
  static_assert(PA % NL == 0, "A pieces must be a multiple of the loader count");
  static_assert(BFRAG || (BN % 64) == 0, "row-major B needs 64-column panels");
  const int lane = vtid() & 63;
  unsigned off[PW];
#pragma unroll
  for (int i = 0; i < PW; ++i) {
    if (i < PA / NL) {
      const int p = lw + i * NL;
      if constexpr (AFRAG) {
        off[i] = (unsigned)(((p >> 1) * lda + (p & 1)) * 1024 + lane * 16);
      } else {
        off[i] = (unsigned)((8 * p + (lane >> 3)) * lda * 2 + (((lane & 7) ^ (lane >> 3)) << 4));
      }
    } else {
      const int p = lw + i * NL - PA;
      if constexpr (BFRAG) {
        off[i] = (unsigned)(((p >> 1) * ldb + (p & 1)) * 1024 + lane * 16);
      } else {
        int k = (p & 7) * 8 + (lane >> 3);
        int cc = (lane & 7) ^ (((lane >> 4) & 1) << 2) ^ ((p & 1) << 1);
        off[i] = (unsigned)(k * ldb * 2 + (p >> 3) * pstride + cc * 16);
      }
    }
  }
  rot = 0;
  int kr = __builtin_amdgcn_readfirstlane(rot), kw = 0;
  auto issue = [&](int slot) {
    char* base = smem + slot * STAGE + lw * 1024;
    const int kk = kw + kr;
    const char* pa = (const char*)A + kk * (AFRAG ? 2048 : 128);
    const char* pb = (const char*)B + (size_t)kk * (BFRAG ? 2048 : 128 * ldb);
#pragma unroll
    for (int i = 0; i < PW; ++i) {
      unsigned o = off[i];
      asm volatile("" : "+v"(o));
      glds16((i < PA / NL ? pa : pb) + o, base + i * NL * 1024);
    }
    if constexpr (KT % 8 == 0) {
      kr = (kr + 1) & 7;
      if (kr == rot) kw += 8;
    } else {
      kr = (kr + 1 == KT) ? 0 : kr + 1;
    }
  };
  constexpr int SPB = C::SPB, KS = C::KS;
  auto body = [&](auto KTC) {
    constexpr int kt = decltype(KTC)::value;
    constexpr int I0 = (kt == 0) ? 0 : cmin3(KS, kt - 1 + PRE, 2 * kt);
    constexpr int I1 = cmin3(KS, kt + PRE, 2 * kt + 2);
#pragma unroll
    for (int u = I0 * SPB; u < I1 * SPB; ++u) issue(u % NST);
    wait_vm<(I1 - kt - 1) * PW * SPB>();
    __builtin_amdgcn_s_barrier();
    asm volatile("" ::: "memory");
#pragma unroll
    for (int u = 0; u < SPB; ++u) hook(smem + ((kt * SPB + u) % NST) * STAGE);
    __builtin_amdgcn_s_waitcnt(0xC07F);
  };
  static_for<KS>(body);
}

struct NoPre {
  DI void operator()() const {}
};

template <int BM, int BN, int WM, int WK, int WN, int KT, int NST, bool AFRAG, bool BFRAG, bool CS, bool FULLN,
          bool SPLITN, int PLAIN, class Pre = NoPre>
DI void gemm_compute(char* smem, int wave,
                     f32x4 (&acc)[GemmCfg<BM, BN, WM, WK, WN, KT, NST>::MF][GemmCfg<BM, BN, WM, WK, WN, KT, NST>::NFW],
                     f32x4 (&cs)[GemmCfg<BM, BN, WM, WK, WN, KT, NST>::NFW], Pre pre = Pre()) {
  using C = GemmCfg<BM, BN, WM, WK, WN, KT, NST>;
  constexpr int STAGE = C::STAGE, MF = C::MF, NFW = C::NFW;
  constexpr int NF = FULLN ? NFW : NFW - 1;
  const int lane = vtid() & 63;
  const int wr = wave % WM, wk = (wave / WM) % WK, wc = wave / (WM * WK);
  const int g = lane >> 4, q = (lane & 15) >> 2, pp = lane & 3;
  constexpr int NKS = (WK == 2) ? 1 : 2;
  int aoff[NKS];
#pragma unroll
  for (int i = 0; i < NKS; ++i) {
    const int ks = (WK == 2) ? wk : i;
    if constexpr (AFRAG) aoff[i] = (wr * MF * 2 + ks) * 1024 + lane * 16;
    else aoff[i] = (wr * (MF * 16) + (lane & 15)) * 128 + (((4 * ks + g) ^ (lane & 7)) << 4);
  }
  int boff[NF];
#pragma unroll
  for (int nf = 0; nf < NF; ++nf) {
    const int f = SPLITN ? ((nf >> 1) * 4 + 2 * wc + (nf & 1)) : (wc * NFW + nf);
    if constexpr (BFRAG) {
      boff[nf] = BM * 128 + (f * 2 + ((WK == 2) ? wk : 0)) * 1024 + lane * 16;
    } else {
      const int kk = 8 * g + q + ((WK == 2) ? wk * 32 : 0);
      boff[nf] = BM * 128 + (f >> 2) * 8192 + bimg(kk, 2 * (f & 3) + (pp >> 1)) + 8 * (pp & 1);
    }
  }
  bf16x8 ones;
#pragma unroll
  for (int j = 0; j < 8; ++j) ones[j] = (__bf16)1.0f;

  constexpr int G = NKS * C::SPB, NSTEP = KT * NKS;
  static_assert(G <= 2, "");
  bf16x8 af[3][MF], bfr[3][NF];
  auto rd = [&](auto JC) {
    constexpr int j = decltype(JC)::value;
    constexpr int kt = j / NKS, i = j % NKS;
    const char* sb = smem + (kt % NST) * STAGE;
#pragma unroll
    for (int mf = 0; mf < MF; ++mf) af[j % 3][mf] = *(const bf16x8*)(sb + aoff[i] + mf * 2048);
#pragma unroll
    for (int nf = 0; nf < NF; ++nf) {
      if constexpr (BFRAG) {
        bfr[j % 3][nf] = *(const bf16x8*)(sb + boff[nf] + i * 1024);
      } else {
        s16x4 lo = tr_read(sb + boff[nf] + i * 4096);
        s16x4 hi = tr_read(sb + boff[nf] + i * 4096 + 512);
        s16x8 v = __builtin_shufflevector(lo, hi, 0, 1, 2, 3, 4, 5, 6, 7);
        bfr[j % 3][nf] = __builtin_bit_cast(bf16x8, v);
      }
    }
  };
  auto mm = [&](auto JC) {
    constexpr int jp = decltype(JC)::value;
#pragma unroll
    for (int nf = 0; nf < NF; ++nf) {
#pragma unroll
      for (int mf = 0; mf < MF; ++mf) {
        if ((PLAIN >> nf) & 1)
          acc[mf][nf] = __builtin_amdgcn_mfma_f32_16x16x32_bf16(af[jp % 3][mf], bfr[jp % 3][nf], acc[mf][nf], 0, 0, 0);
        else
          acc[mf][nf] = __builtin_amdgcn_mfma_f32_16x16x32_bf16(bfr[jp % 3][nf], af[jp % 3][mf], acc[mf][nf], 0, 0, 0);
      }
    }
  };
  auto csum = [&](auto JC) {
    constexpr int jp = decltype(JC)::value;
    if constexpr (CS) {
      if (wr == (jp % WM)) {
#pragma unroll
        for (int nf = 0; nf < NF; ++nf) {
          if ((PLAIN >> nf) & 1) cs[nf] = __builtin_amdgcn_mfma_f32_16x16x32_bf16(ones, bfr[jp % 3][nf], cs[nf], 0, 0, 0);
          else cs[nf] = __builtin_amdgcn_mfma_f32_16x16x32_bf16(bfr[jp % 3][nf], ones, cs[nf], 0, 0, 0);
        }
      }
    }
  };
  auto group = [&](auto GC) {
    constexpr int j0 = decltype(GC)::value * G;
    constexpr int NRD = G * (MF + (BFRAG ? NF : 2 * NF)), NMM = MF * NF;
    constexpr int RPM = (NRD + NMM - 1) / NMM;
    __builtin_amdgcn_s_barrier();
    asm volatile("" ::: "memory");
    if constexpr (decltype(GC)::value == NSTEP / G - 2) pre();
    rd(IC<j0>{});
    if constexpr (G == 2) rd(IC<j0 + 1>{});
    if constexpr (j0 > 0) {
      mm(IC<j0 - 1>{});
#pragma unroll
      for (int u = 0; u < NMM; ++u) {
        __builtin_amdgcn_sched_group_barrier(0x8, 1, 0);
        __builtin_amdgcn_sched_group_barrier(0x100, RPM, 0);
      }
      __builtin_amdgcn_sched_barrier(0);
      csum(IC<j0 - 1>{});
    }
    if constexpr (G == 2) {
      __builtin_amdgcn_sched_barrier(0);
      mm(IC<j0>{});
      csum(IC<j0>{});
    }
    __builtin_amdgcn_sched_barrier(0);
    __builtin_amdgcn_s_waitcnt(0xC07F);
  };
  static_for<NSTEP / G>(group);
  mm(IC<NSTEP - 1>{});
  csum(IC<NSTEP - 1>{});
}

template <int BM, int BN, int WM, int WK, int WN, int KT, int NST, bool AFRAG, bool BFRAG, bool CS, bool SPLITN,
          int PLAIN, class Hook, class Pre = NoPre>
DI bool gemm_main(char* smem, const bf16_t* __restrict__ A, int lda, const bf16_t* __restrict__ B, int ldb, int pstride,
                  int rot,
                  f32x4 (&acc)[GemmCfg<BM, BN, WM, WK, WN, KT, NST>::MF][GemmCfg<BM, BN, WM, WK, WN, KT, NST>::NFW],
                  f32x4 (&cs)[GemmCfg<BM, BN, WM, WK, WN, KT, NST>::NFW], Hook& hook, Pre pre = Pre()) {
  using C = GemmCfg<BM, BN, WM, WK, WN, KT, NST>;
  const int wave = __builtin_amdgcn_readfirstlane(vtid() >> 6);
  if (wave >= C::NW) {
    const int lw = wave - C::NW;
    __builtin_amdgcn_s_setprio(3);
    constexpr bool EVEN = (C::P % C::NL) == 0;
    if (EVEN || lw < (C::P % C::NL))
      gemm_loader<BM, BN, WM, WK, WN, KT, NST, AFRAG, BFRAG, C::PPL>(smem, A, lda, B, ldb, pstride, lw, rot, hook);
    else if constexpr (!EVEN)
      gemm_loader<BM, BN, WM, WK, WN, KT, NST, AFRAG, BFRAG, C::PPL - 1>(smem, A, lda, B, ldb, pstride, lw, rot, hook);
    return false;
  }
  const int wc = wave / (WM * WK);
  constexpr bool ALLFULL = (C::NFRAG == WN * C::NFW);
  if (ALLFULL || wc != WN - 1)
    gemm_compute<BM, BN, WM, WK, WN, KT, NST, AFRAG, BFRAG, CS, true, SPLITN, PLAIN>(smem, wave, acc, cs, pre);
  else if constexpr (!ALLFULL)
    gemm_compute<BM, BN, WM, WK, WN, KT, NST, AFRAG, BFRAG, CS, false, SPLITN, PLAIN>(smem, wave, acc, cs, pre);
  return true;
}

template <int NFW>
DI void splitk_combine(char* smem, f32x4 (&acc)[2][NFW], f32x4 (&res)[NFW], int pairid, int wk, int lane, bool fulln) {
  __syncthreads();
  char* mine = smem + (pairid * 2 + wk) * (NFW * 1024) + lane * 16;
  char* theirs = smem + (pairid * 2 + (wk ^ 1)) * (NFW * 1024) + lane * 16;
#pragma unroll
  for (int nf = 0; nf < NFW; ++nf)
    if (nf < NFW - 1 || fulln) *(f32x4*)(mine + nf * 1024) = wk ? acc[0][nf] : acc[1][nf];
  __syncthreads();
#pragma unroll
  for (int nf = 0; nf < NFW; ++nf)
    if (nf < NFW - 1 || fulln) res[nf] = (wk ? acc[1][nf] : acc[0][nf]) + *(const f32x4*)(theirs + nf * 1024);
}

DI void chan_eq(float& mean, float& m2, float mean_b, float m2_b, float n) {
  float d = mean_b - mean;
  m2 = m2 + m2_b + d * d * (0.5f * n);
  mean = 0.5f * (mean + mean_b);
}

DI void ln_consts(float mean, float var, float& mean_b, float& rinv) {
  mean_b = rbf(mean);
  float v = rbf(var);
  float t = rbf(v + 1e-5f);
  float sd = rbf(sqrtf(t));
  rinv = 1.0f / sd;
}

DI void l2_prefetch(const char* base, int nlines, int t, int nt) {
  unsigned acc = 0u;
  for (int i = t; i < nlines; i += 4 * nt) {
    unsigned v0 = *(const unsigned*)(base + (size_t)i * 128);
    unsigned v1 = (i + nt < nlines) ? *(const unsigned*)(base + (size_t)(i + nt) * 128) : 0u;
    unsigned v2 = (i + 2 * nt < nlines) ? *(const unsigned*)(base + (size_t)(i + 2 * nt) * 128) : 0u;
    unsigned v3 = (i + 3 * nt < nlines) ? *(const unsigned*)(base + (size_t)(i + 3 * nt) * 128) : 0u;
    acc ^= v0 ^ v1 ^ v2 ^ v3;
  }
  asm volatile("" ::"v"(acc));
}

DI float row16_sum(float v) {
  v += __builtin_bit_cast(float, __builtin_amdgcn_update_dpp(0, __builtin_bit_cast(int, v), 0x128, 0xf, 0xf, false));
  v += __builtin_bit_cast(float, __builtin_amdgcn_update_dpp(0, __builtin_bit_cast(int, v), 0x124, 0xf, 0xf, false));
  v += __builtin_bit_cast(float, __builtin_amdgcn_update_dpp(0, __builtin_bit_cast(int, v), 0x122, 0xf, 0xf, false));
  v += __builtin_bit_cast(float, __builtin_amdgcn_update_dpp(0, __builtin_bit_cast(int, v), 0x121, 0xf, 0xf, false));
  return v;
}

struct StatHook {
  float s1, s2;
  int o0;
  DI void operator()(const char* sb) {
    bf16x2 one2 = {(__bf16)1.0f, (__bf16)1.0f};
    u32x4 v0 = *(const u32x4*)(sb + o0);
    u32x4 v1 = *(const u32x4*)(sb + o0 + 16);
#pragma unroll
    for (int e = 0; e < 4; ++e) {
      const unsigned ua = v0[e], ub = v1[e];
      bf16x2 a = __builtin_bit_cast(bf16x2, ua), b = __builtin_bit_cast(bf16x2, ub);
      s1 = __builtin_amdgcn_fdot2_f32_bf16(a, one2, s1, false);
      s2 = __builtin_amdgcn_fdot2_f32_bf16(a, a, s2, false);
      s1 = __builtin_amdgcn_fdot2_f32_bf16(b, one2, s1, false);
      s2 = __builtin_amdgcn_fdot2_f32_bf16(b, b, s2, false);
    }
  }
};

__global__ __launch_bounds__(1024, 4) void k1_qkv(const bf16_t* __restrict__ x, const bf16_t* __restrict__ w,
                                                 bf16_t* __restrict__ qkf, bf16_t* __restrict__ v2,
                                                 u32x4* __restrict__ exz, const bf16_t* __restrict__ wo,
                                                 const bf16_t* __restrict__ win, float* __restrict__ wcs) {
  constexpr int BM = 64, BN = 192, WM = 2, WK = 1, WN = 4, KT = 8, NST = 4;
  using C = GemmCfg<BM, BN, WM, WK, WN, KT, NST>;
  __shared__ __attribute__((aligned(16))) char smem[C::LDS + 2048];
  if (vtid() < 256) exz[blockIdx.x * 256 + vtid()] = u32x4{0u, 0u, 0u, 0u};
  if (blockIdx.x == 0 && vtid() < 2) {
    const unsigned cv = vtid() ? 0u : 0x3f803f80u;
    exz[65536 + vtid()] = u32x4{cv, cv, cv, cv};
  }

  const int b = blockIdx.x, xcd = b & 7, idx = b >> 3;
  const int tm = xcd * 4 + (idx >> 3), tn = idx & 7;
  const int m0 = tm * BM, n0 = tn * BN;
  f32x4 acc[C::MF][C::NFW], cs[C::NFW];
#pragma unroll
  for (int i = 0; i < C::MF; ++i)
#pragma unroll
    for (int j = 0; j < C::NFW; ++j) acc[i][j] = f32x4{0.f, 0.f, 0.f, 0.f};
#pragma unroll
  for (int j = 0; j < C::NFW; ++j) cs[j] = f32x4{0.f, 0.f, 0.f, 0.f};
  StatHook hook;
  const int srow = (vtid() >> 2) & 63, sq = vtid() & 3;
  hook.s1 = 0.f; hook.s2 = 0.f;
  hook.o0 = srow * 128 + (sq << 5);
  const int lane = vtid() & 63, wave = __builtin_amdgcn_readfirstlane(vtid() >> 6), wr = wave & 1,
            wc = (wave >> 1) & 3;
  const int nw0 = n0 + wc * 48;
  const int plain = (nw0 >= 1024) ? 7 : ((nw0 + 16 >= 1024) ? 6 : 0);
  const bool comp = wave < 8;
  if (wave >= 12) {
#pragma unroll
    for (int kt = 0; kt < KT; ++kt) {
      __builtin_amdgcn_s_barrier();
      asm volatile("" ::: "memory");
      hook(smem + (kt % NST) * C::STAGE);
      __builtin_amdgcn_s_waitcnt(0xC07F);
    }
  } else if (wave >= 8) {
    __builtin_amdgcn_s_setprio(3);
    NoHook nh;
    gemm_loader<BM, BN, WM, WK, WN, KT, NST, false, false, C::PPL>(smem, x + (size_t)m0 * D_MODEL, D_MODEL, w + n0,
                                                                   3 * D_MODEL, 128, wave - 8, 0, nh);
    {
      const int lt = vtid() - 512;
      if (lt < 72) {
        const char* p0 = (lt < 16) ? (const char*)wo + (size_t)(b * 16 + lt) * 128
                                   : (const char*)win + (size_t)(b * 56 + lt - 16) * 128;
        unsigned v0 = *(const unsigned*)p0;
        asm volatile("" ::"v"(v0));
      }
      if (b < 28) {
        const bf16_t* wp = win + (size_t)((lt >> 3) * 16) * (2 * FH) + b * 64 + (lt & 7) * 8;
        float a[8];
#pragma unroll
        for (int e = 0; e < 8; ++e) a[e] = 0.f;
#pragma unroll
        for (int i = 0; i < 16; ++i) {
          const u32x4 v = *(const u32x4*)(wp + (size_t)i * (2 * FH));
#pragma unroll
          for (int e = 0; e < 4; ++e) { a[2 * e] += lo_f(v[e]); a[2 * e + 1] += hi_f(v[e]); }
        }
#pragma unroll
        for (int e = 0; e < 8; ++e) {
          a[e] += __shfl_xor(a[e], 8);
          a[e] += __shfl_xor(a[e], 16);
          a[e] += __shfl_xor(a[e], 32);
        }
        if ((lt & 63) < 8) {
          float* dp = wcs + (lt >> 6) * (2 * FH) + b * 64 + (lt & 7) * 8;
          *(f32x4*)dp = f32x4{a[0], a[1], a[2], a[3]};
          *(f32x4*)(dp + 4) = f32x4{a[4], a[5], a[6], a[7]};
        }
      }
    }
    return;
  } else if (plain == 7) {
    gemm_compute<BM, BN, WM, WK, WN, KT, NST, false, false, true, true, false, 7>(smem, wave, acc, cs);
  } else if (plain == 6) {
    gemm_compute<BM, BN, WM, WK, WN, KT, NST, false, false, true, true, false, 6>(smem, wave, acc, cs);
  } else {
    gemm_compute<BM, BN, WM, WK, WN, KT, NST, false, false, true, true, false, 0>(smem, wave, acc, cs);
  }

  float* tmean = (float*)(smem + C::LDS);
  float* trinv = tmean + 64;
  float* tcs = trinv + 64;
  if (!comp) {
    float s1 = hook.s1 + __shfl_xor(hook.s1, 1);
    float s2 = hook.s2 + __shfl_xor(hook.s2, 1);
    s1 += __shfl_xor(s1, 2);
    s2 += __shfl_xor(s2, 2);
    const float mean = s1 * (1.0f / 512.0f), ex2 = s2 * (1.0f / 512.0f);
    float var = ex2 - mean * mean;
    if (var < ex2 * (1.0f / 1024.0f)) {
      const bf16_t* xr = x + (size_t)(m0 + srow) * D_MODEL + sq * 128;
      float a2 = 0.f;
      for (int j = 0; j < 16; ++j) {
        u32x4 v = *(const u32x4*)(xr + j * 8);
#pragma unroll
        for (int e = 0; e < 4; ++e) {
          float d0 = lo_f(v[e]) - mean, d1 = hi_f(v[e]) - mean;
          a2 = fmaf(d0, d0, a2);
          a2 = fmaf(d1, d1, a2);
        }
      }
      a2 += __shfl_xor(a2, 1);
      a2 += __shfl_xor(a2, 2);
      var = a2 * (1.0f / 512.0f);
    }
    float mb, ri;
    ln_consts(mean, var, mb, ri);
    if (sq == 0) { tmean[srow] = mb; trinv[srow] = ri; }
    __syncthreads();
    return;
  }
  const int g = lane >> 4;
#pragma unroll
  for (int nf = 0; nf < C::NFW; ++nf) {
    if ((plain >> nf) & 1) {
      if (lane < 16) tcs[wr * 192 + wc * 48 + nf * 16 + lane] = cs[nf][0];
    } else if ((lane & 15) == 0) {
      *(f32x4*)(tcs + wr * 192 + wc * 48 + nf * 16 + 4 * g) = cs[nf];
    }
  }
  __syncthreads();
#pragma unroll
  for (int nf = 0; nf < C::NFW; ++nf) {
    if ((plain >> nf) & 1) {
      const int nl = wc * 48 + nf * 16 + (lane & 15);
      const float c = tcs[nl] + tcs[192 + nl];
      const int nv = n0 + nl - 1024;
#pragma unroll
      for (int mf = 0; mf < C::MF; ++mf) {
        const int ml = wr * 32 + mf * 16 + 4 * g;
        const f32x4 mb = *(const f32x4*)(tmean + ml), ri = *(const f32x4*)(trinv + ml);
        u32x2 o = {pack2((acc[mf][nf][0] - mb[0] * c) * ri[0], (acc[mf][nf][1] - mb[1] * c) * ri[1]),
                   pack2((acc[mf][nf][2] - mb[2] * c) * ri[2], (acc[mf][nf][3] - mb[3] * c) * ri[3])};
        const int m = m0 + ml;
        ST2(v2 + (((size_t)(nv >> 4) * 256 + (m >> 3)) * 16 + (nv & 15)) * 8 + (m & 7), o);
      }
    } else {
      const int nl = wc * 48 + nf * 16 + 4 * g;
      f32x4 c = *(const f32x4*)(tcs + nl) + *(const f32x4*)(tcs + 192 + nl);
      const int n = n0 + nl;
#pragma unroll
      for (int mf = 0; mf < C::MF; ++mf) {
        const int ml = wr * 32 + mf * 16 + (lane & 15);
        const float mb = tmean[ml], ri = trinv[ml];
        u32x2 o = {pack2((acc[mf][nf][0] - mb * c[0]) * ri, (acc[mf][nf][1] - mb * c[1]) * ri),
                   pack2((acc[mf][nf][2] - mb * c[2]) * ri, (acc[mf][nf][3] - mb * c[3]) * ri)};
        ST2(qkf + ((size_t)(n >> 3) * S_LEN + m0 + ml) * 8 + (n & 7), o);
      }
    }
  }
}

template <bool EDGE>
DI void attn_body(const bf16_t* __restrict__ qkf, const bf16_t* __restrict__ v2, const bf16_t* __restrict__ cst,
                  bf16_t* __restrict__ o, int qt, int h, const char* __restrict__ pf) {
  const int lane = threadIdx.x & 63;
  const int r = lane & 31, hf = lane >> 5;
  const int q0 = qt * 32;
  const int t0 = EDGE ? 8 - qt : 0;

  const bf16x8 qf = *(const bf16x8*)(qkf + ((size_t)(h * 2 + hf) * S_LEN + q0 + r) * 8);
  const int pr = 16 * ((r >> 2) & 1) + 4 * (r >> 3) + (r & 3);
  const bf16_t* kp = qkf + ((size_t)(64 + h * 2 + hf) * S_LEN + pr) * 8;
  bf16x8 kf[9];
#pragma unroll
  for (int t = 0; t < 9; ++t) {
    int kb = q0 - 256 + 32 * t;
    if (EDGE && kb < 0) kb = 0;
    kf[t] = *(const bf16x8*)(kp + kb * 8);
  }
  bf16x8 vf[9][2];
  const bf16_t* vp = (r < 16) ? v2 + ((size_t)h * 256 * 16 + 2 * hf * 16 + r) * 8 : cst + ((r == 16) ? 0 : 8);
  const int vm = (r < 16) ? 16 : 0;
#pragma unroll
  for (int t = 0; t < 9; ++t) {
    int kb = q0 - 256 + 32 * t;
    if (EDGE && kb < 0) kb = 0;
    vf[t][0] = *(const bf16x8*)(vp + kb * vm);
    vf[t][1] = *(const bf16x8*)(vp + (kb + 8) * vm);
  }
  const unsigned touch = *(const unsigned*)pf;
  f32x16 s[9];
  f32x16 zero;
#pragma unroll
  for (int i = 0; i < 16; ++i) zero[i] = 0.f;
#pragma unroll
  for (int t = 0; t < 9; ++t) s[t] = __builtin_amdgcn_mfma_f32_32x32x16_bf16(kf[t], qf, zero, 0, 0, 0);

  const float NEG = -3.0e38f;
#pragma unroll
  for (int i = 0; i < 16; ++i) {
    if (!(16 * hf + i > r)) s[0][i] = NEG;
    if (!(16 * hf + i <= r)) s[8][i] = NEG;
  }
  if constexpr (EDGE) {
#pragma unroll
    for (int t = 0; t < 8; ++t) {
      if (t < t0) {
#pragma unroll
        for (int i = 0; i < 16; ++i) s[t][i] = NEG;
      }
    }
  }
  float mx = NEG;
#pragma unroll
  for (int t = 0; t < 9; ++t)
#pragma unroll
    for (int i = 0; i < 16; ++i) mx = fmaxf(mx, s[t][i]);
  mx = fmaxf(mx, __shfl_xor(mx, 32));
  const float c = 0.25f * 1.4426950408889634f;
  const f32x2 c2 = {c, c}, mc2 = {-mx * c, -mx * c};
  f32x16 oacc = zero;
#pragma unroll
  for (int t = 0; t < 9; ++t) {
#pragma unroll
    for (int st = 0; st < 2; ++st) {
      bf16x8 pf;
#pragma unroll
      for (int j = 0; j < 8; j += 2) {
        f32x2 e = {s[t][8 * st + j], s[t][8 * st + j + 1]};
        e = __builtin_elementwise_fma(e, c2, mc2);
        pf[j] = (__bf16)__builtin_amdgcn_exp2f(e[0]);
        pf[j + 1] = (__bf16)__builtin_amdgcn_exp2f(e[1]);
      }
      oacc = __builtin_amdgcn_mfma_f32_32x32x16_bf16(vf[t][st], pf, oacc, 0, 0, 0);
    }
  }
  const float l = __shfl(oacc[8], r);
  const float il = 1.0f / l;
  bf16_t* op = o + fragaddr(q0 + r, h * 16 + 4 * hf, 16);
  u32x2 o0 = {pack2(oacc[0] * il, oacc[1] * il), pack2(oacc[2] * il, oacc[3] * il)};
  u32x2 o1 = {pack2(oacc[4] * il, oacc[5] * il), pack2(oacc[6] * il, oacc[7] * il)};
  ST2(op, o0);
  ST2(op + 128, o1);
  asm volatile("" ::"v"(touch));
}

__global__ __launch_bounds__(512) void k2_attn(const bf16_t* __restrict__ qkf, const bf16_t* __restrict__ v2,
                                               const bf16_t* __restrict__ cst, bf16_t* __restrict__ o,
                                               const bf16_t* __restrict__ wsp, const bf16_t* __restrict__ wout) {
  const int b = blockIdx.x, xcd = b & 7, idx = b >> 3;
  const int qt = xcd * 8 + (idx >> 2), hg = idx & 3;
  const int h = hg * 8 + (threadIdx.x >> 6);
  const int gl = b * 512 + threadIdx.x;
  const char* pf = (gl < 65536) ? (const char*)wsp + (size_t)gl * 128
                                : (const char*)wout + (size_t)((gl - 65536) % 7168) * 128;
  if (qt < 8) attn_body<true>(qkf, v2, cst, o, qt, h, pf);
  else attn_body<false>(qkf, v2, cst, o, qt, h, pf);
}

__global__ __launch_bounds__(768, 3) void k3_wo(const bf16_t* __restrict__ o, const bf16_t* __restrict__ w,
                                                const bf16_t* __restrict__ x, bf16_t* __restrict__ h,
                                                f32x2* __restrict__ stats) {
  constexpr int BM = 64, BN = 64, WM = 2, WK = 2, WN = 2, KT = 8, NST = 8;
  using C = GemmCfg<BM, BN, WM, WK, WN, KT, NST>;
  __shared__ __attribute__((aligned(16))) char smem[C::LDS + 8192];
  const int b = blockIdx.x, xcd = b & 7, idx = b >> 3;
  const int tm = xcd * 4 + (idx >> 3), tn = idx & 7;
  const int m0 = tm * BM, n0 = tn * BN;
  const int lane = vtid() & 63, wave = vtid() >> 6, wr = wave & 1, wk = (wave >> 1) & 1, wc = (wave >> 2) & 1;
  char* xs = smem + C::LDS;
  if (wave >= 8) {
    const int lw = __builtin_amdgcn_readfirstlane(wave) - 8;
#pragma unroll
    for (int i = 0; i < 2; ++i) {
      const int p = lw + 4 * i;
      glds16((const char*)(x + (size_t)(m0 + 8 * p + (lane >> 3)) * D_MODEL + n0) + (((lane & 7) ^ (lane >> 3)) << 4),
             xs + p * 1024);
    }
  }
  f32x4 acc[C::MF][C::NFW], cs[C::NFW], r[C::NFW];
#pragma unroll
  for (int i = 0; i < C::MF; ++i)
#pragma unroll
    for (int j = 0; j < C::NFW; ++j) acc[i][j] = f32x4{0.f, 0.f, 0.f, 0.f};
  NoHook hook;
  if (!gemm_main<BM, BN, WM, WK, WN, KT, NST, true, false, false, false, 0>(
          smem, o + (size_t)m0 * D_MODEL, 16, w + n0, D_MODEL, 128, (2 * (idx >> 3) + (idx & 7)) & 7, acc, cs, hook)) {
    return;
  }
  splitk_combine<2>(smem, acc, r, wr + 2 * wc, wk, lane, true);
  const int ml = wr * 32 + wk * 16 + (lane & 15);
  const int nl = wc * 32 + 4 * (lane >> 4);
  const u32x2 rv0 = *(const u32x2*)(xs + ml * 128 + (((nl >> 3) ^ (ml & 7)) << 4) + (nl & 4) * 2);
  const u32x2 rv1 = *(const u32x2*)(xs + ml * 128 + ((((nl + 16) >> 3) ^ (ml & 7)) << 4) + (nl & 4) * 2);
  float hv[8];
  hv[0] = rbf(lo_f(rv0[0]) + rbf(r[0][0])); hv[1] = rbf(hi_f(rv0[0]) + rbf(r[0][1]));
  hv[2] = rbf(lo_f(rv0[1]) + rbf(r[0][2])); hv[3] = rbf(hi_f(rv0[1]) + rbf(r[0][3]));
  hv[4] = rbf(lo_f(rv1[0]) + rbf(r[1][0])); hv[5] = rbf(hi_f(rv1[0]) + rbf(r[1][1]));
  hv[6] = rbf(lo_f(rv1[1]) + rbf(r[1][2])); hv[7] = rbf(hi_f(rv1[1]) + rbf(r[1][3]));
  u32x2 o0 = {pack2(hv[0], hv[1]), pack2(hv[2], hv[3])};
  u32x2 o1 = {pack2(hv[4], hv[5]), pack2(hv[6], hv[7])};
  bf16_t* hp = h + fragaddr(m0 + ml, n0 + nl, 16);
  ST2(hp, o0);
  ST2(hp + 256, o1);
  float mean = 0.f;
#pragma unroll
  for (int j = 0; j < 8; ++j) mean += hv[j];
  mean *= 0.125f;
  float m2 = 0.f;
#pragma unroll
  for (int j = 0; j < 8; ++j) { float d = hv[j] - mean; m2 = fmaf(d, d, m2); }
  chan_eq(mean, m2, __shfl_xor(mean, 16), __shfl_xor(m2, 16), 8.f);
  chan_eq(mean, m2, __shfl_xor(mean, 32), __shfl_xor(m2, 32), 16.f);
  if (lane < 16) stats[(size_t)(tn * 2 + wc) * S_LEN + m0 + ml] = f32x2{mean, m2};
}

DI float gelu_f(float x) {
  const float k0 = 0.7978845608028654f * 2.0f * 1.4426950408889634f;
  float y = k0 * fmaf(0.044715f * x * x, x, x);
  float e = __builtin_amdgcn_exp2f(-y);
  return x * __builtin_amdgcn_rcpf(1.0f + e);
}

__global__ __launch_bounds__(768, 3) void k4_in(const bf16_t* __restrict__ h, const bf16_t* __restrict__ w,
                                                const f32x2* __restrict__ hstats, bf16_t* __restrict__ z1c,
                                                bf16_t* __restrict__ z2f, u64* __restrict__ ex,
                                                const float* __restrict__ wcs) {
  constexpr int BM = 128, BN = 128, WM = 4, WK = 1, WN = 2, KT = 8, NST = 4;
  using C = GemmCfg<BM, BN, WM, WK, WN, KT, NST>;
  __shared__ __attribute__((aligned(16))) char smem[C::LDS + 8192];
  const int b = blockIdx.x, xcd = b & 7, idx = b >> 3;
  const int tm = xcd * 2 + (idx >= 14 ? 1 : 0), tn = (idx >= 14) ? idx - 14 : idx;
  const int m0 = tm * BM, n0 = tn * 64;
  const int lane = vtid() & 63, wave = vtid() >> 6, wr = wave & 3, wc = (wave >> 2) & 1;
  const int g = lane >> 4;
  float* tmean = (float*)(smem + C::LDS);
  float* trinv = tmean + 128;
  float* tcs = trinv + 128;
  float* tmean2 = tcs + 512;
  float* trinv2 = tmean2 + 128;
  float* tpart = trinv2 + 128;
  f32x2* tpart2 = (f32x2*)(tpart + 512);
  f32x4 acc[C::MF][C::NFW], cs[C::NFW];
#pragma unroll
  for (int i = 0; i < C::MF; ++i)
#pragma unroll
    for (int j = 0; j < C::NFW; ++j) acc[i][j] = f32x4{0.f, 0.f, 0.f, 0.f};
#pragma unroll
  for (int j = 0; j < C::NFW; ++j) cs[j] = f32x4{0.f, 0.f, 0.f, 0.f};
  f32x2 hv[8];
  float cv[4];
  if (vtid() >= 512) {
    const f32x2* hp = hstats + (size_t)((vtid() >> 7) & 1) * 8 * S_LEN + m0 + (vtid() & 127);
#pragma unroll
    for (int j = 0; j < 8; ++j) hv[j] = hp[(size_t)j * S_LEN];
    const float* cp = wcs + n0 + (vtid() & 63) + ((vtid() >> 6) & 1) * FH;
#pragma unroll
    for (int j = 0; j < 4; ++j) cv[j] = cp[j * 2 * FH];
  }
  NoHook hook;
  const bool comp = gemm_main<BM, BN, WM, WK, WN, KT, NST, true, false, false, true, 12>(
      smem, h + (size_t)m0 * D_MODEL, 16, w + n0, 2 * FH, FH * 2, ((idx >= 14 ? 4 : 0) + tn) & 7, acc, cs, hook);
  if (!comp) {
    const int lt = vtid() - 512;
    float mean = 0.f;
#pragma unroll
    for (int j = 0; j < 8; ++j) mean += hv[j][0];
    mean *= 0.125f;
    float m2 = 0.f;
#pragma unroll
    for (int j = 0; j < 8; ++j) { float d = hv[j][0] - mean; m2 += hv[j][1] + 32.f * d * d; }
    if (lt >= 128) tpart2[lt - 128] = f32x2{mean, m2};
    else tcs[lt] = (cv[0] + cv[1]) + (cv[2] + cv[3]);
    __syncthreads();
    if (lt < 128) {
      const f32x2 o = tpart2[lt];
      chan_eq(mean, m2, o[0], o[1], 256.f);
      float mb, ri;
      ln_consts(mean, m2 * (1.0f / 512.0f), mb, ri);
      tmean[lt] = mb;
      trinv[lt] = ri;
    }
    __syncthreads();
    return;
  }
  __syncthreads();
  __syncthreads();
  u64* exp_ = ex + (size_t)tm * (14 * 128);
  {
    float c2[2];
#pragma unroll
    for (int nf = 0; nf < 2; ++nf) {
      const int nl = 64 + wc * 32 + nf * 16 + (lane & 15);
      c2[nf] = tcs[nl];
    }
#pragma unroll
    for (int mf = 0; mf < C::MF; ++mf) {
      const int ml = wr * 32 + mf * 16 + 4 * g;
      const f32x4 mb = *(const f32x4*)(tmean + ml), ri = *(const f32x4*)(trinv + ml);
      float pm = 0.f, pq = 0.f;
#pragma unroll
      for (int r = 0; r < 4; ++r) {
        const float a = rbf(gelu_f(rbf((acc[mf][2][r] - mb[r] * c2[0]) * ri[r])));
        const float bq = rbf(gelu_f(rbf((acc[mf][3][r] - mb[r] * c2[1]) * ri[r])));
        acc[mf][2][r] = a;
        acc[mf][3][r] = bq;
        const float mean = row16_sum(a + bq) * (1.0f / 32.0f);
        const float da = a - mean, db = bq - mean;
        const float m2 = row16_sum(fmaf(da, da, db * db));
        if ((lane & 3) == r) { pm = mean; pq = m2; }
      }
      if ((lane & 15) < 4) ((f32x2*)tpart)[wc * 128 + ml + (lane & 3)] = f32x2{pm, pq};
    }
  }
  __syncthreads();
  if (vtid() < 128) {
    f32x2 a = ((const f32x2*)tpart)[vtid()], bq = ((const f32x2*)tpart)[128 + vtid()];
    float mean = a[0], m2 = a[1];
    chan_eq(mean, m2, bq[0], bq[1], 32.f);
    __hip_atomic_store(exp_ + tn * 128 + vtid(),
                       ((u64)(__float_as_uint(m2) | 1u) << 32) | (u64)__float_as_uint(mean), __ATOMIC_RELAXED,
                       __HIP_MEMORY_SCOPE_AGENT);
  }
#pragma unroll
  for (int nf = 0; nf < 2; ++nf) {
    const int nl = wc * 32 + nf * 16 + 4 * g;
    const f32x4 c = *(const f32x4*)(tcs + nl);
#pragma unroll
    for (int mf = 0; mf < C::MF; ++mf) {
      const int ml = wr * 32 + mf * 16 + (lane & 15);
      const float mb = tmean[ml], ri = trinv[ml];
      float v[4];
#pragma unroll
      for (int r = 0; r < 4; ++r) v[r] = gelu_f(rbf((acc[mf][nf][r] - mb * c[r]) * ri));
      u32x2 ov = {pack2(v[0], v[1]), pack2(v[2], v[3])};
      ST2(z1c + ((size_t)((m0 >> 4) + wr * 2 + mf) * 56 + (n0 >> 4) + wc * 2 + nf) * 256 + lane * 4, ov);
    }
  }
  if (vtid() < 256) {
    const int t = vtid(), row = t & 127, half = t >> 7;
    const u64* gp = exp_ + half * (7 * 128) + row;
    float vm[7], vq[7];
    for (unsigned spins = 0;; ++spins) {
      bool ok = true;
#pragma unroll
      for (int j = 0; j < 7; ++j) {
        u64 gg = __hip_atomic_load(gp + j * 128, __ATOMIC_RELAXED, __HIP_MEMORY_SCOPE_AGENT);
        vm[j] = __uint_as_float((unsigned)gg);
        vq[j] = __uint_as_float((unsigned)(gg >> 32));
        ok &= ((unsigned)(gg >> 32) != 0u);
      }
      if (__all(ok)) break;
      if (spins > 4000000u) break;
      __builtin_amdgcn_s_sleep(1);
    }
    float hm = 0.f, hq = 0.f;
#pragma unroll
    for (int j = 0; j < 7; ++j) { hm += vm[j]; hq += vq[j]; }
    hm *= (1.0f / 7.0f);
#pragma unroll
    for (int j = 0; j < 7; ++j) { float d = vm[j] - hm; hq = fmaf(64.f * d, d, hq); }
    tpart2[t] = f32x2{hm, hq};
  }
  __syncthreads();
  if (vtid() < 128) {
    f32x2 a = tpart2[vtid()], bq = tpart2[128 + vtid()];
    float mean = a[0], m2 = a[1];
    chan_eq(mean, m2, bq[0], bq[1], 448.f);
    float mb, ri;
    ln_consts(mean, m2 * (1.0f / 896.0f), mb, ri);
    tmean2[vtid()] = mb;
    trinv2[vtid()] = ri;
  }
  __syncthreads();
#pragma unroll
  for (int mf = 0; mf < C::MF; ++mf) {
    const int ml = wr * 32 + mf * 16 + 4 * g;
    const f32x4 mb = *(const f32x4*)(tmean2 + ml), ri = *(const f32x4*)(trinv2 + ml);
#pragma unroll
    for (int nf = 2; nf < 4; ++nf) {
      const int n = n0 + wc * 32 + (nf & 1) * 16 + (lane & 15);
      float v0 = rbf(acc[mf][nf][0] - mb[0]) * ri[0], v1 = rbf(acc[mf][nf][1] - mb[1]) * ri[1];
      float v2 = rbf(acc[mf][nf][2] - mb[2]) * ri[2], v3 = rbf(acc[mf][nf][3] - mb[3]) * ri[3];
      u32x2 ov = {pack2(v0, v1), pack2(v2, v3)};
      ST2(z2f + fragaddr(n, m0 + ml, 64), ov);
    }
  }
}

__global__ __launch_bounds__(768, 3) void k5_sp(const bf16_t* __restrict__ wsp, const bf16_t* __restrict__ z2f,
                                                const bf16_t* __restrict__ bsp, const bf16_t* __restrict__ z1c,
                                                bf16_t* __restrict__ y) {
  constexpr int BM = 64, BN = 112, WM = 2, WK = 2, WN = 2, KT = 32, NST = (SPB5 == 2) ? 6 : 7;
  using C = GemmCfg<BM, BN, WM, WK, WN, KT, NST>;
  __shared__ __attribute__((aligned(16))) char smem[C::LDS];
  const int b = blockIdx.x, xcd = b & 7, idx = b >> 3;
  const int tm = (xcd >> 2) * 16 + (idx >> 1), tn = (xcd & 3) * 2 + (idx & 1);
  const int m0 = tm * BM, n0 = tn * BN;
  const int lane = vtid() & 63, wave = vtid() >> 6, wr = wave & 1, wk = (wave >> 1) & 1, wc = (wave >> 2) & 1;
  const bool fulln = (wc == 0);
  const int m = m0 + wr * 32 + wk * 16 + (lane & 15);
  const int nb = n0 + wc * 64;
  float bb = 0.f;
  u32x2 zv[4];
  auto pre = [&]() {
    bb = (float)bsp[m];
#pragma unroll
    for (int nf = 0; nf < 4; ++nf)
      if (nf < 3 || fulln) zv[nf] = *(const u32x2*)(z1c + ((size_t)(m >> 4) * 56 + (nb >> 4) + nf) * 256 + lane * 4);
  };
  f32x4 acc[C::MF][C::NFW], cs[C::NFW], r[C::NFW];
#pragma unroll
  for (int i = 0; i < C::MF; ++i)
#pragma unroll
    for (int j = 0; j < C::NFW; ++j) acc[i][j] = f32x4{0.f, 0.f, 0.f, 0.f};
  NoHook hook;
  if (!gemm_main<BM, BN, WM, WK, WN, KT, NST, false, true, false, false, 0>(
          smem, wsp + (size_t)m0 * S_LEN, S_LEN, z2f + (size_t)(n0 >> 4) * 64 * 512, 64, 0,
          (idx >> 1) & 7, acc, cs, hook, pre))
    return;
  splitk_combine<4>(smem, acc, r, wr + 2 * wc, wk, lane, fulln);
#pragma unroll
  for (int nf = 0; nf < C::NFW; ++nf) {
    if (nf < 3 || fulln) {
      float v0 = lo_f(zv[nf][0]) * rbf(rbf(r[nf][0]) + bb);
      float v1 = hi_f(zv[nf][0]) * rbf(rbf(r[nf][1]) + bb);
      float v2 = lo_f(zv[nf][1]) * rbf(rbf(r[nf][2]) + bb);
      float v3 = hi_f(zv[nf][1]) * rbf(rbf(r[nf][3]) + bb);
      u32x2 ov = {pack2(v0, v1), pack2(v2, v3)};
      ST2(y + fragaddr(m, nb + nf * 16 + 4 * (lane >> 4), 28), ov);
    }
  }
}

__global__ __launch_bounds__(768, 3) void k6_out(const bf16_t* __restrict__ y, const bf16_t* __restrict__ w,
                                                 const bf16_t* __restrict__ h, bf16_t* __restrict__ out) {
  constexpr int BM = 64, BN = 64, WM = 2, WK = 2, WN = 2, KT = 14, NST = (SPB6 == 2) ? 8 : 9;
  using C = GemmCfg<BM, BN, WM, WK, WN, KT, NST>;
  __shared__ __attribute__((aligned(16))) char smem[C::LDS + 8192];
  const int b = blockIdx.x, xcd = b & 7, idx = b >> 3;
  const int tm = xcd * 4 + (idx >> 3), tn = idx & 7;
  const int m0 = tm * BM, n0 = tn * BN;
  const int lane = vtid() & 63, wave = vtid() >> 6, wr = wave & 1, wk = (wave >> 1) & 1, wc = (wave >> 2) & 1;
  const int ml = wr * 32 + wk * 16 + (lane & 15);
  const int nl = wc * 32 + 4 * (lane >> 4);
  u32x2 rv0 = {0u, 0u}, rv1 = {0u, 0u};
  auto pre = [&]() {
    const bf16_t* hp = h + fragaddr(m0 + ml, n0 + nl, 16);
    rv0 = *(const u32x2*)(hp);
    rv1 = *(const u32x2*)(hp + 256);
  };
  f32x4 acc[C::MF][C::NFW], cs[C::NFW], r[C::NFW];
#pragma unroll
  for (int i = 0; i < C::MF; ++i)
#pragma unroll
    for (int j = 0; j < C::NFW; ++j) acc[i][j] = f32x4{0.f, 0.f, 0.f, 0.f};
  NoHook hook;
  if (!gemm_main<BM, BN, WM, WK, WN, KT, NST, true, false, false, false, 0>(
          smem, y + (size_t)(m0 >> 4) * 28 * 512, 28, w + n0, D_MODEL, 128, ((idx >> 3) * 3 + (idx & 7) * 2) % KT, acc, cs,
          hook, pre))
    return;
  splitk_combine<2>(smem, acc, r, wr + 2 * wc, wk, lane, true);
  u32x2 o0 = {pack2(lo_f(rv0[0]) + rbf(r[0][0]), hi_f(rv0[0]) + rbf(r[0][1])),
              pack2(lo_f(rv0[1]) + rbf(r[0][2]), hi_f(rv0[1]) + rbf(r[0][3]))};
  u32x2 o1 = {pack2(lo_f(rv1[0]) + rbf(r[1][0]), hi_f(rv1[0]) + rbf(r[1][1])),
              pack2(lo_f(rv1[1]) + rbf(r[1][2]), hi_f(rv1[1]) + rbf(r[1][3]))};
  char* os = smem + C::LDS;
  *(u32x2*)(os + ml * 128 + (((nl >> 3) ^ (ml & 7)) << 4) + (nl & 4) * 2) = o0;
  *(u32x2*)(os + ml * 128 + ((((nl + 16) >> 3) ^ (ml & 7)) << 4) + (nl & 4) * 2) = o1;
  __syncthreads();
  const int row = vtid() >> 3, ch = vtid() & 7;
  const u32x4 ov = *(const u32x4*)(os + row * 128 + ((ch ^ (row & 7)) << 4));
  *(u32x4*)(out + (size_t)(m0 + row) * D_MODEL + n0 + ch * 8) = ov;
}

#define RUNK(i)
extern "C" void kernel_launch(void* const* d_in, const int* in_sizes, int n_in, void* d_out, int out_size,
                              void* d_ws, size_t ws_size, hipStream_t stream) {
  const bf16_t* x = (const bf16_t*)d_in[0];
  const bf16_t* wqkv = (const bf16_t*)d_in[1];
  const bf16_t* wo = (const bf16_t*)d_in[2];
  const bf16_t* w_in = (const bf16_t*)d_in[4];
  const bf16_t* w_sp = (const bf16_t*)d_in[5];
  const bf16_t* b_sp = (const bf16_t*)d_in[6];
  const bf16_t* w_out = (const bf16_t*)d_in[7];
  bf16_t* out = (bf16_t*)d_out;

  char* ws = (char*)d_ws;
  size_t off = 0;
  auto take = [&](size_t bytes) { char* p = ws + off; off += (bytes + 255) & ~(size_t)255; return p; };
  bf16_t* qkf = (bf16_t*)take((size_t)S_LEN * 1024 * 2);
  bf16_t* v2 = (bf16_t*)take((size_t)512 * S_LEN * 2);
  bf16_t* o = (bf16_t*)take((size_t)S_LEN * D_MODEL * 2);
  bf16_t* h = (bf16_t*)take((size_t)S_LEN * D_MODEL * 2);
  f32x2* hstats = (f32x2*)take((size_t)S_LEN * 16 * 8);
  bf16_t* z1c = (bf16_t*)take((size_t)S_LEN * FH * 2);
  bf16_t* z2f = (bf16_t*)take((size_t)S_LEN * FH * 2);
  bf16_t* y = (bf16_t*)take((size_t)S_LEN * FH * 2);
  float* wcs = (float*)take((size_t)4 * 2 * FH * 4);
  u64* ex = (u64*)take((size_t)1024 * 1024 + 256);

  RUNK(1) k1_qkv<<<256, 1024, 0, stream>>>(x, wqkv, qkf, v2, (u32x4*)ex, wo, w_in, wcs);
  RUNK(2) k2_attn<<<256, 512, 0, stream>>>(qkf, v2, (const bf16_t*)(ex + 131072), o, w_sp, w_out);
  RUNK(3) k3_wo<<<256, 768, 0, stream>>>(o, wo, x, h, hstats);
  RUNK(4) k4_in<<<224, 768, 0, stream>>>(h, w_in, hstats, z1c, z2f, ex, wcs);
  RUNK(5) k5_sp<<<256, 768, 0, stream>>>(w_sp, z2f, b_sp, z1c, y);
  RUNK(6) k6_out<<<256, 768, 0, stream>>>(y, w_out, h, out);
}
```

```cpp
#include <hip/hip_runtime.h>
#include <hip/hip_bf16.h>
#include <stdint.h>

typedef __bf16 bf16_t;
typedef __attribute__((ext_vector_type(8))) __bf16 bf16x8;
typedef __attribute__((ext_vector_type(2))) __bf16 bf16x2;
typedef __attribute__((ext_vector_type(4))) short s16x4;
typedef __attribute__((ext_vector_type(8))) short s16x8;
typedef __attribute__((ext_vector_type(4))) float f32x4;
typedef __attribute__((ext_vector_type(2))) float f32x2;
typedef __attribute__((ext_vector_type(16))) float f32x16;
typedef __attribute__((ext_vector_type(4))) unsigned u32x4;
typedef __attribute__((ext_vector_type(2))) unsigned u32x2;
typedef unsigned long long u64;

#define DI __device__ __forceinline__

constexpr int S_LEN = 2048;
constexpr int D_MODEL = 512;
constexpr int FH = 896;

DI float rbf(float x) { return (float)(__bf16)x; }
DI unsigned pack2(float a, float b) {
  bf16x2 v = {(__bf16)a, (__bf16)b};
  return __builtin_bit_cast(unsigned, v);
}
DI float lo_f(unsigned u) { return __uint_as_float(u << 16); }
DI float hi_f(unsigned u) { return __uint_as_float(u & 0xffff0000u); }

#define ST2(p, v) (*(u32x2*)(p) = (v))
DI unsigned vtid() {
  return threadIdx.x < 256u ? threadIdx.x + 512u : (threadIdx.x < 768u ? threadIdx.x - 256u : threadIdx.x);
}
template <int N> DI void wait_vm() { asm volatile("s_waitcnt vmcnt(%0)" ::"n"(N) : "memory"); }

DI void glds16(const void* g, char* l) {
  __builtin_amdgcn_global_load_lds((const __attribute__((address_space(1))) void*)g,
                                   (__attribute__((address_space(3))) void*)l, 16, 0, 0);
}
DI s16x4 tr_read(const char* l) {
  return __builtin_amdgcn_ds_read_tr16_b64_v4i16((__attribute__((address_space(3))) s16x4*)l);
}

template <int V> struct IC { static constexpr int value = V; };
constexpr int cmin3(int a, int b, int c) { return a < b ? (a < c ? a : c) : (b < c ? b : c); }
template <int N, int I = 0, class F> DI void static_for(F& f) {
  if constexpr (I < N) {
    f(IC<I>{});
    static_for<N, I + 1>(f);
  }
}

template <int N, int PER, int I = 0, class F> DI void static_from(F& f, IC<I> = IC<I>{}) {
  if constexpr (I < N) {
    f(IC<I>{});
    static_from<N, PER, I + 1>(f);
  }
}
template <int N, int PER, class F> DI void rolled_for(F& f) {
  constexpr int NL = (PER > 0) ? (N - 4) / PER : 0;
  if constexpr (NL >= 2) {
    f(IC<0>{});
#pragma unroll 1
    for (int it = 0; it < NL; ++it) static_from<1 + PER, PER, 1>(f);
    static_from<N, PER, 1 + NL * PER>(f);
  } else {
    static_for<N>(f);
  }
}

DI int fragaddr(int m, int k, int K32) {
  return ((m >> 4) * K32 + (k >> 5)) * 512 + ((m & 15) + 16 * ((k & 31) >> 3)) * 8 + (k & 7);
}

DI int bimg(int k, int cc) {
  return k * 128 + ((((cc >> 2) & 1) ^ ((k >> 1) & 1)) << 6) + (((cc & 3) ^ (((k >> 3) & 1) << 1)) << 4);
}

#define PF4 0
#define SPB5 2
#define SPB6 2
#define SPB3 2
template <int BM, int BN, int WM, int WK, int WN, int KT, int NST>
struct GemmCfg {
  static constexpr int NW = WM * WN * WK;
  static constexpr int NL = 4;
  static constexpr int PA = BM / 8, PB = BN / 8, P = PA + PB;
  static constexpr int PPL = (P + NL - 1) / NL;
  static constexpr int STAGE = (BM + BN) * 128;
  static constexpr int MF = BM / WM / 16;
  static constexpr int NFRAG = BN / 16;
  static constexpr int NFW = (NFRAG + WN - 1) / WN;
  static constexpr int LDS = STAGE * NST;
  static constexpr bool RESIDENT = NST >= KT;
  static constexpr int SPB = (KT == 32) ? SPB5 : (KT == 14) ? SPB6 : (BN == 64) ? SPB3 : 1;
  static constexpr int KS = KT / SPB;
  static constexpr int PRE = RESIDENT ? KS : NST / SPB - 1;
  static_assert(KT % SPB == 0 && NST % SPB == 0, "");
  static constexpr int PER = (NST / SPB == 3) ? 3 : 0;
};

struct NoHook {
  DI void operator()(const char*) const {}
};

template <int BM, int BN, int WM, int WK, int WN, int KT, int NST, bool AFRAG, bool BFRAG, int PW, class Hook>
DI void gemm_loader(char* smem, const bf16_t* __restrict__ A, int lda, const bf16_t* __restrict__ B, int ldb,
                    int pstride, int lw, int rot, Hook& hook) {
  using C = GemmCfg<BM, BN, WM, WK, WN, KT, NST>;
  constexpr int NL = C::NL, PA = C::PA, STAGE = C::STAGE, PRE = C::PRE;
  static_assert(PA % NL == 0, "A pieces must be a multiple of the loader count");
  static_assert(BFRAG || (BN % 64) == 0, "row-major B needs 64-column panels");
  const int lane = vtid() & 63;
  unsigned off[PW];
#pragma unroll
  for (int i = 0; i < PW; ++i) {
    if (i < PA / NL) {
      const int p = lw + i * NL;
      if constexpr (AFRAG) {
        off[i] = (unsigned)(((p >> 1) * lda + (p & 1)) * 1024 + lane * 16);
      } else {
        off[i] = (unsigned)((8 * p + (lane >> 3)) * lda * 2 + (((lane & 7) ^ (lane >> 3)) << 4));
      }
    } else {
      const int p = lw + i * NL - PA;
      if constexpr (BFRAG) {
        off[i] = (unsigned)(((p >> 1) * ldb + (p & 1)) * 1024 + lane * 16);
      } else {
        int k = (p & 7) * 8 + (lane >> 3);
        int cc = (lane & 7) ^ (((lane >> 4) & 1) << 2) ^ ((p & 1) << 1);
        off[i] = (unsigned)(k * ldb * 2 + (p >> 3) * pstride + cc * 16);
      }
    }
  }
  rot = 0;
  int kr = __builtin_amdgcn_readfirstlane(rot), kw = 0;
  auto issue = [&](int slot) {
    char* base = smem + slot * STAGE + lw * 1024;
    const int kk = kw + kr;
    const char* pa = (const char*)A + kk * (AFRAG ? 2048 : 128);
    const char* pb = (const char*)B + (size_t)kk * (BFRAG ? 2048 : 128 * ldb);
#pragma unroll
    for (int i = 0; i < PW; ++i) {
      unsigned o = off[i];
      asm volatile("" : "+v"(o));
      glds16((i < PA / NL ? pa : pb) + o, base + i * NL * 1024);
    }
    if constexpr (KT % 8 == 0) {
      kr = (kr + 1) & 7;
      if (kr == rot) kw += 8;
    } else {
      kr = (kr + 1 == KT) ? 0 : kr + 1;
    }
  };
  constexpr int SPB = C::SPB, KS = C::KS;
  auto body = [&](auto KTC) {
    constexpr int kt = decltype(KTC)::value;
    constexpr int I0 = (kt == 0) ? 0 : cmin3(KS, kt - 1 + PRE, 2 * kt);
    constexpr int I1 = cmin3(KS, kt + PRE, 2 * kt + 2);
#pragma unroll
    for (int u = I0 * SPB; u < I1 * SPB; ++u) issue(u % NST);
    wait_vm<(I1 - kt - 1) * PW * SPB>();
    __builtin_amdgcn_s_barrier();
    asm volatile("" ::: "memory");
#pragma unroll
    for (int u = 0; u < SPB; ++u) hook(smem + ((kt * SPB + u) % NST) * STAGE);
    __builtin_amdgcn_s_waitcnt(0xC07F);
  };
  rolled_for<KS, C::PER>(body);
}

struct NoPre {
  DI void operator()() const {}
};

template <int BM, int BN, int WM, int WK, int WN, int KT, int NST, bool AFRAG, bool BFRAG, bool CS, bool FULLN,
          bool SPLITN, int PLAIN, class Pre = NoPre>
DI void gemm_compute(char* smem, int wave,
                     f32x4 (&acc)[GemmCfg<BM, BN, WM, WK, WN, KT, NST>::MF][GemmCfg<BM, BN, WM, WK, WN, KT, NST>::NFW],
                     f32x4 (&cs)[GemmCfg<BM, BN, WM, WK, WN, KT, NST>::NFW], Pre pre = Pre()) {
  using C = GemmCfg<BM, BN, WM, WK, WN, KT, NST>;
  constexpr int STAGE = C::STAGE, MF = C::MF, NFW = C::NFW;
  constexpr int NF = FULLN ? NFW : NFW - 1;
  const int lane = vtid() & 63;
  const int wr = wave % WM, wk = (wave / WM) % WK, wc = wave / (WM * WK);
  const int g = lane >> 4, q = (lane & 15) >> 2, pp = lane & 3;
  constexpr int NKS = (WK == 2) ? 1 : 2;
  int aoff[NKS];
#pragma unroll
  for (int i = 0; i < NKS; ++i) {
    const int ks = (WK == 2) ? wk : i;
    if constexpr (AFRAG) aoff[i] = (wr * MF * 2 + ks) * 1024 + lane * 16;
    else aoff[i] = (wr * (MF * 16) + (lane & 15)) * 128 + (((4 * ks + g) ^ (lane & 7)) << 4);
  }
  int boff[NF];
#pragma unroll
  for (int nf = 0; nf < NF; ++nf) {
    const int f = SPLITN ? ((nf >> 1) * 4 + 2 * wc + (nf & 1)) : (wc * NFW + nf);
    if constexpr (BFRAG) {
      boff[nf] = BM * 128 + (f * 2 + ((WK == 2) ? wk : 0)) * 1024 + lane * 16;
    } else {
      const int kk = 8 * g + q + ((WK == 2) ? wk * 32 : 0);
      boff[nf] = BM * 128 + (f >> 2) * 8192 + bimg(kk, 2 * (f & 3) + (pp >> 1)) + 8 * (pp & 1);
    }
  }
  bf16x8 ones;
#pragma unroll
  for (int j = 0; j < 8; ++j) ones[j] = (__bf16)1.0f;

  constexpr int G = NKS * C::SPB, NSTEP = KT * NKS;
  static_assert(G <= 2, "");
  bf16x8 af[3][MF], bfr[3][NF];
  auto rd = [&](auto JC) {
    constexpr int j = decltype(JC)::value;
    constexpr int kt = j / NKS, i = j % NKS;
    const char* sb = smem + (kt % NST) * STAGE;
#pragma unroll
    for (int mf = 0; mf < MF; ++mf) af[j % 3][mf] = *(const bf16x8*)(sb + aoff[i] + mf * 2048);
#pragma unroll
    for (int nf = 0; nf < NF; ++nf) {
      if constexpr (BFRAG) {
        bfr[j % 3][nf] = *(const bf16x8*)(sb + boff[nf] + i * 1024);
      } else {
        s16x4 lo = tr_read(sb + boff[nf] + i * 4096);
        s16x4 hi = tr_read(sb + boff[nf] + i * 4096 + 512);
        s16x8 v = __builtin_shufflevector(lo, hi, 0, 1, 2, 3, 4, 5, 6, 7);
        bfr[j % 3][nf] = __builtin_bit_cast(bf16x8, v);
      }
    }
  };
  auto mm = [&](auto JC) {
    constexpr int jp = decltype(JC)::value;
#pragma unroll
    for (int nf = 0; nf < NF; ++nf) {
#pragma unroll
      for (int mf = 0; mf < MF; ++mf) {
        if ((PLAIN >> nf) & 1)
          acc[mf][nf] = __builtin_amdgcn_mfma_f32_16x16x32_bf16(af[jp % 3][mf], bfr[jp % 3][nf], acc[mf][nf], 0, 0, 0);
        else
          acc[mf][nf] = __builtin_amdgcn_mfma_f32_16x16x32_bf16(bfr[jp % 3][nf], af[jp % 3][mf], acc[mf][nf], 0, 0, 0);
      }
    }
  };
  auto csum = [&](auto JC) {
    constexpr int jp = decltype(JC)::value;
    if constexpr (CS) {
      if (wr == (jp % WM)) {
#pragma unroll
        for (int nf = 0; nf < NF; ++nf) {
          if ((PLAIN >> nf) & 1) cs[nf] = __builtin_amdgcn_mfma_f32_16x16x32_bf16(ones, bfr[jp % 3][nf], cs[nf], 0, 0, 0);
          else cs[nf] = __builtin_amdgcn_mfma_f32_16x16x32_bf16(bfr[jp % 3][nf], ones, cs[nf], 0, 0, 0);
        }
      }
    }
  };
  auto group = [&](auto GC) {
    constexpr int j0 = decltype(GC)::value * G;
    constexpr int NRD = G * (MF + (BFRAG ? NF : 2 * NF)), NMM = MF * NF;
    constexpr int RPM = (NRD + NMM - 1) / NMM;
    __builtin_amdgcn_s_barrier();
    asm volatile("" ::: "memory");
    if constexpr (decltype(GC)::value == NSTEP / G - 2) pre();
    rd(IC<j0>{});
    if constexpr (G == 2) rd(IC<j0 + 1>{});
    if constexpr (j0 > 0) {
      mm(IC<j0 - 1>{});
#pragma unroll
      for (int u = 0; u < NMM; ++u) {
        __builtin_amdgcn_sched_group_barrier(0x8, 1, 0);
        __builtin_amdgcn_sched_group_barrier(0x100, RPM, 0);
      }
      __builtin_amdgcn_sched_barrier(0);
      csum(IC<j0 - 1>{});
    }
    if constexpr (G == 2) {
      __builtin_amdgcn_sched_barrier(0);
      mm(IC<j0>{});
      csum(IC<j0>{});
    }
    __builtin_amdgcn_sched_barrier(0);
    __builtin_amdgcn_s_waitcnt(0xC07F);
  };
  rolled_for<NSTEP / G, C::PER>(group);
  mm(IC<NSTEP - 1>{});
  csum(IC<NSTEP - 1>{});
}

template <int BM, int BN, int WM, int WK, int WN, int KT, int NST, bool AFRAG, bool BFRAG, bool CS, bool SPLITN,
          int PLAIN, class Hook, class Pre = NoPre>
DI bool gemm_main(char* smem, const bf16_t* __restrict__ A, int lda, const bf16_t* __restrict__ B, int ldb, int pstride,
                  int rot,
                  f32x4 (&acc)[GemmCfg<BM, BN, WM, WK, WN, KT, NST>::MF][GemmCfg<BM, BN, WM, WK, WN, KT, NST>::NFW],
                  f32x4 (&cs)[GemmCfg<BM, BN, WM, WK, WN, KT, NST>::NFW], Hook& hook, Pre pre = Pre()) {
  using C = GemmCfg<BM, BN, WM, WK, WN, KT, NST>;
  const int wave = __builtin_amdgcn_readfirstlane(vtid() >> 6);
  if (wave >= C::NW) {
    const int lw = wave - C::NW;
    __builtin_amdgcn_s_setprio(3);
    constexpr bool EVEN = (C::P % C::NL) == 0;
    if (EVEN || lw < (C::P % C::NL))
      gemm_loader<BM, BN, WM, WK, WN, KT, NST, AFRAG, BFRAG, C::PPL>(smem, A, lda, B, ldb, pstride, lw, rot, hook);
    else if constexpr (!EVEN)
      gemm_loader<BM, BN, WM, WK, WN, KT, NST, AFRAG, BFRAG, C::PPL - 1>(smem, A, lda, B, ldb, pstride, lw, rot, hook);
    return false;
  }
  const int wc = wave / (WM * WK);
  constexpr bool ALLFULL = (C::NFRAG == WN * C::NFW);
  if (ALLFULL || wc != WN - 1)
    gemm_compute<BM, BN, WM, WK, WN, KT, NST, AFRAG, BFRAG, CS, true, SPLITN, PLAIN>(smem, wave, acc, cs, pre);
  else if constexpr (!ALLFULL)
    gemm_compute<BM, BN, WM, WK, WN, KT, NST, AFRAG, BFRAG, CS, false, SPLITN, PLAIN>(smem, wave, acc, cs, pre);
  return true;
}

template <int NFW>
DI void splitk_combine(char* smem, f32x4 (&acc)[2][NFW], f32x4 (&res)[NFW], int pairid, int wk, int lane, bool fulln) {
  __syncthreads();
  char* mine = smem + (pairid * 2 + wk) * (NFW * 1024) + lane * 16;
  char* theirs = smem + (pairid * 2 + (wk ^ 1)) * (NFW * 1024) + lane * 16;
#pragma unroll
  for (int nf = 0; nf < NFW; ++nf)
    if (nf < NFW - 1 || fulln) *(f32x4*)(mine + nf * 1024) = wk ? acc[0][nf] : acc[1][nf];
  __syncthreads();
#pragma unroll
  for (int nf = 0; nf < NFW; ++nf)
    if (nf < NFW - 1 || fulln) res[nf] = (wk ? acc[1][nf] : acc[0][nf]) + *(const f32x4*)(theirs + nf * 1024);
}

DI void chan_eq(float& mean, float& m2, float mean_b, float m2_b, float n) {
  float d = mean_b - mean;
  m2 = m2 + m2_b + d * d * (0.5f * n);
  mean = 0.5f * (mean + mean_b);
}

DI void ln_consts(float mean, float var, float& mean_b, float& rinv) {
  mean_b = rbf(mean);
  float v = rbf(var);
  float t = rbf(v + 1e-5f);
  float sd = rbf(sqrtf(t));
  rinv = 1.0f / sd;
}

DI void l2_prefetch(const char* base, int nlines, int t, int nt) {
  unsigned acc = 0u;
  for (int i = t; i < nlines; i += 4 * nt) {
    unsigned v0 = *(const unsigned*)(base + (size_t)i * 128);
    unsigned v1 = (i + nt < nlines) ? *(const unsigned*)(base + (size_t)(i + nt) * 128) : 0u;
    unsigned v2 = (i + 2 * nt < nlines) ? *(const unsigned*)(base + (size_t)(i + 2 * nt) * 128) : 0u;
    unsigned v3 = (i + 3 * nt < nlines) ? *(const unsigned*)(base + (size_t)(i + 3 * nt) * 128) : 0u;
    acc ^= v0 ^ v1 ^ v2 ^ v3;
  }
  asm volatile("" ::"v"(acc));
}

DI float row16_sum(float v) {
  v += __builtin_bit_cast(float, __builtin_amdgcn_update_dpp(0, __builtin_bit_cast(int, v), 0x128, 0xf, 0xf, false));
  v += __builtin_bit_cast(float, __builtin_amdgcn_update_dpp(0, __builtin_bit_cast(int, v), 0x124, 0xf, 0xf, false));
  v += __builtin_bit_cast(float, __builtin_amdgcn_update_dpp(0, __builtin_bit_cast(int, v), 0x122, 0xf, 0xf, false));
  v += __builtin_bit_cast(float, __builtin_amdgcn_update_dpp(0, __builtin_bit_cast(int, v), 0x121, 0xf, 0xf, false));
  return v;
}

struct StatHook {
  float s1, s2;
  int o0;
  DI void operator()(const char* sb) {
    bf16x2 one2 = {(__bf16)1.0f, (__bf16)1.0f};
    u32x4 v0 = *(const u32x4*)(sb + o0);
    u32x4 v1 = *(const u32x4*)(sb + o0 + 16);
#pragma unroll
    for (int e = 0; e < 4; ++e) {
      const unsigned ua = v0[e], ub = v1[e];
      bf16x2 a = __builtin_bit_cast(bf16x2, ua), b = __builtin_bit_cast(bf16x2, ub);
      s1 = __builtin_amdgcn_fdot2_f32_bf16(a, one2, s1, false);
      s2 = __builtin_amdgcn_fdot2_f32_bf16(a, a, s2, false);
      s1 = __builtin_amdgcn_fdot2_f32_bf16(b, one2, s1, false);
      s2 = __builtin_amdgcn_fdot2_f32_bf16(b, b, s2, false);
    }
  }
};

__global__ __launch_bounds__(1024, 4) void k1_qkv(const bf16_t* __restrict__ x, const bf16_t* __restrict__ w,
                                                 bf16_t* __restrict__ qkf, bf16_t* __restrict__ v2,
                                                 u32x4* __restrict__ exz, const bf16_t* __restrict__ wo,
                                                 const bf16_t* __restrict__ win, float* __restrict__ wcs) {
  constexpr int BM = 64, BN = 192, WM = 2, WK = 1, WN = 4, KT = 8, NST = 4;
  using C = GemmCfg<BM, BN, WM, WK, WN, KT, NST>;
  __shared__ __attribute__((aligned(16))) char smem[C::LDS + 2048];
  if (vtid() < 256) exz[blockIdx.x * 256 + vtid()] = u32x4{0u, 0u, 0u, 0u};
  if (blockIdx.x == 0 && vtid() < 2) {
    const unsigned cv = vtid() ? 0u : 0x3f803f80u;
    exz[65536 + vtid()] = u32x4{cv, cv, cv, cv};
  }

  const int b = blockIdx.x, xcd = b & 7, idx = b >> 3;
  const int tm = xcd * 4 + (idx >> 3), tn = idx & 7;
  const int m0 = tm * BM, n0 = tn * BN;
  f32x4 acc[C::MF][C::NFW], cs[C::NFW];
#pragma unroll
  for (int i = 0; i < C::MF; ++i)
#pragma unroll
    for (int j = 0; j < C::NFW; ++j) acc[i][j] = f32x4{0.f, 0.f, 0.f, 0.f};
#pragma unroll
  for (int j = 0; j < C::NFW; ++j) cs[j] = f32x4{0.f, 0.f, 0.f, 0.f};
  StatHook hook;
  const int srow = (vtid() >> 2) & 63, sq = vtid() & 3;
  hook.s1 = 0.f; hook.s2 = 0.f;
  hook.o0 = srow * 128 + (sq << 5);
  const int lane = vtid() & 63, wave = __builtin_amdgcn_readfirstlane(vtid() >> 6), wr = wave & 1,
            wc = (wave >> 1) & 3;
  const int nw0 = n0 + wc * 48;
  const int plain = (nw0 >= 1024) ? 7 : ((nw0 + 16 >= 1024) ? 6 : 0);
  const bool comp = wave < 8;
  if (wave >= 12) {
#pragma unroll
    for (int kt = 0; kt < KT; ++kt) {
      __builtin_amdgcn_s_barrier();
      asm volatile("" ::: "memory");
      hook(smem + (kt % NST) * C::STAGE);
      __builtin_amdgcn_s_waitcnt(0xC07F);
    }
  } else if (wave >= 8) {
    __builtin_amdgcn_s_setprio(3);
    NoHook nh;
    gemm_loader<BM, BN, WM, WK, WN, KT, NST, false, false, C::PPL>(smem, x + (size_t)m0 * D_MODEL, D_MODEL, w + n0,
                                                                   3 * D_MODEL, 128, wave - 8, 0, nh);
    {
      const int lt = vtid() - 512;
      if (lt < 72) {
        const char* p0 = (lt < 16) ? (const char*)wo + (size_t)(b * 16 + lt) * 128
                                   : (const char*)win + (size_t)(b * 56 + lt - 16) * 128;
        unsigned v0 = *(const unsigned*)p0;
        asm volatile("" ::"v"(v0));
      }
      if (b < 28) {
        const bf16_t* wp = win + (size_t)((lt >> 3) * 16) * (2 * FH) + b * 64 + (lt & 7) * 8;
        float a[8];
#pragma unroll
        for (int e = 0; e < 8; ++e) a[e] = 0.f;
#pragma unroll
        for (int i = 0; i < 16; ++i) {
          const u32x4 v = *(const u32x4*)(wp + (size_t)i * (2 * FH));
#pragma unroll
          for (int e = 0; e < 4; ++e) { a[2 * e] += lo_f(v[e]); a[2 * e + 1] += hi_f(v[e]); }
        }
#pragma unroll
        for (int e = 0; e < 8; ++e) {
          a[e] += __shfl_xor(a[e], 8);
          a[e] += __shfl_xor(a[e], 16);
          a[e] += __shfl_xor(a[e], 32);
        }
        if ((lt & 63) < 8) {
          float* dp = wcs + (lt >> 6) * (2 * FH) + b * 64 + (lt & 7) * 8;
          *(f32x4*)dp = f32x4{a[0], a[1], a[2], a[3]};
          *(f32x4*)(dp + 4) = f32x4{a[4], a[5], a[6], a[7]};
        }
      }
    }
    return;
  } else if (plain == 7) {
    gemm_compute<BM, BN, WM, WK, WN, KT, NST, false, false, true, true, false, 7>(smem, wave, acc, cs);
  } else if (plain == 6) {
    gemm_compute<BM, BN, WM, WK, WN, KT, NST, false, false, true, true, false, 6>(smem, wave, acc, cs);
  } else {
    gemm_compute<BM, BN, WM, WK, WN, KT, NST, false, false, true, true, false, 0>(smem, wave, acc, cs);
  }

  float* tmean = (float*)(smem + C::LDS);
  float* trinv = tmean + 64;
  float* tcs = trinv + 64;
  if (!comp) {
    float s1 = hook.s1 + __shfl_xor(hook.s1, 1);
    float s2 = hook.s2 + __shfl_xor(hook.s2, 1);
    s1 += __shfl_xor(s1, 2);
    s2 += __shfl_xor(s2, 2);
    const float mean = s1 * (1.0f / 512.0f), ex2 = s2 * (1.0f / 512.0f);
    float var = ex2 - mean * mean;
    if (var < ex2 * (1.0f / 1024.0f)) {
      const bf16_t* xr = x + (size_t)(m0 + srow) * D_MODEL + sq * 128;
      float a2 = 0.f;
      for (int j = 0; j < 16; ++j) {
        u32x4 v = *(const u32x4*)(xr + j * 8);
#pragma unroll
        for (int e = 0; e < 4; ++e) {
          float d0 = lo_f(v[e]) - mean, d1 = hi_f(v[e]) - mean;
          a2 = fmaf(d0, d0, a2);
          a2 = fmaf(d1, d1, a2);
        }
      }
      a2 += __shfl_xor(a2, 1);
      a2 += __shfl_xor(a2, 2);
      var = a2 * (1.0f / 512.0f);
    }
    float mb, ri;
    ln_consts(mean, var, mb, ri);
    if (sq == 0) { tmean[srow] = mb; trinv[srow] = ri; }
    __syncthreads();
    return;
  }
  const int g = lane >> 4;
#pragma unroll
  for (int nf = 0; nf < C::NFW; ++nf) {
    if ((plain >> nf) & 1) {
      if (lane < 16) tcs[wr * 192 + wc * 48 + nf * 16 + lane] = cs[nf][0];
    } else if ((lane & 15) == 0) {
      *(f32x4*)(tcs + wr * 192 + wc * 48 + nf * 16 + 4 * g) = cs[nf];
    }
  }
  __syncthreads();
#pragma unroll
  for (int nf = 0; nf < C::NFW; ++nf) {
    if ((plain >> nf) & 1) {
      const int nl = wc * 48 + nf * 16 + (lane & 15);
      const float c = tcs[nl] + tcs[192 + nl];
      const int nv = n0 + nl - 1024;
#pragma unroll
      for (int mf = 0; mf < C::MF; ++mf) {
        const int ml = wr * 32 + mf * 16 + 4 * g;
        const f32x4 mb = *(const f32x4*)(tmean + ml), ri = *(const f32x4*)(trinv + ml);
        u32x2 o = {pack2((acc[mf][nf][0] - mb[0] * c) * ri[0], (acc[mf][nf][1] - mb[1] * c) * ri[1]),
                   pack2((acc[mf][nf][2] - mb[2] * c) * ri[2], (acc[mf][nf][3] - mb[3] * c) * ri[3])};
        const int m = m0 + ml;
        ST2(v2 + (((size_t)(nv >> 4) * 256 + (m >> 3)) * 16 + (nv & 15)) * 8 + (m & 7), o);
      }
    } else {
      const int nl = wc * 48 + nf * 16 + 4 * g;
      f32x4 c = *(const f32x4*)(tcs + nl) + *(const f32x4*)(tcs + 192 + nl);
      const int n = n0 + nl;
#pragma unroll
      for (int mf = 0; mf < C::MF; ++mf) {
        const int ml = wr * 32 + mf * 16 + (lane & 15);
        const float mb = tmean[ml], ri = trinv[ml];
        u32x2 o = {pack2((acc[mf][nf][0] - mb * c[0]) * ri, (acc[mf][nf][1] - mb * c[1]) * ri),
                   pack2((acc[mf][nf][2] - mb * c[2]) * ri, (acc[mf][nf][3] - mb * c[3]) * ri)};
        ST2(qkf + ((size_t)(n >> 3) * S_LEN + m0 + ml) * 8 + (n & 7), o);
      }
    }
  }
}

template <bool EDGE>
DI void attn_body(const bf16_t* __restrict__ qkf, const bf16_t* __restrict__ v2, const bf16_t* __restrict__ cst,
                  bf16_t* __restrict__ o, int qt, int h, const char* __restrict__ pf) {
  const int lane = threadIdx.x & 63;
  const int r = lane & 31, hf = lane >> 5;
  const int q0 = qt * 32;
  const int t0 = EDGE ? 8 - qt : 0;

  const bf16x8 qf = *(const bf16x8*)(qkf + ((size_t)(h * 2 + hf) * S_LEN + q0 + r) * 8);
  const int pr = 16 * ((r >> 2) & 1) + 4 * (r >> 3) + (r & 3);
  const bf16_t* kp = qkf + ((size_t)(64 + h * 2 + hf) * S_LEN + pr) * 8;
  bf16x8 kf[9];
#pragma unroll
  for (int t = 0; t < 9; ++t) {
    int kb = q0 - 256 + 32 * t;
    if (EDGE && kb < 0) kb = 0;
    kf[t] = *(const bf16x8*)(kp + kb * 8);
  }
  bf16x8 vf[9][2];
  const bf16_t* vp = (r < 16) ? v2 + ((size_t)h * 256 * 16 + 2 * hf * 16 + r) * 8 : cst + ((r == 16) ? 0 : 8);
  const int vm = (r < 16) ? 16 : 0;
#pragma unroll
  for (int t = 0; t < 9; ++t) {
    int kb = q0 - 256 + 32 * t;
    if (EDGE && kb < 0) kb = 0;
    vf[t][0] = *(const bf16x8*)(vp + kb * vm);
    vf[t][1] = *(const bf16x8*)(vp + (kb + 8) * vm);
  }
  const unsigned touch = *(const unsigned*)pf;
  f32x16 s[9];
  f32x16 zero;
#pragma unroll
  for (int i = 0; i < 16; ++i) zero[i] = 0.f;
#pragma unroll
  for (int t = 0; t < 9; ++t) s[t] = __builtin_amdgcn_mfma_f32_32x32x16_bf16(kf[t], qf, zero, 0, 0, 0);

  const float NEG = -3.0e38f;
#pragma unroll
  for (int i = 0; i < 16; ++i) {
    if (!(16 * hf + i > r)) s[0][i] = NEG;
    if (!(16 * hf + i <= r)) s[8][i] = NEG;
  }
  if constexpr (EDGE) {
#pragma unroll
    for (int t = 0; t < 8; ++t) {
      if (t < t0) {
#pragma unroll
        for (int i = 0; i < 16; ++i) s[t][i] = NEG;
      }
    }
  }
  float mx = NEG;
#pragma unroll
  for (int t = 0; t < 9; ++t)
#pragma unroll
    for (int i = 0; i < 16; ++i) mx = fmaxf(mx, s[t][i]);
  mx = fmaxf(mx, __shfl_xor(mx, 32));
  const float c = 0.25f * 1.4426950408889634f;
  const f32x2 c2 = {c, c}, mc2 = {-mx * c, -mx * c};
  f32x16 oacc = zero;
#pragma unroll
  for (int t = 0; t < 9; ++t) {
#pragma unroll
    for (int st = 0; st < 2; ++st) {
      bf16x8 pf;
#pragma unroll
      for (int j = 0; j < 8; j += 2) {
        f32x2 e = {s[t][8 * st + j], s[t][8 * st + j + 1]};
        e = __builtin_elementwise_fma(e, c2, mc2);
        pf[j] = (__bf16)__builtin_amdgcn_exp2f(e[0]);
        pf[j + 1] = (__bf16)__builtin_amdgcn_exp2f(e[1]);
      }
      oacc = __builtin_amdgcn_mfma_f32_32x32x16_bf16(vf[t][st], pf, oacc, 0, 0, 0);
    }
  }
  const float l = __shfl(oacc[8], r);
  const float il = 1.0f / l;
  bf16_t* op = o + fragaddr(q0 + r, h * 16 + 4 * hf, 16);
  u32x2 o0 = {pack2(oacc[0] * il, oacc[1] * il), pack2(oacc[2] * il, oacc[3] * il)};
  u32x2 o1 = {pack2(oacc[4] * il, oacc[5] * il), pack2(oacc[6] * il, oacc[7] * il)};
  ST2(op, o0);
  ST2(op + 128, o1);
  asm volatile("" ::"v"(touch));
}

__global__ __launch_bounds__(512) void k2_attn(const bf16_t* __restrict__ qkf, const bf16_t* __restrict__ v2,
                                               const bf16_t* __restrict__ cst, bf16_t* __restrict__ o,
                                               const bf16_t* __restrict__ wsp, const bf16_t* __restrict__ wout) {
  const int b = blockIdx.x, xcd = b & 7, idx = b >> 3;
  const int qt = xcd * 8 + (idx >> 2), hg = idx & 3;
  const int h = hg * 8 + (threadIdx.x >> 6);
  const int gl = b * 512 + threadIdx.x;
  const char* pf = (gl < 65536) ? (const char*)wsp + (size_t)gl * 128
                                : (const char*)wout + (size_t)((gl - 65536) % 7168) * 128;
  if (qt < 8) attn_body<true>(qkf, v2, cst, o, qt, h, pf);
  else attn_body<false>(qkf, v2, cst, o, qt, h, pf);
}

__global__ __launch_bounds__(768, 3) void k3_wo(const bf16_t* __restrict__ o, const bf16_t* __restrict__ w,
                                                const bf16_t* __restrict__ x, bf16_t* __restrict__ h,
                                                f32x2* __restrict__ stats) {
  constexpr int BM = 64, BN = 64, WM = 2, WK = 2, WN = 2, KT = 8, NST = 8;
  using C = GemmCfg<BM, BN, WM, WK, WN, KT, NST>;
  __shared__ __attribute__((aligned(16))) char smem[C::LDS + 8192];
  const int b = blockIdx.x, xcd = b & 7, idx = b >> 3;
  const int tm = xcd * 4 + (idx >> 3), tn = idx & 7;
  const int m0 = tm * BM, n0 = tn * BN;
  const int lane = vtid() & 63, wave = vtid() >> 6, wr = wave & 1, wk = (wave >> 1) & 1, wc = (wave >> 2) & 1;
  char* xs = smem + C::LDS;
  if (wave >= 8) {
    const int lw = __builtin_amdgcn_readfirstlane(wave) - 8;
#pragma unroll
    for (int i = 0; i < 2; ++i) {
      const int p = lw + 4 * i;
      glds16((const char*)(x + (size_t)(m0 + 8 * p + (lane >> 3)) * D_MODEL + n0) + (((lane & 7) ^ (lane >> 3)) << 4),
             xs + p * 1024);
    }
  }
  f32x4 acc[C::MF][C::NFW], cs[C::NFW], r[C::NFW];
#pragma unroll
  for (int i = 0; i < C::MF; ++i)
#pragma unroll
    for (int j = 0; j < C::NFW; ++j) acc[i][j] = f32x4{0.f, 0.f, 0.f, 0.f};
  NoHook hook;
  if (!gemm_main<BM, BN, WM, WK, WN, KT, NST, true, false, false, false, 0>(
          smem, o + (size_t)m0 * D_MODEL, 16, w + n0, D_MODEL, 128, (2 * (idx >> 3) + (idx & 7)) & 7, acc, cs, hook)) {
    return;
  }
  splitk_combine<2>(smem, acc, r, wr + 2 * wc, wk, lane, true);
  const int ml = wr * 32 + wk * 16 + (lane & 15);
  const int nl = wc * 32 + 4 * (lane >> 4);
  const u32x2 rv0 = *(const u32x2*)(xs + ml * 128 + (((nl >> 3) ^ (ml & 7)) << 4) + (nl & 4) * 2);
  const u32x2 rv1 = *(const u32x2*)(xs + ml * 128 + ((((nl + 16) >> 3) ^ (ml & 7)) << 4) + (nl & 4) * 2);
  float hv[8];
  hv[0] = rbf(lo_f(rv0[0]) + rbf(r[0][0])); hv[1] = rbf(hi_f(rv0[0]) + rbf(r[0][1]));
  hv[2] = rbf(lo_f(rv0[1]) + rbf(r[0][2])); hv[3] = rbf(hi_f(rv0[1]) + rbf(r[0][3]));
  hv[4] = rbf(lo_f(rv1[0]) + rbf(r[1][0])); hv[5] = rbf(hi_f(rv1[0]) + rbf(r[1][1]));
  hv[6] = rbf(lo_f(rv1[1]) + rbf(r[1][2])); hv[7] = rbf(hi_f(rv1[1]) + rbf(r[1][3]));
  u32x2 o0 = {pack2(hv[0], hv[1]), pack2(hv[2], hv[3])};
  u32x2 o1 = {pack2(hv[4], hv[5]), pack2(hv[6], hv[7])};
  bf16_t* hp = h + fragaddr(m0 + ml, n0 + nl, 16);
  ST2(hp, o0);
  ST2(hp + 256, o1);
  float mean = 0.f;
#pragma unroll
  for (int j = 0; j < 8; ++j) mean += hv[j];
  mean *= 0.125f;
  float m2 = 0.f;
#pragma unroll
  for (int j = 0; j < 8; ++j) { float d = hv[j] - mean; m2 = fmaf(d, d, m2); }
  chan_eq(mean, m2, __shfl_xor(mean, 16), __shfl_xor(m2, 16), 8.f);
  chan_eq(mean, m2, __shfl_xor(mean, 32), __shfl_xor(m2, 32), 16.f);
  if (lane < 16) stats[(size_t)(tn * 2 + wc) * S_LEN + m0 + ml] = f32x2{mean, m2};
}

DI float gelu_f(float x) {
  const float k0 = 0.7978845608028654f * 2.0f * 1.4426950408889634f;
  float y = k0 * fmaf(0.044715f * x * x, x, x);
  float e = __builtin_amdgcn_exp2f(-y);
  return x * __builtin_amdgcn_rcpf(1.0f + e);
}

__global__ __launch_bounds__(768, 3) void k4_in(const bf16_t* __restrict__ h, const bf16_t* __restrict__ w,
                                                const f32x2* __restrict__ hstats, bf16_t* __restrict__ z1c,
                                                bf16_t* __restrict__ z2f, u64* __restrict__ ex,
                                                const float* __restrict__ wcs) {
  constexpr int BM = 128, BN = 128, WM = 4, WK = 1, WN = 2, KT = 8, NST = 4;
  using C = GemmCfg<BM, BN, WM, WK, WN, KT, NST>;
  __shared__ __attribute__((aligned(16))) char smem[C::LDS + 8192];
  const int b = blockIdx.x, xcd = b & 7, idx = b >> 3;
  const int tm = xcd * 2 + (idx >= 14 ? 1 : 0), tn = (idx >= 14) ? idx - 14 : idx;
  const int m0 = tm * BM, n0 = tn * 64;
  const int lane = vtid() & 63, wave = vtid() >> 6, wr = wave & 3, wc = (wave >> 2) & 1;
  const int g = lane >> 4;
  float* tmean = (float*)(smem + C::LDS);
  float* trinv = tmean + 128;
  float* tcs = trinv + 128;
  float* tmean2 = tcs + 512;
  float* trinv2 = tmean2 + 128;
  float* tpart = trinv2 + 128;
  f32x2* tpart2 = (f32x2*)(tpart + 512);
  f32x4 acc[C::MF][C::NFW], cs[C::NFW];
#pragma unroll
  for (int i = 0; i < C::MF; ++i)
#pragma unroll
    for (int j = 0; j < C::NFW; ++j) acc[i][j] = f32x4{0.f, 0.f, 0.f, 0.f};
#pragma unroll
  for (int j = 0; j < C::NFW; ++j) cs[j] = f32x4{0.f, 0.f, 0.f, 0.f};
  f32x2 hv[8];
  float cv[4];
  if (vtid() >= 512) {
    const f32x2* hp = hstats + (size_t)((vtid() >> 7) & 1) * 8 * S_LEN + m0 + (vtid() & 127);
#pragma unroll
    for (int j = 0; j < 8; ++j) hv[j] = hp[(size_t)j * S_LEN];
    const float* cp = wcs + n0 + (vtid() & 63) + ((vtid() >> 6) & 1) * FH;
#pragma unroll
    for (int j = 0; j < 4; ++j) cv[j] = cp[j * 2 * FH];
  }
  NoHook hook;
  const bool comp = gemm_main<BM, BN, WM, WK, WN, KT, NST, true, false, false, true, 12>(
      smem, h + (size_t)m0 * D_MODEL, 16, w + n0, 2 * FH, FH * 2, ((idx >= 14 ? 4 : 0) + tn) & 7, acc, cs, hook);
  if (!comp) {
    const int lt = vtid() - 512;
    float mean = 0.f;
#pragma unroll
    for (int j = 0; j < 8; ++j) mean += hv[j][0];
    mean *= 0.125f;
    float m2 = 0.f;
#pragma unroll
    for (int j = 0; j < 8; ++j) { float d = hv[j][0] - mean; m2 += hv[j][1] + 32.f * d * d; }
    if (lt >= 128) tpart2[lt - 128] = f32x2{mean, m2};
    else tcs[lt] = (cv[0] + cv[1]) + (cv[2] + cv[3]);
    __syncthreads();
    if (lt < 128) {
      const f32x2 o = tpart2[lt];
      chan_eq(mean, m2, o[0], o[1], 256.f);
      float mb, ri;
      ln_consts(mean, m2 * (1.0f / 512.0f), mb, ri);
      tmean[lt] = mb;
      trinv[lt] = ri;
    }
    __syncthreads();
    return;
  }
  __syncthreads();
  __syncthreads();
  u64* exp_ = ex + (size_t)tm * (14 * 128);
  {
    float c2[2];
#pragma unroll
    for (int nf = 0; nf < 2; ++nf) {
      const int nl = 64 + wc * 32 + nf * 16 + (lane & 15);
      c2[nf] = tcs[nl];
    }
#pragma unroll
    for (int mf = 0; mf < C::MF; ++mf) {
      const int ml = wr * 32 + mf * 16 + 4 * g;
      const f32x4 mb = *(const f32x4*)(tmean + ml), ri = *(const f32x4*)(trinv + ml);
      float pm = 0.f, pq = 0.f;
#pragma unroll
      for (int r = 0; r < 4; ++r) {
        const float a = rbf(gelu_f(rbf((acc[mf][2][r] - mb[r] * c2[0]) * ri[r])));
        const float bq = rbf(gelu_f(rbf((acc[mf][3][r] - mb[r] * c2[1]) * ri[r])));
        acc[mf][2][r] = a;
        acc[mf][3][r] = bq;
        const float mean = row16_sum(a + bq) * (1.0f / 32.0f);
        const float da = a - mean, db = bq - mean;
        const float m2 = row16_sum(fmaf(da, da, db * db));
        if ((lane & 3) == r) { pm = mean; pq = m2; }
      }
      if ((lane & 15) < 4) ((f32x2*)tpart)[wc * 128 + ml + (lane & 3)] = f32x2{pm, pq};
    }
  }
  __syncthreads();
  if (vtid() < 128) {
    f32x2 a = ((const f32x2*)tpart)[vtid()], bq = ((const f32x2*)tpart)[128 + vtid()];
    float mean = a[0], m2 = a[1];
    chan_eq(mean, m2, bq[0], bq[1], 32.f);
    __hip_atomic_store(exp_ + tn * 128 + vtid(),
                       ((u64)(__float_as_uint(m2) | 1u) << 32) | (u64)__float_as_uint(mean), __ATOMIC_RELAXED,
                       __HIP_MEMORY_SCOPE_AGENT);
  }
#pragma unroll
  for (int nf = 0; nf < 2; ++nf) {
    const int nl = wc * 32 + nf * 16 + 4 * g;
    const f32x4 c = *(const f32x4*)(tcs + nl);
#pragma unroll
    for (int mf = 0; mf < C::MF; ++mf) {
      const int ml = wr * 32 + mf * 16 + (lane & 15);
      const float mb = tmean[ml], ri = trinv[ml];
      float v[4];
#pragma unroll
      for (int r = 0; r < 4; ++r) v[r] = gelu_f(rbf((acc[mf][nf][r] - mb * c[r]) * ri));
      u32x2 ov = {pack2(v[0], v[1]), pack2(v[2], v[3])};
      ST2(z1c + ((size_t)((m0 >> 4) + wr * 2 + mf) * 56 + (n0 >> 4) + wc * 2 + nf) * 256 + lane * 4, ov);
    }
  }
  if (vtid() < 256) {
    const int t = vtid(), row = t & 127, half = t >> 7;
    const u64* gp = exp_ + half * (7 * 128) + row;
    float vm[7], vq[7];
    for (unsigned spins = 0;; ++spins) {
      bool ok = true;
#pragma unroll
      for (int j = 0; j < 7; ++j) {
        u64 gg = __hip_atomic_load(gp + j * 128, __ATOMIC_RELAXED, __HIP_MEMORY_SCOPE_AGENT);
        vm[j] = __uint_as_float((unsigned)gg);
        vq[j] = __uint_as_float((unsigned)(gg >> 32));
        ok &= ((unsigned)(gg >> 32) != 0u);
      }
      if (__all(ok)) break;
      if (spins > 4000000u) break;
      __builtin_amdgcn_s_sleep(1);
    }
    float hm = 0.f, hq = 0.f;
#pragma unroll
    for (int j = 0; j < 7; ++j) { hm += vm[j]; hq += vq[j]; }
    hm *= (1.0f / 7.0f);
#pragma unroll
    for (int j = 0; j < 7; ++j) { float d = vm[j] - hm; hq = fmaf(64.f * d, d, hq); }
    tpart2[t] = f32x2{hm, hq};
  }
  __syncthreads();
  if (vtid() < 128) {
    f32x2 a = tpart2[vtid()], bq = tpart2[128 + vtid()];
    float mean = a[0], m2 = a[1];
    chan_eq(mean, m2, bq[0], bq[1], 448.f);
    float mb, ri;
    ln_consts(mean, m2 * (1.0f / 896.0f), mb, ri);
    tmean2[vtid()] = mb;
    trinv2[vtid()] = ri;
  }
  __syncthreads();
#pragma unroll
  for (int mf = 0; mf < C::MF; ++mf) {
    const int ml = wr * 32 + mf * 16 + 4 * g;
    const f32x4 mb = *(const f32x4*)(tmean2 + ml), ri = *(const f32x4*)(trinv2 + ml);
#pragma unroll
    for (int nf = 2; nf < 4; ++nf) {
      const int n = n0 + wc * 32 + (nf & 1) * 16 + (lane & 15);
      float v0 = rbf(acc[mf][nf][0] - mb[0]) * ri[0], v1 = rbf(acc[mf][nf][1] - mb[1]) * ri[1];
      float v2 = rbf(acc[mf][nf][2] - mb[2]) * ri[2], v3 = rbf(acc[mf][nf][3] - mb[3]) * ri[3];
      u32x2 ov = {pack2(v0, v1), pack2(v2, v3)};
      ST2(z2f + fragaddr(n, m0 + ml, 64), ov);
    }
  }
}

__global__ __launch_bounds__(768, 3) void k5_sp(const bf16_t* __restrict__ wsp, const bf16_t* __restrict__ z2f,
                                                const bf16_t* __restrict__ bsp, const bf16_t* __restrict__ z1c,
                                                bf16_t* __restrict__ y) {
  constexpr int BM = 64, BN = 112, WM = 2, WK = 2, WN = 2, KT = 32, NST = (SPB5 == 2) ? 6 : 7;
  using C = GemmCfg<BM, BN, WM, WK, WN, KT, NST>;
  __shared__ __attribute__((aligned(16))) char smem[C::LDS];
  const int b = blockIdx.x, xcd = b & 7, idx = b >> 3;
  const int tm = (xcd >> 2) * 16 + (idx >> 1), tn = (xcd & 3) * 2 + (idx & 1);
  const int m0 = tm * BM, n0 = tn * BN;
  const int lane = vtid() & 63, wave = vtid() >> 6, wr = wave & 1, wk = (wave >> 1) & 1, wc = (wave >> 2) & 1;
  const bool fulln = (wc == 0);
  const int m = m0 + wr * 32 + wk * 16 + (lane & 15);
  const int nb = n0 + wc * 64;
  float bb = 0.f;
  u32x2 zv[4];
  auto pre = [&]() {
    bb = (float)bsp[m];
#pragma unroll
    for (int nf = 0; nf < 4; ++nf)
      if (nf < 3 || fulln) zv[nf] = *(const u32x2*)(z1c + ((size_t)(m >> 4) * 56 + (nb >> 4) + nf) * 256 + lane * 4);
  };
  f32x4 acc[C::MF][C::NFW], cs[C::NFW], r[C::NFW];
#pragma unroll
  for (int i = 0; i < C::MF; ++i)
#pragma unroll
    for (int j = 0; j < C::NFW; ++j) acc[i][j] = f32x4{0.f, 0.f, 0.f, 0.f};
  NoHook hook;
  if (!gemm_main<BM, BN, WM, WK, WN, KT, NST, false, true, false, false, 0>(
          smem, wsp + (size_t)m0 * S_LEN, S_LEN, z2f + (size_t)(n0 >> 4) * 64 * 512, 64, 0,
          (idx >> 1) & 7, acc, cs, hook, pre))
    return;
  splitk_combine<4>(smem, acc, r, wr + 2 * wc, wk, lane, fulln);
#pragma unroll
  for (int nf = 0; nf < C::NFW; ++nf) {
    if (nf < 3 || fulln) {
      float v0 = lo_f(zv[nf][0]) * rbf(rbf(r[nf][0]) + bb);
      float v1 = hi_f(zv[nf][0]) * rbf(rbf(r[nf][1]) + bb);
      float v2 = lo_f(zv[nf][1]) * rbf(rbf(r[nf][2]) + bb);
      float v3 = hi_f(zv[nf][1]) * rbf(rbf(r[nf][3]) + bb);
      u32x2 ov = {pack2(v0, v1), pack2(v2, v3)};
      ST2(y + fragaddr(m, nb + nf * 16 + 4 * (lane >> 4), 28), ov);
    }
  }
}

__global__ __launch_bounds__(768, 3) void k6_out(const bf16_t* __restrict__ y, const bf16_t* __restrict__ w,
                                                 const bf16_t* __restrict__ h, bf16_t* __restrict__ out) {
  constexpr int BM = 64, BN = 64, WM = 2, WK = 2, WN = 2, KT = 14, NST = (SPB6 == 2) ? 8 : 9;
  using C = GemmCfg<BM, BN, WM, WK, WN, KT, NST>;
  __shared__ __attribute__((aligned(16))) char smem[C::LDS + 8192];
  const int b = blockIdx.x, xcd = b & 7, idx = b >> 3;
  const int tm = xcd * 4 + (idx >> 3), tn = idx & 7;
  const int m0 = tm * BM, n0 = tn * BN;
  const int lane = vtid() & 63, wave = vtid() >> 6, wr = wave & 1, wk = (wave >> 1) & 1, wc = (wave >> 2) & 1;
  const int ml = wr * 32 + wk * 16 + (lane & 15);
  const int nl = wc * 32 + 4 * (lane >> 4);
  u32x2 rv0 = {0u, 0u}, rv1 = {0u, 0u};
  auto pre = [&]() {
    const bf16_t* hp = h + fragaddr(m0 + ml, n0 + nl, 16);
    rv0 = *(const u32x2*)(hp);
    rv1 = *(const u32x2*)(hp + 256);
  };
  f32x4 acc[C::MF][C::NFW], cs[C::NFW], r[C::NFW];
#pragma unroll
  for (int i = 0; i < C::MF; ++i)
#pragma unroll
    for (int j = 0; j < C::NFW; ++j) acc[i][j] = f32x4{0.f, 0.f, 0.f, 0.f};
  NoHook hook;
  if (!gemm_main<BM, BN, WM, WK, WN, KT, NST, true, false, false, false, 0>(
          smem, y + (size_t)(m0 >> 4) * 28 * 512, 28, w + n0, D_MODEL, 128, ((idx >> 3) * 3 + (idx & 7) * 2) % KT, acc, cs,
          hook, pre))
    return;
  splitk_combine<2>(smem, acc, r, wr + 2 * wc, wk, lane, true);
  u32x2 o0 = {pack2(lo_f(rv0[0]) + rbf(r[0][0]), hi_f(rv0[0]) + rbf(r[0][1])),
              pack2(lo_f(rv0[1]) + rbf(r[0][2]), hi_f(rv0[1]) + rbf(r[0][3]))};
  u32x2 o1 = {pack2(lo_f(rv1[0]) + rbf(r[1][0]), hi_f(rv1[0]) + rbf(r[1][1])),
              pack2(lo_f(rv1[1]) + rbf(r[1][2]), hi_f(rv1[1]) + rbf(r[1][3]))};
  char* os = smem + C::LDS;
  *(u32x2*)(os + ml * 128 + (((nl >> 3) ^ (ml & 7)) << 4) + (nl & 4) * 2) = o0;
  *(u32x2*)(os + ml * 128 + ((((nl + 16) >> 3) ^ (ml & 7)) << 4) + (nl & 4) * 2) = o1;
  __syncthreads();
  const int row = vtid() >> 3, ch = vtid() & 7;
  const u32x4 ov = *(const u32x4*)(os + row * 128 + ((ch ^ (row & 7)) << 4));
  *(u32x4*)(out + (size_t)(m0 + row) * D_MODEL + n0 + ch * 8) = ov;
}

#define RUNK(i)
extern "C" void kernel_launch(void* const* d_in, const int* in_sizes, int n_in, void* d_out, int out_size,
                              void* d_ws, size_t ws_size, hipStream_t stream) {
  const bf16_t* x = (const bf16_t*)d_in[0];
  const bf16_t* wqkv = (const bf16_t*)d_in[1];
  const bf16_t* wo = (const bf16_t*)d_in[2];
  const bf16_t* w_in = (const bf16_t*)d_in[4];
  const bf16_t* w_sp = (const bf16_t*)d_in[5];
  const bf16_t* b_sp = (const bf16_t*)d_in[6];
  const bf16_t* w_out = (const bf16_t*)d_in[7];
  bf16_t* out = (bf16_t*)d_out;

  char* ws = (char*)d_ws;
  size_t off = 0;
  auto take = [&](size_t bytes) { char* p = ws + off; off += (bytes + 255) & ~(size_t)255; return p; };
  bf16_t* qkf = (bf16_t*)take((size_t)S_LEN * 1024 * 2);
  bf16_t* v2 = (bf16_t*)take((size_t)512 * S_LEN * 2);
  bf16_t* o = (bf16_t*)take((size_t)S_LEN * D_MODEL * 2);
  bf16_t* h = (bf16_t*)take((size_t)S_LEN * D_MODEL * 2);
  f32x2* hstats = (f32x2*)take((size_t)S_LEN * 16 * 8);
  bf16_t* z1c = (bf16_t*)take((size_t)S_LEN * FH * 2);
  bf16_t* z2f = (bf16_t*)take((size_t)S_LEN * FH * 2);
  bf16_t* y = (bf16_t*)take((size_t)S_LEN * FH * 2);
  float* wcs = (float*)take((size_t)4 * 2 * FH * 4);
  u64* ex = (u64*)take((size_t)1024 * 1024 + 256);

  RUNK(1) k1_qkv<<<256, 1024, 0, stream>>>(x, wqkv, qkf, v2, (u32x4*)ex, wo, w_in, wcs);
  RUNK(2) k2_attn<<<256, 512, 0, stream>>>(qkf, v2, (const bf16_t*)(ex + 131072), o, w_sp, w_out);
  RUNK(3) k3_wo<<<256, 768, 0, stream>>>(o, wo, x, h, hstats);
  RUNK(4) k4_in<<<224, 768, 0, stream>>>(h, w_in, hstats, z1c, z2f, ex, wcs);
  RUNK(5) k5_sp<<<256, 768, 0, stream>>>(w_sp, z2f, b_sp, z1c, y);
  RUNK(6) k6_out<<<256, 768, 0, stream>>>(y, w_out, h, out);
}
```

```cpp
#include <hip/hip_runtime.h>
#include <hip/hip_bf16.h>
#include <stdint.h>

typedef __bf16 bf16_t;
typedef __attribute__((ext_vector_type(8))) __bf16 bf16x8;
typedef __attribute__((ext_vector_type(2))) __bf16 bf16x2;
typedef __attribute__((ext_vector_type(4))) short s16x4;
typedef __attribute__((ext_vector_type(8))) short s16x8;
typedef __attribute__((ext_vector_type(4))) float f32x4;
typedef __attribute__((ext_vector_type(2))) float f32x2;
typedef __attribute__((ext_vector_type(16))) float f32x16;
typedef __attribute__((ext_vector_type(4))) unsigned u32x4;
typedef __attribute__((ext_vector_type(2))) unsigned u32x2;
typedef unsigned long long u64;

#define DI __device__ __forceinline__

constexpr int S_LEN = 2048;
constexpr int D_MODEL = 512;
constexpr int FH = 896;

DI float rbf(float x) { return (float)(__bf16)x; }
DI unsigned pack2(float a, float b) {
  bf16x2 v = {(__bf16)a, (__bf16)b};
  return __builtin_bit_cast(unsigned, v);
}
DI float lo_f(unsigned u) { return __uint_as_float(u << 16); }
DI float hi_f(unsigned u) { return __uint_as_float(u & 0xffff0000u); }

#define ST2(p, v) (*(u32x2*)(p) = (v))
DI unsigned vtid() {
  return threadIdx.x < 256u ? threadIdx.x + 512u : (threadIdx.x < 768u ? threadIdx.x - 256u : threadIdx.x);
}
template <int N> DI void wait_vm() { asm volatile("s_waitcnt vmcnt(%0)" ::"n"(N) : "memory"); }

DI void glds16(const void* g, char* l) {
  __builtin_amdgcn_global_load_lds((const __attribute__((address_space(1))) void*)g,
                                   (__attribute__((address_space(3))) void*)l, 16, 0, 0);
}
DI s16x4 tr_read(const char* l) {
  return __builtin_amdgcn_ds_read_tr16_b64_v4i16((__attribute__((address_space(3))) s16x4*)l);
}

template <int V> struct IC { static constexpr int value = V; };
constexpr int cmin3(int a, int b, int c) { return a < b ? (a < c ? a : c) : (b < c ? b : c); }
template <int N, int I = 0, class F> DI void static_for(F& f) {
  if constexpr (I < N) {
    f(IC<I>{});
    static_for<N, I + 1>(f);
  }
}

template <int N, int PER, int I = 0, class F> DI void static_from(F& f, IC<I> = IC<I>{}) {
  if constexpr (I < N) {
    f(IC<I>{});
    static_from<N, PER, I + 1>(f);
  }
}
template <int N, int PER, class F> DI void rolled_for(F& f) {
  constexpr int NL = (PER > 0) ? (N - 4) / PER : 0;
  if constexpr (NL >= 2) {
    f(IC<0>{});
#pragma unroll 1
    for (int it = 0; it < NL; ++it) static_from<1 + PER, PER, 1>(f);
    static_from<N, PER, 1 + NL * PER>(f);
  } else {
    static_for<N>(f);
  }
}

DI int fragaddr(int m, int k, int K32) {
  return ((m >> 4) * K32 + (k >> 5)) * 512 + ((m & 15) + 16 * ((k & 31) >> 3)) * 8 + (k & 7);
}

DI int bimg(int k, int cc) {
  return k * 128 + ((((cc >> 2) & 1) ^ ((k >> 1) & 1)) << 6) + (((cc & 3) ^ (((k >> 3) & 1) << 1)) << 4);
}

#define SPB5 2
#define SPB6 2
#define SPB3 2
template <int BM, int BN, int WM, int WK, int WN, int KT, int NST>
struct GemmCfg {
  static constexpr int NW = WM * WN * WK;
  static constexpr int NL = 4;
  static constexpr int PA = BM / 8, PB = BN / 8, P = PA + PB;
  static constexpr int PPL = (P + NL - 1) / NL;
  static constexpr int STAGE = (BM + BN) * 128;
  static constexpr int MF = BM / WM / 16;
  static constexpr int NFRAG = BN / 16;
  static constexpr int NFW = (NFRAG + WN - 1) / WN;
  static constexpr int LDS = STAGE * NST;
  static constexpr bool RESIDENT = NST >= KT;
  static constexpr int SPB = (KT == 32) ? SPB5 : (KT == 14) ? SPB6 : (BN == 64) ? SPB3 : 1;
  static constexpr int KS = KT / SPB;
  static constexpr int PRE = RESIDENT ? KS : NST / SPB - 1;
  static_assert(KT % SPB == 0 && NST % SPB == 0, "");
  static constexpr int PER = (NST / SPB == 3) ? 3 : 0;
};

struct NoHook {
  DI void operator()(const char*) const {}
};

template <int BM, int BN, int WM, int WK, int WN, int KT, int NST, bool AFRAG, bool BFRAG, int PW, class Hook>
DI void gemm_loader(char* smem, const bf16_t* __restrict__ A, int lda, const bf16_t* __restrict__ B, int ldb,
                    int pstride, int lw, int rot, Hook& hook) {
  using C = GemmCfg<BM, BN, WM, WK, WN, KT, NST>;
  constexpr int NL = C::NL, PA = C::PA, STAGE = C::STAGE, PRE = C::PRE;
  static_assert(PA % NL == 0, "A pieces must be a multiple of the loader count");
  static_assert(BFRAG || (BN % 64) == 0, "row-major B needs 64-column panels");
  const int lane = vtid() & 63;
  unsigned off[PW];
#pragma unroll
  for (int i = 0; i < PW; ++i) {
    if (i < PA / NL) {
      const int p = lw + i * NL;
      if constexpr (AFRAG) {
        off[i] = (unsigned)(((p >> 1) * lda + (p & 1)) * 1024 + lane * 16);
      } else {
        off[i] = (unsigned)((8 * p + (lane >> 3)) * lda * 2 + (((lane & 7) ^ (lane >> 3)) << 4));
      }
    } else {
      const int p = lw + i * NL - PA;
      if constexpr (BFRAG) {
        off[i] = (unsigned)(((p >> 1) * ldb + (p & 1)) * 1024 + lane * 16);
      } else {
        int k = (p & 7) * 8 + (lane >> 3);
        int cc = (lane & 7) ^ (((lane >> 4) & 1) << 2) ^ ((p & 1) << 1);
        off[i] = (unsigned)(k * ldb * 2 + (p >> 3) * pstride + cc * 16);
      }
    }
  }
  rot = 0;
  int kr = __builtin_amdgcn_readfirstlane(rot), kw = 0;
  auto issue = [&](int slot) {
    char* base = smem + slot * STAGE + lw * 1024;
    const int kk = kw + kr;
    const char* pa = (const char*)A + kk * (AFRAG ? 2048 : 128);
    const char* pb = (const char*)B + (size_t)kk * (BFRAG ? 2048 : 128 * ldb);
#pragma unroll
    for (int i = 0; i < PW; ++i) {
      unsigned o = off[i];
      asm volatile("" : "+v"(o));
      glds16((i < PA / NL ? pa : pb) + o, base + i * NL * 1024);
    }
    if constexpr (KT % 8 == 0) {
      kr = (kr + 1) & 7;
      if (kr == rot) kw += 8;
    } else {
      kr = (kr + 1 == KT) ? 0 : kr + 1;
    }
  };
  constexpr int SPB = C::SPB, KS = C::KS;
  auto body = [&](auto KTC) {
    constexpr int kt = decltype(KTC)::value;
    constexpr int I0 = (kt == 0) ? 0 : cmin3(KS, kt - 1 + PRE, 2 * kt);
    constexpr int I1 = cmin3(KS, kt + PRE, 2 * kt + 2);
#pragma unroll
    for (int u = I0 * SPB; u < I1 * SPB; ++u) issue(u % NST);
    wait_vm<(I1 - kt - 1) * PW * SPB>();
    __builtin_amdgcn_s_barrier();
    asm volatile("" ::: "memory");
#pragma unroll
    for (int u = 0; u < SPB; ++u) hook(smem + ((kt * SPB + u) % NST) * STAGE);
    __builtin_amdgcn_s_waitcnt(0xC07F);
  };
  rolled_for<KS, C::PER>(body);
}

struct NoPre {
  DI void operator()() const {}
};

template <int BM, int BN, int WM, int WK, int WN, int KT, int NST, bool AFRAG, bool BFRAG, bool CS, bool FULLN,
          bool SPLITN, int PLAIN, class Pre = NoPre>
DI void gemm_compute(char* smem, int wave,
                     f32x4 (&acc)[GemmCfg<BM, BN, WM, WK, WN, KT, NST>::MF][GemmCfg<BM, BN, WM, WK, WN, KT, NST>::NFW],
                     f32x4 (&cs)[GemmCfg<BM, BN, WM, WK, WN, KT, NST>::NFW], Pre pre = Pre()) {
  using C = GemmCfg<BM, BN, WM, WK, WN, KT, NST>;
  constexpr int STAGE = C::STAGE, MF = C::MF, NFW = C::NFW;
  constexpr int NF = FULLN ? NFW : NFW - 1;
  const int lane = vtid() & 63;
  const int wr = wave % WM, wk = (wave / WM) % WK, wc = wave / (WM * WK);
  const int g = lane >> 4, q = (lane & 15) >> 2, pp = lane & 3;
  constexpr int NKS = (WK == 2) ? 1 : 2;
  int aoff[NKS];
#pragma unroll
  for (int i = 0; i < NKS; ++i) {
    const int ks = (WK == 2) ? wk : i;
    if constexpr (AFRAG) aoff[i] = (wr * MF * 2 + ks) * 1024 + lane * 16;
    else aoff[i] = (wr * (MF * 16) + (lane & 15)) * 128 + (((4 * ks + g) ^ (lane & 7)) << 4);
  }
  int boff[NF];
#pragma unroll
  for (int nf = 0; nf < NF; ++nf) {
    const int f = SPLITN ? ((nf >> 1) * 4 + 2 * wc + (nf & 1)) : (wc * NFW + nf);
    if constexpr (BFRAG) {
      boff[nf] = BM * 128 + (f * 2 + ((WK == 2) ? wk : 0)) * 1024 + lane * 16;
    } else {
      const int kk = 8 * g + q + ((WK == 2) ? wk * 32 : 0);
      boff[nf] = BM * 128 + (f >> 2) * 8192 + bimg(kk, 2 * (f & 3) + (pp >> 1)) + 8 * (pp & 1);
    }
  }
  bf16x8 ones;
#pragma unroll
  for (int j = 0; j < 8; ++j) ones[j] = (__bf16)1.0f;

  constexpr int G = NKS * C::SPB, NSTEP = KT * NKS;
  static_assert(G <= 2, "");
  bf16x8 af[3][MF], bfr[3][NF];
  auto rd = [&](auto JC) {
    constexpr int j = decltype(JC)::value;
    constexpr int kt = j / NKS, i = j % NKS;
    const char* sb = smem + (kt % NST) * STAGE;
#pragma unroll
    for (int mf = 0; mf < MF; ++mf) af[j % 3][mf] = *(const bf16x8*)(sb + aoff[i] + mf * 2048);
#pragma unroll
    for (int nf = 0; nf < NF; ++nf) {
      if constexpr (BFRAG) {
        bfr[j % 3][nf] = *(const bf16x8*)(sb + boff[nf] + i * 1024);
      } else {
        s16x4 lo = tr_read(sb + boff[nf] + i * 4096);
        s16x4 hi = tr_read(sb + boff[nf] + i * 4096 + 512);
        s16x8 v = __builtin_shufflevector(lo, hi, 0, 1, 2, 3, 4, 5, 6, 7);
        bfr[j % 3][nf] = __builtin_bit_cast(bf16x8, v);
      }
    }
  };
  auto mm = [&](auto JC) {
    constexpr int jp = decltype(JC)::value;
#pragma unroll
    for (int nf = 0; nf < NF; ++nf) {
#pragma unroll
      for (int mf = 0; mf < MF; ++mf) {
        if ((PLAIN >> nf) & 1)
          acc[mf][nf] = __builtin_amdgcn_mfma_f32_16x16x32_bf16(af[jp % 3][mf], bfr[jp % 3][nf], acc[mf][nf], 0, 0, 0);
        else
          acc[mf][nf] = __builtin_amdgcn_mfma_f32_16x16x32_bf16(bfr[jp % 3][nf], af[jp % 3][mf], acc[mf][nf], 0, 0, 0);
      }
    }
  };
  auto csum = [&](auto JC) {
    constexpr int jp = decltype(JC)::value;
    if constexpr (CS) {
      if (wr == (jp % WM)) {
#pragma unroll
        for (int nf = 0; nf < NF; ++nf) {
          if ((PLAIN >> nf) & 1) cs[nf] = __builtin_amdgcn_mfma_f32_16x16x32_bf16(ones, bfr[jp % 3][nf], cs[nf], 0, 0, 0);
          else cs[nf] = __builtin_amdgcn_mfma_f32_16x16x32_bf16(bfr[jp % 3][nf], ones, cs[nf], 0, 0, 0);
        }
      }
    }
  };
  auto group = [&](auto GC) {
    constexpr int j0 = decltype(GC)::value * G;
    constexpr int NRD = G * (MF + (BFRAG ? NF : 2 * NF)), NMM = MF * NF;
    constexpr int RPM = (NRD + NMM - 1) / NMM;
    __builtin_amdgcn_s_barrier();
    asm volatile("" ::: "memory");
    if constexpr (decltype(GC)::value == NSTEP / G - 2) pre();
    rd(IC<j0>{});
    if constexpr (G == 2) rd(IC<j0 + 1>{});
    if constexpr (j0 > 0) {
      mm(IC<j0 - 1>{});
#pragma unroll
      for (int u = 0; u < NMM; ++u) {
        __builtin_amdgcn_sched_group_barrier(0x8, 1, 0);
        __builtin_amdgcn_sched_group_barrier(0x100, RPM, 0);
      }
      __builtin_amdgcn_sched_barrier(0);
      csum(IC<j0 - 1>{});
    }
    if constexpr (G == 2) {
      __builtin_amdgcn_sched_barrier(0);
      mm(IC<j0>{});
      csum(IC<j0>{});
    }
    __builtin_amdgcn_sched_barrier(0);
    __builtin_amdgcn_s_waitcnt(0xC07F);
  };
  rolled_for<NSTEP / G, C::PER>(group);
  mm(IC<NSTEP - 1>{});
  csum(IC<NSTEP - 1>{});
}

template <int BM, int BN, int WM, int WK, int WN, int KT, int NST, bool AFRAG, bool BFRAG, bool CS, bool SPLITN,
          int PLAIN, class Hook, class Pre = NoPre>
DI bool gemm_main(char* smem, const bf16_t* __restrict__ A, int lda, const bf16_t* __restrict__ B, int ldb, int pstride,
                  int rot,
                  f32x4 (&acc)[GemmCfg<BM, BN, WM, WK, WN, KT, NST>::MF][GemmCfg<BM, BN, WM, WK, WN, KT, NST>::NFW],
                  f32x4 (&cs)[GemmCfg<BM, BN, WM, WK, WN, KT, NST>::NFW], Hook& hook, Pre pre = Pre()) {
  using C = GemmCfg<BM, BN, WM, WK, WN, KT, NST>;
  const int wave = __builtin_amdgcn_readfirstlane(vtid() >> 6);
  if (wave >= C::NW) {
    const int lw = wave - C::NW;
    __builtin_amdgcn_s_setprio(3);
    constexpr bool EVEN = (C::P % C::NL) == 0;
    if (EVEN || lw < (C::P % C::NL))
      gemm_loader<BM, BN, WM, WK, WN, KT, NST, AFRAG, BFRAG, C::PPL>(smem, A, lda, B, ldb, pstride, lw, rot, hook);
    else if constexpr (!EVEN)
      gemm_loader<BM, BN, WM, WK, WN, KT, NST, AFRAG, BFRAG, C::PPL - 1>(smem, A, lda, B, ldb, pstride, lw, rot, hook);
    return false;
  }
  const int wc = wave / (WM * WK);
  constexpr bool ALLFULL = (C::NFRAG == WN * C::NFW);
  if (ALLFULL || wc != WN - 1)
    gemm_compute<BM, BN, WM, WK, WN, KT, NST, AFRAG, BFRAG, CS, true, SPLITN, PLAIN>(smem, wave, acc, cs, pre);
  else if constexpr (!ALLFULL)
    gemm_compute<BM, BN, WM, WK, WN, KT, NST, AFRAG, BFRAG, CS, false, SPLITN, PLAIN>(smem, wave, acc, cs, pre);
  return true;
}

template <int NFW>
DI void splitk_combine(char* smem, f32x4 (&acc)[2][NFW], f32x4 (&res)[NFW], int pairid, int wk, int lane, bool fulln) {
  __syncthreads();
  char* mine = smem + (pairid * 2 + wk) * (NFW * 1024) + lane * 16;
  char* theirs = smem + (pairid * 2 + (wk ^ 1)) * (NFW * 1024) + lane * 16;
#pragma unroll
  for (int nf = 0; nf < NFW; ++nf)
    if (nf < NFW - 1 || fulln) *(f32x4*)(mine + nf * 1024) = wk ? acc[0][nf] : acc[1][nf];
  __syncthreads();
#pragma unroll
  for (int nf = 0; nf < NFW; ++nf)
    if (nf < NFW - 1 || fulln) res[nf] = (wk ? acc[1][nf] : acc[0][nf]) + *(const f32x4*)(theirs + nf * 1024);
}

DI void chan_eq(float& mean, float& m2, float mean_b, float m2_b, float n) {
  float d = mean_b - mean;
  m2 = m2 + m2_b + d * d * (0.5f * n);
  mean = 0.5f * (mean + mean_b);
}

DI void ln_consts(float mean, float var, float& mean_b, float& rinv) {
  mean_b = rbf(mean);
  float v = rbf(var);
  float t = rbf(v + 1e-5f);
  float sd = rbf(sqrtf(t));
  rinv = 1.0f / sd;
}

DI float row16_sum(float v) {
  v += __builtin_bit_cast(float, __builtin_amdgcn_update_dpp(0, __builtin_bit_cast(int, v), 0x128, 0xf, 0xf, false));
  v += __builtin_bit_cast(float, __builtin_amdgcn_update_dpp(0, __builtin_bit_cast(int, v), 0x124, 0xf, 0xf, false));
  v += __builtin_bit_cast(float, __builtin_amdgcn_update_dpp(0, __builtin_bit_cast(int, v), 0x122, 0xf, 0xf, false));
  v += __builtin_bit_cast(float, __builtin_amdgcn_update_dpp(0, __builtin_bit_cast(int, v), 0x121, 0xf, 0xf, false));
  return v;
}

struct StatHook {
  float s1, s2;
  int o0;
  DI void operator()(const char* sb) {
    bf16x2 one2 = {(__bf16)1.0f, (__bf16)1.0f};
    u32x4 v0 = *(const u32x4*)(sb + o0);
    u32x4 v1 = *(const u32x4*)(sb + o0 + 16);
#pragma unroll
    for (int e = 0; e < 4; ++e) {
      const unsigned ua = v0[e], ub = v1[e];
      bf16x2 a = __builtin_bit_cast(bf16x2, ua), b = __builtin_bit_cast(bf16x2, ub);
      s1 = __builtin_amdgcn_fdot2_f32_bf16(a, one2, s1, false);
      s2 = __builtin_amdgcn_fdot2_f32_bf16(a, a, s2, false);
      s1 = __builtin_amdgcn_fdot2_f32_bf16(b, one2, s1, false);
      s2 = __builtin_amdgcn_fdot2_f32_bf16(b, b, s2, false);
    }
  }
};

__global__ __launch_bounds__(1024, 4) void k1_qkv(const bf16_t* __restrict__ x, const bf16_t* __restrict__ w,
                                                 bf16_t* __restrict__ qkf, bf16_t* __restrict__ v2,
                                                 u32x4* __restrict__ exz, const bf16_t* __restrict__ wo,
                                                 const bf16_t* __restrict__ win, float* __restrict__ wcs) {
  constexpr int BM = 64, BN = 192, WM = 2, WK = 1, WN = 4, KT = 8, NST = 4;
  using C = GemmCfg<BM, BN, WM, WK, WN, KT, NST>;
  __shared__ __attribute__((aligned(16))) char smem[C::LDS + 2048];
  if (vtid() < 64) exz[blockIdx.x * 64 + vtid()] = u32x4{0u, 0u, 0u, 0u};
  if (blockIdx.x == 0 && vtid() < 2) {
    const unsigned cv = vtid() ? 0u : 0x3f803f80u;
    exz[65536 + vtid()] = u32x4{cv, cv, cv, cv};
  }

  const int b = blockIdx.x, xcd = b & 7, idx = b >> 3;
  const int tm = xcd * 4 + (idx >> 3), tn = idx & 7;
  const int m0 = tm * BM, n0 = tn * BN;
  f32x4 acc[C::MF][C::NFW], cs[C::NFW];
#pragma unroll
  for (int i = 0; i < C::MF; ++i)
#pragma unroll
    for (int j = 0; j < C::NFW; ++j) acc[i][j] = f32x4{0.f, 0.f, 0.f, 0.f};
#pragma unroll
  for (int j = 0; j < C::NFW; ++j) cs[j] = f32x4{0.f, 0.f, 0.f, 0.f};
  StatHook hook;
  const int srow = (vtid() >> 2) & 63, sq = vtid() & 3;
  hook.s1 = 0.f; hook.s2 = 0.f;
  hook.o0 = srow * 128 + (sq << 5);
  const int lane = vtid() & 63, wave = __builtin_amdgcn_readfirstlane(vtid() >> 6), wr = wave & 1,
            wc = (wave >> 1) & 3;
  const int nw0 = n0 + wc * 48;
  const int plain = (nw0 >= 1024) ? 7 : ((nw0 + 16 >= 1024) ? 6 : 0);
  const bool comp = wave < 8;
  if (wave >= 12) {
#pragma unroll
    for (int kt = 0; kt < KT; ++kt) {
      __builtin_amdgcn_s_barrier();
      asm volatile("" ::: "memory");
      hook(smem + (kt % NST) * C::STAGE);
      __builtin_amdgcn_s_waitcnt(0xC07F);
    }
  } else if (wave >= 8) {
    __builtin_amdgcn_s_setprio(3);
    NoHook nh;
    gemm_loader<BM, BN, WM, WK, WN, KT, NST, false, false, C::PPL>(smem, x + (size_t)m0 * D_MODEL, D_MODEL, w + n0,
                                                                   3 * D_MODEL, 128, wave - 8, 0, nh);
    {
      const int lt = vtid() - 512;
      if (lt < 72) {
        const char* p0 = (lt < 16) ? (const char*)wo + (size_t)(b * 16 + lt) * 128
                                   : (const char*)win + (size_t)(b * 56 + lt - 16) * 128;
        unsigned v0 = *(const unsigned*)p0;
        asm volatile("" ::"v"(v0));
      }
      if (b < 28) {
        const bf16_t* wp = win + (size_t)((lt >> 3) * 16) * (2 * FH) + b * 64 + (lt & 7) * 8;
        float a[8];
#pragma unroll
        for (int e = 0; e < 8; ++e) a[e] = 0.f;
#pragma unroll
        for (int i = 0; i < 16; ++i) {
          const u32x4 v = *(const u32x4*)(wp + (size_t)i * (2 * FH));
#pragma unroll
          for (int e = 0; e < 4; ++e) { a[2 * e] += lo_f(v[e]); a[2 * e + 1] += hi_f(v[e]); }
        }
#pragma unroll
        for (int e = 0; e < 8; ++e) {
          a[e] += __shfl_xor(a[e], 8);
          a[e] += __shfl_xor(a[e], 16);
          a[e] += __shfl_xor(a[e], 32);
        }
        if ((lt & 63) < 8) {
          float* dp = wcs + (lt >> 6) * (2 * FH) + b * 64 + (lt & 7) * 8;
          *(f32x4*)dp = f32x4{a[0], a[1], a[2], a[3]};
          *(f32x4*)(dp + 4) = f32x4{a[4], a[5], a[6], a[7]};
        }
      }
    }
    return;
  } else if (plain == 7) {
    gemm_compute<BM, BN, WM, WK, WN, KT, NST, false, false, true, true, false, 7>(smem, wave, acc, cs);
  } else if (plain == 6) {
    gemm_compute<BM, BN, WM, WK, WN, KT, NST, false, false, true, true, false, 6>(smem, wave, acc, cs);
  } else {
    gemm_compute<BM, BN, WM, WK, WN, KT, NST, false, false, true, true, false, 0>(smem, wave, acc, cs);
  }

  float* tmean = (float*)(smem + C::LDS);
  float* trinv = tmean + 64;
  float* tcs = trinv + 64;
  if (!comp) {
    float s1 = hook.s1 + __shfl_xor(hook.s1, 1);
    float s2 = hook.s2 + __shfl_xor(hook.s2, 1);
    s1 += __shfl_xor(s1, 2);
    s2 += __shfl_xor(s2, 2);
    const float mean = s1 * (1.0f / 512.0f), ex2 = s2 * (1.0f / 512.0f);
    float var = ex2 - mean * mean;
    if (var < ex2 * (1.0f / 1024.0f)) {
      const bf16_t* xr = x + (size_t)(m0 + srow) * D_MODEL + sq * 128;
      float a2 = 0.f;
      for (int j = 0; j < 16; ++j) {
        u32x4 v = *(const u32x4*)(xr + j * 8);
#pragma unroll
        for (int e = 0; e < 4; ++e) {
          float d0 = lo_f(v[e]) - mean, d1 = hi_f(v[e]) - mean;
          a2 = fmaf(d0, d0, a2);
          a2 = fmaf(d1, d1, a2);
        }
      }
      a2 += __shfl_xor(a2, 1);
      a2 += __shfl_xor(a2, 2);
      var = a2 * (1.0f / 512.0f);
    }
    float mb, ri;
    ln_consts(mean, var, mb, ri);
    if (sq == 0) { tmean[srow] = mb; trinv[srow] = ri; }
    __syncthreads();
    return;
  }
  const int g = lane >> 4;
#pragma unroll
  for (int nf = 0; nf < C::NFW; ++nf) {
    if ((plain >> nf) & 1) {
      if (lane < 16) tcs[wr * 192 + wc * 48 + nf * 16 + lane] = cs[nf][0];
    } else if ((lane & 15) == 0) {
      *(f32x4*)(tcs + wr * 192 + wc * 48 + nf * 16 + 4 * g) = cs[nf];
    }
  }
  __syncthreads();
#pragma unroll
  for (int nf = 0; nf < C::NFW; ++nf) {
    if ((plain >> nf) & 1) {
      const int nl = wc * 48 + nf * 16 + (lane & 15);
      const float c = tcs[nl] + tcs[192 + nl];
      const int nv = n0 + nl - 1024;
#pragma unroll
      for (int mf = 0; mf < C::MF; ++mf) {
        const int ml = wr * 32 + mf * 16 + 4 * g;
        const f32x4 mb = *(const f32x4*)(tmean + ml), ri = *(const f32x4*)(trinv + ml);
        u32x2 o = {pack2((acc[mf][nf][0] - mb[0] * c) * ri[0], (acc[mf][nf][1] - mb[1] * c) * ri[1]),
                   pack2((acc[mf][nf][2] - mb[2] * c) * ri[2], (acc[mf][nf][3] - mb[3] * c) * ri[3])};
        const int m = m0 + ml;
        ST2(v2 + (((size_t)(nv >> 4) * 256 + (m >> 3)) * 16 + (nv & 15)) * 8 + (m & 7), o);
      }
    } else {
      const int nl = wc * 48 + nf * 16 + 4 * g;
      f32x4 c = *(const f32x4*)(tcs + nl) + *(const f32x4*)(tcs + 192 + nl);
      const int n = n0 + nl;
#pragma unroll
      for (int mf = 0; mf < C::MF; ++mf) {
        const int ml = wr * 32 + mf * 16 + (lane & 15);
        const float mb = tmean[ml], ri = trinv[ml];
        u32x2 o = {pack2((acc[mf][nf][0] - mb * c[0]) * ri, (acc[mf][nf][1] - mb * c[1]) * ri),
                   pack2((acc[mf][nf][2] - mb * c[2]) * ri, (acc[mf][nf][3] - mb * c[3]) * ri)};
        ST2(qkf + ((size_t)(n >> 3) * S_LEN + m0 + ml) * 8 + (n & 7), o);
      }
    }
  }
}

template <bool EDGE>
DI void attn_body(const bf16_t* __restrict__ qkf, const bf16_t* __restrict__ v2, const bf16_t* __restrict__ cst,
                  bf16_t* __restrict__ o, int qt, int h, const char* __restrict__ pf) {
  const int lane = threadIdx.x & 63;
  const int r = lane & 31, hf = lane >> 5;
  const int q0 = qt * 32;
  const int t0 = EDGE ? 8 - qt : 0;

  const bf16x8 qf = *(const bf16x8*)(qkf + ((size_t)(h * 2 + hf) * S_LEN + q0 + r) * 8);
  const int pr = 16 * ((r >> 2) & 1) + 4 * (r >> 3) + (r & 3);
  const bf16_t* kp = qkf + ((size_t)(64 + h * 2 + hf) * S_LEN + pr) * 8;
  bf16x8 kf[9];
#pragma unroll
  for (int t = 0; t < 9; ++t) {
    int kb = q0 - 256 + 32 * t;
    if (EDGE && kb < 0) kb = 0;
    kf[t] = *(const bf16x8*)(kp + kb * 8);
  }
  bf16x8 vf[9][2];
  const bf16_t* vp = (r < 16) ? v2 + ((size_t)h * 256 * 16 + 2 * hf * 16 + r) * 8 : cst + ((r == 16) ? 0 : 8);
  const int vm = (r < 16) ? 16 : 0;
#pragma unroll
  for (int t = 0; t < 9; ++t) {
    int kb = q0 - 256 + 32 * t;
    if (EDGE && kb < 0) kb = 0;
    vf[t][0] = *(const bf16x8*)(vp + kb * vm);
    vf[t][1] = *(const bf16x8*)(vp + (kb + 8) * vm);
  }
  const unsigned touch = *(const unsigned*)pf;
  f32x16 s[9];
  f32x16 zero;
#pragma unroll
  for (int i = 0; i < 16; ++i) zero[i] = 0.f;
#pragma unroll
  for (int t = 0; t < 9; ++t) s[t] = __builtin_amdgcn_mfma_f32_32x32x16_bf16(kf[t], qf, zero, 0, 0, 0);

  const float NEG = -3.0e38f;
#pragma unroll
  for (int i = 0; i < 16; ++i) {
    if (!(16 * hf + i > r)) s[0][i] = NEG;
    if (!(16 * hf + i <= r)) s[8][i] = NEG;
  }
  if constexpr (EDGE) {
#pragma unroll
    for (int t = 0; t < 8; ++t) {
      if (t < t0) {
#pragma unroll
        for (int i = 0; i < 16; ++i) s[t][i] = NEG;
      }
    }
  }
  float mx = NEG;
#pragma unroll
  for (int t = 0; t < 9; ++t)
#pragma unroll
    for (int i = 0; i < 16; ++i) mx = fmaxf(mx, s[t][i]);
  mx = fmaxf(mx, __shfl_xor(mx, 32));
  const float c = 0.25f * 1.4426950408889634f;
  const f32x2 c2 = {c, c}, mc2 = {-mx * c, -mx * c};
  f32x16 oacc = zero;
#pragma unroll
  for (int t = 0; t < 9; ++t) {
#pragma unroll
    for (int st = 0; st < 2; ++st) {
      bf16x8 pf;
#pragma unroll
      for (int j = 0; j < 8; j += 2) {
        f32x2 e = {s[t][8 * st + j], s[t][8 * st + j + 1]};
        e = __builtin_elementwise_fma(e, c2, mc2);
        pf[j] = (__bf16)__builtin_amdgcn_exp2f(e[0]);
        pf[j + 1] = (__bf16)__builtin_amdgcn_exp2f(e[1]);
      }
      oacc = __builtin_amdgcn_mfma_f32_32x32x16_bf16(vf[t][st], pf, oacc, 0, 0, 0);
    }
  }
  const float l = __shfl(oacc[8], r);
  const float il = 1.0f / l;
  bf16_t* op = o + fragaddr(q0 + r, h * 16 + 4 * hf, 16);
  u32x2 o0 = {pack2(oacc[0] * il, oacc[1] * il), pack2(oacc[2] * il, oacc[3] * il)};
  u32x2 o1 = {pack2(oacc[4] * il, oacc[5] * il), pack2(oacc[6] * il, oacc[7] * il)};
  ST2(op, o0);
  ST2(op + 128, o1);
  asm volatile("" ::"v"(touch));
}

__global__ __launch_bounds__(512) void k2_attn(const bf16_t* __restrict__ qkf, const bf16_t* __restrict__ v2,
                                               const bf16_t* __restrict__ cst, bf16_t* __restrict__ o,
                                               const bf16_t* __restrict__ wsp, const bf16_t* __restrict__ wout) {
  const int b = blockIdx.x, xcd = b & 7, idx = b >> 3;
  const int qt = xcd * 8 + (idx >> 2), hg = idx & 3;
  const int h = hg * 8 + (threadIdx.x >> 6);
  const int gl = b * 512 + threadIdx.x;
  const char* pf = (gl < 65536) ? (const char*)wsp + (size_t)gl * 128
                                : (const char*)wout + (size_t)((gl - 65536) % 7168) * 128;
  if (qt < 8) attn_body<true>(qkf, v2, cst, o, qt, h, pf);
  else attn_body<false>(qkf, v2, cst, o, qt, h, pf);
}

__global__ __launch_bounds__(768, 3) void k3_wo(const bf16_t* __restrict__ o, const bf16_t* __restrict__ w,
                                                const bf16_t* __restrict__ x, bf16_t* __restrict__ h,
                                                f32x2* __restrict__ stats) {
  constexpr int BM = 64, BN = 64, WM = 2, WK = 2, WN = 2, KT = 8, NST = 8;
  using C = GemmCfg<BM, BN, WM, WK, WN, KT, NST>;
  __shared__ __attribute__((aligned(16))) char smem[C::LDS + 8192];
  const int b = blockIdx.x, xcd = b & 7, idx = b >> 3;
  const int tm = xcd * 4 + (idx >> 3), tn = idx & 7;
  const int m0 = tm * BM, n0 = tn * BN;
  const int lane = vtid() & 63, wave = vtid() >> 6, wr = wave & 1, wk = (wave >> 1) & 1, wc = (wave >> 2) & 1;
  char* xs = smem + C::LDS;
  if (wave >= 8) {
    const int lw = __builtin_amdgcn_readfirstlane(wave) - 8;
#pragma unroll
    for (int i = 0; i < 2; ++i) {
      const int p = lw + 4 * i;
      glds16((const char*)(x + (size_t)(m0 + 8 * p + (lane >> 3)) * D_MODEL + n0) + (((lane & 7) ^ (lane >> 3)) << 4),
             xs + p * 1024);
    }
  }
  f32x4 acc[C::MF][C::NFW], cs[C::NFW], r[C::NFW];
#pragma unroll
  for (int i = 0; i < C::MF; ++i)
#pragma unroll
    for (int j = 0; j < C::NFW; ++j) acc[i][j] = f32x4{0.f, 0.f, 0.f, 0.f};
  NoHook hook;
  if (!gemm_main<BM, BN, WM, WK, WN, KT, NST, true, false, false, false, 0>(
          smem, o + (size_t)m0 * D_MODEL, 16, w + n0, D_MODEL, 128, (2 * (idx >> 3) + (idx & 7)) & 7, acc, cs, hook)) {
    return;
  }
  splitk_combine<2>(smem, acc, r, wr + 2 * wc, wk, lane, true);
  const int ml = wr * 32 + wk * 16 + (lane & 15);
  const int nl = wc * 32 + 4 * (lane >> 4);
  const u32x2 rv0 = *(const u32x2*)(xs + ml * 128 + (((nl >> 3) ^ (ml & 7)) << 4) + (nl & 4) * 2);
  const u32x2 rv1 = *(const u32x2*)(xs + ml * 128 + ((((nl + 16) >> 3) ^ (ml & 7)) << 4) + (nl & 4) * 2);
  float hv[8];
  hv[0] = rbf(lo_f(rv0[0]) + rbf(r[0][0])); hv[1] = rbf(hi_f(rv0[0]) + rbf(r[0][1]));
  hv[2] = rbf(lo_f(rv0[1]) + rbf(r[0][2])); hv[3] = rbf(hi_f(rv0[1]) + rbf(r[0][3]));
  hv[4] = rbf(lo_f(rv1[0]) + rbf(r[1][0])); hv[5] = rbf(hi_f(rv1[0]) + rbf(r[1][1]));
  hv[6] = rbf(lo_f(rv1[1]) + rbf(r[1][2])); hv[7] = rbf(hi_f(rv1[1]) + rbf(r[1][3]));
  u32x2 o0 = {pack2(hv[0], hv[1]), pack2(hv[2], hv[3])};
  u32x2 o1 = {pack2(hv[4], hv[5]), pack2(hv[6], hv[7])};
  bf16_t* hp = h + fragaddr(m0 + ml, n0 + nl, 16);
  ST2(hp, o0);
  ST2(hp + 256, o1);
  float mean = 0.f;
#pragma unroll
  for (int j = 0; j < 8; ++j) mean += hv[j];
  mean *= 0.125f;
  float m2 = 0.f;
#pragma unroll
  for (int j = 0; j < 8; ++j) { float d = hv[j] - mean; m2 = fmaf(d, d, m2); }
  chan_eq(mean, m2, __shfl_xor(mean, 16), __shfl_xor(m2, 16), 8.f);
  chan_eq(mean, m2, __shfl_xor(mean, 32), __shfl_xor(m2, 32), 16.f);
  if (lane < 16) stats[(size_t)(tn * 2 + wc) * S_LEN + m0 + ml] = f32x2{mean, m2};
}

DI f32x2 gelu2(f32x2 x) {
  const float k0 = -0.7978845608028654f * 2.0f * 1.4426950408889634f;
  const f32x2 one = {1.0f, 1.0f}, c1 = {0.044715f, 0.044715f}, kk = {k0, k0};
  f32x2 t = __builtin_elementwise_fma(x * x, c1, one);
  f32x2 y = (x * kk) * t;
  f32x2 e = {__builtin_amdgcn_exp2f(y[0]), __builtin_amdgcn_exp2f(y[1])};
  e = e + one;
  f32x2 r = {__builtin_amdgcn_rcpf(e[0]), __builtin_amdgcn_rcpf(e[1])};
  return x * r;
}
DI f32x2 rbf2(f32x2 x) {
  const unsigned u = pack2(x[0], x[1]);
  return f32x2{lo_f(u), hi_f(u)};
}

__global__ __launch_bounds__(768, 3) void k4_in(const bf16_t* __restrict__ h, const bf16_t* __restrict__ w,
                                                const f32x2* __restrict__ hstats, bf16_t* __restrict__ z1c,
                                                bf16_t* __restrict__ z2f, u64* __restrict__ ex,
                                                const float* __restrict__ wcs) {
  constexpr int BM = 128, BN = 128, WM = 4, WK = 1, WN = 2, KT = 8, NST = 4;
  using C = GemmCfg<BM, BN, WM, WK, WN, KT, NST>;
  __shared__ __attribute__((aligned(16))) char smem[C::LDS + 8192];
  const int b = blockIdx.x, xcd = b & 7, idx = b >> 3;
  const int tm = xcd * 2 + (idx >= 14 ? 1 : 0), tn = (idx >= 14) ? idx - 14 : idx;
  const int m0 = tm * BM, n0 = tn * 64;
  const int lane = vtid() & 63, wave = vtid() >> 6, wr = wave & 3, wc = (wave >> 2) & 1;
  const int g = lane >> 4;
  float* tmean = (float*)(smem + C::LDS);
  float* trinv = tmean + 128;
  float* tcs = trinv + 128;
  float* tmean2 = tcs + 512;
  float* trinv2 = tmean2 + 128;
  float* tpart = trinv2 + 128;
  f32x2* tpart2 = (f32x2*)(tpart + 512);
  f32x4 acc[C::MF][C::NFW], cs[C::NFW];
#pragma unroll
  for (int i = 0; i < C::MF; ++i)
#pragma unroll
    for (int j = 0; j < C::NFW; ++j) acc[i][j] = f32x4{0.f, 0.f, 0.f, 0.f};
#pragma unroll
  for (int j = 0; j < C::NFW; ++j) cs[j] = f32x4{0.f, 0.f, 0.f, 0.f};
  f32x2 hv[8];
  float cv[4];
  if (vtid() >= 512) {
    const f32x2* hp = hstats + (size_t)((vtid() >> 7) & 1) * 8 * S_LEN + m0 + (vtid() & 127);
#pragma unroll
    for (int j = 0; j < 8; ++j) hv[j] = hp[(size_t)j * S_LEN];
    const float* cp = wcs + n0 + (vtid() & 63) + ((vtid() >> 6) & 1) * FH;
#pragma unroll
    for (int j = 0; j < 4; ++j) cv[j] = cp[j * 2 * FH];
  }
  NoHook hook;
  const bool comp = gemm_main<BM, BN, WM, WK, WN, KT, NST, true, false, false, true, 12>(
      smem, h + (size_t)m0 * D_MODEL, 16, w + n0, 2 * FH, FH * 2, ((idx >= 14 ? 4 : 0) + tn) & 7, acc, cs, hook);
  if (!comp) {
    const int lt = vtid() - 512;
    float mean = 0.f;
#pragma unroll
    for (int j = 0; j < 8; ++j) mean += hv[j][0];
    mean *= 0.125f;
    float m2 = 0.f;
#pragma unroll
    for (int j = 0; j < 8; ++j) { float d = hv[j][0] - mean; m2 += hv[j][1] + 32.f * d * d; }
    if (lt >= 128) tpart2[lt - 128] = f32x2{mean, m2};
    else tcs[lt] = (cv[0] + cv[1]) + (cv[2] + cv[3]);
    __syncthreads();
    if (lt < 128) {
      const f32x2 o = tpart2[lt];
      chan_eq(mean, m2, o[0], o[1], 256.f);
      float mb, ri;
      ln_consts(mean, m2 * (1.0f / 512.0f), mb, ri);
      tmean[lt] = mb;
      trinv[lt] = ri;
    }
    __syncthreads();
    return;
  }
  __syncthreads();
  __syncthreads();
  u64* exp_ = ex + (size_t)tm * (14 * 128);
  {
    float c2[2];
#pragma unroll
    for (int nf = 0; nf < 2; ++nf) {
      const int nl = 64 + wc * 32 + nf * 16 + (lane & 15);
      c2[nf] = tcs[nl];
    }
#pragma unroll
    for (int mf = 0; mf < C::MF; ++mf) {
      const int ml = wr * 32 + mf * 16 + 4 * g;
      const f32x4 mb = *(const f32x4*)(tmean + ml), ri = *(const f32x4*)(trinv + ml);
      float pm = 0.f, pq = 0.f;
#pragma unroll
      for (int r = 0; r < 4; ++r) {
        const f32x2 u2 = (f32x2{acc[mf][2][r], acc[mf][3][r]} - f32x2{c2[0], c2[1]} * mb[r]) * ri[r];
        const f32x2 z2 = rbf2(gelu2(rbf2(u2)));
        const float a = z2[0], bq = z2[1];
        acc[mf][2][r] = a;
        acc[mf][3][r] = bq;
        const float mean = row16_sum(a + bq) * (1.0f / 32.0f);
        const float da = a - mean, db = bq - mean;
        const float m2 = row16_sum(fmaf(da, da, db * db));
        if ((lane & 3) == r) { pm = mean; pq = m2; }
      }
      if ((lane & 15) < 4) ((f32x2*)tpart)[wc * 128 + ml + (lane & 3)] = f32x2{pm, pq};
    }
  }
  __syncthreads();
  if (vtid() < 128) {
    f32x2 a = ((const f32x2*)tpart)[vtid()], bq = ((const f32x2*)tpart)[128 + vtid()];
    float mean = a[0], m2 = a[1];
    chan_eq(mean, m2, bq[0], bq[1], 32.f);
    __hip_atomic_store(exp_ + tn * 128 + vtid(),
                       ((u64)(__float_as_uint(m2) | 1u) << 32) | (u64)__float_as_uint(mean), __ATOMIC_RELAXED,
                       __HIP_MEMORY_SCOPE_AGENT);
  }
#pragma unroll
  for (int nf = 0; nf < 2; ++nf) {
    const int nl = wc * 32 + nf * 16 + 4 * g;
    const f32x4 c = *(const f32x4*)(tcs + nl);
#pragma unroll
    for (int mf = 0; mf < C::MF; ++mf) {
      const int ml = wr * 32 + mf * 16 + (lane & 15);
      const float mb = tmean[ml], ri = trinv[ml];
      const f32x2 ua = (f32x2{acc[mf][nf][0], acc[mf][nf][1]} - f32x2{c[0], c[1]} * mb) * ri;
      const f32x2 ub = (f32x2{acc[mf][nf][2], acc[mf][nf][3]} - f32x2{c[2], c[3]} * mb) * ri;
      const f32x2 va = gelu2(rbf2(ua)), vb = gelu2(rbf2(ub));
      u32x2 ov = {pack2(va[0], va[1]), pack2(vb[0], vb[1])};
      ST2(z1c + ((size_t)((m0 >> 4) + wr * 2 + mf) * 56 + (n0 >> 4) + wc * 2 + nf) * 256 + lane * 4, ov);
    }
  }
  if (vtid() < 256) {
    const int t = vtid(), row = t & 127, half = t >> 7;
    const u64* gp = exp_ + half * (7 * 128) + row;
    float vm[7], vq[7];
    for (unsigned spins = 0;; ++spins) {
      bool ok = true;
#pragma unroll
      for (int j = 0; j < 7; ++j) {
        u64 gg = __hip_atomic_load(gp + j * 128, __ATOMIC_RELAXED, __HIP_MEMORY_SCOPE_AGENT);
        vm[j] = __uint_as_float((unsigned)gg);
        vq[j] = __uint_as_float((unsigned)(gg >> 32));
        ok &= ((unsigned)(gg >> 32) != 0u);
      }
      if (__all(ok)) break;
      if (spins > 4000000u) break;
      __builtin_amdgcn_s_sleep(1);
    }
    float hm = 0.f, hq = 0.f;
#pragma unroll
    for (int j = 0; j < 7; ++j) { hm += vm[j]; hq += vq[j]; }
    hm *= (1.0f / 7.0f);
#pragma unroll
    for (int j = 0; j < 7; ++j) { float d = vm[j] - hm; hq = fmaf(64.f * d, d, hq); }
    tpart2[t] = f32x2{hm, hq};
  }
  __syncthreads();
  if (vtid() < 128) {
    f32x2 a = tpart2[vtid()], bq = tpart2[128 + vtid()];
    float mean = a[0], m2 = a[1];
    chan_eq(mean, m2, bq[0], bq[1], 448.f);
    float mb, ri;
    ln_consts(mean, m2 * (1.0f / 896.0f), mb, ri);
    tmean2[vtid()] = mb;
    trinv2[vtid()] = ri;
  }
  __syncthreads();
#pragma unroll
  for (int mf = 0; mf < C::MF; ++mf) {
    const int ml = wr * 32 + mf * 16 + 4 * g;
    const f32x4 mb = *(const f32x4*)(tmean2 + ml), ri = *(const f32x4*)(trinv2 + ml);
#pragma unroll
    for (int nf = 2; nf < 4; ++nf) {
      const int n = n0 + wc * 32 + (nf & 1) * 16 + (lane & 15);
      float v0 = rbf(acc[mf][nf][0] - mb[0]) * ri[0], v1 = rbf(acc[mf][nf][1] - mb[1]) * ri[1];
      float v2 = rbf(acc[mf][nf][2] - mb[2]) * ri[2], v3 = rbf(acc[mf][nf][3] - mb[3]) * ri[3];
      u32x2 ov = {pack2(v0, v1), pack2(v2, v3)};
      ST2(z2f + fragaddr(n, m0 + ml, 64), ov);
    }
  }
}

__global__ __launch_bounds__(768, 3) void k5_sp(const bf16_t* __restrict__ wsp, const bf16_t* __restrict__ z2f,
                                                const bf16_t* __restrict__ bsp, const bf16_t* __restrict__ z1c,
                                                bf16_t* __restrict__ y) {
  constexpr int BM = 64, BN = 112, WM = 2, WK = 2, WN = 2, KT = 32, NST = (SPB5 == 2) ? 6 : 7;
  using C = GemmCfg<BM, BN, WM, WK, WN, KT, NST>;
  __shared__ __attribute__((aligned(16))) char smem[C::LDS];
  const int b = blockIdx.x, xcd = b & 7, idx = b >> 3;
  const int tm = (xcd >> 2) * 16 + (idx >> 1), tn = (xcd & 3) * 2 + (idx & 1);
  const int m0 = tm * BM, n0 = tn * BN;
  const int lane = vtid() & 63, wave = vtid() >> 6, wr = wave & 1, wk = (wave >> 1) & 1, wc = (wave >> 2) & 1;
  const bool fulln = (wc == 0);
  const int m = m0 + wr * 32 + wk * 16 + (lane & 15);
  const int nb = n0 + wc * 64;
  float bb = 0.f;
  u32x2 zv[4];
  auto pre = [&]() {
    bb = (float)bsp[m];
#pragma unroll
    for (int nf = 0; nf < 4; ++nf)
      if (nf < 3 || fulln) zv[nf] = *(const u32x2*)(z1c + ((size_t)(m >> 4) * 56 + (nb >> 4) + nf) * 256 + lane * 4);
  };
  f32x4 acc[C::MF][C::NFW], cs[C::NFW], r[C::NFW];
#pragma unroll
  for (int i = 0; i < C::MF; ++i)
#pragma unroll
    for (int j = 0; j < C::NFW; ++j) acc[i][j] = f32x4{0.f, 0.f, 0.f, 0.f};
  NoHook hook;
  if (!gemm_main<BM, BN, WM, WK, WN, KT, NST, false, true, false, false, 0>(
          smem, wsp + (size_t)m0 * S_LEN, S_LEN, z2f + (size_t)(n0 >> 4) * 64 * 512, 64, 0,
          (idx >> 1) & 7, acc, cs, hook, pre))
    return;
  splitk_combine<4>(smem, acc, r, wr + 2 * wc, wk, lane, fulln);
#pragma unroll
  for (int nf = 0; nf < C::NFW; ++nf) {
    if (nf < 3 || fulln) {
      float v0 = lo_f(zv[nf][0]) * rbf(rbf(r[nf][0]) + bb);
      float v1 = hi_f(zv[nf][0]) * rbf(rbf(r[nf][1]) + bb);
      float v2 = lo_f(zv[nf][1]) * rbf(rbf(r[nf][2]) + bb);
      float v3 = hi_f(zv[nf][1]) * rbf(rbf(r[nf][3]) + bb);
      u32x2 ov = {pack2(v0, v1), pack2(v2, v3)};
      ST2(y + fragaddr(m, nb + nf * 16 + 4 * (lane >> 4), 28), ov);
    }
  }
}

__global__ __launch_bounds__(768, 3) void k6_out(const bf16_t* __restrict__ y, const bf16_t* __restrict__ w,
                                                 const bf16_t* __restrict__ h, bf16_t* __restrict__ out) {
  constexpr int BM = 64, BN = 64, WM = 2, WK = 2, WN = 2, KT = 14, NST = (SPB6 == 2) ? 8 : 9;
  using C = GemmCfg<BM, BN, WM, WK, WN, KT, NST>;
  __shared__ __attribute__((aligned(16))) char smem[C::LDS + 8192];
  const int b = blockIdx.x, xcd = b & 7, idx = b >> 3;
  const int tm = xcd * 4 + (idx >> 3), tn = idx & 7;
  const int m0 = tm * BM, n0 = tn * BN;
  const int lane = vtid() & 63, wave = vtid() >> 6, wr = wave & 1, wk = (wave >> 1) & 1, wc = (wave >> 2) & 1;
  const int ml = wr * 32 + wk * 16 + (lane & 15);
  const int nl = wc * 32 + 4 * (lane >> 4);
  u32x2 rv0 = {0u, 0u}, rv1 = {0u, 0u};
  auto pre = [&]() {
    const bf16_t* hp = h + fragaddr(m0 + ml, n0 + nl, 16);
    rv0 = *(const u32x2*)(hp);
    rv1 = *(const u32x2*)(hp + 256);
  };
  f32x4 acc[C::MF][C::NFW], cs[C::NFW], r[C::NFW];
#pragma unroll
  for (int i = 0; i < C::MF; ++i)
#pragma unroll
    for (int j = 0; j < C::NFW; ++j) acc[i][j] = f32x4{0.f, 0.f, 0.f, 0.f};
  NoHook hook;
  if (!gemm_main<BM, BN, WM, WK, WN, KT, NST, true, false, false, false, 0>(
          smem, y + (size_t)(m0 >> 4) * 28 * 512, 28, w + n0, D_MODEL, 128, ((idx >> 3) * 3 + (idx & 7) * 2) % KT, acc, cs,
          hook, pre))
    return;
  splitk_combine<2>(smem, acc, r, wr + 2 * wc, wk, lane, true);
  u32x2 o0 = {pack2(lo_f(rv0[0]) + rbf(r[0][0]), hi_f(rv0[0]) + rbf(r[0][1])),
              pack2(lo_f(rv0[1]) + rbf(r[0][2]), hi_f(rv0[1]) + rbf(r[0][3]))};
  u32x2 o1 = {pack2(lo_f(rv1[0]) + rbf(r[1][0]), hi_f(rv1[0]) + rbf(r[1][1])),
              pack2(lo_f(rv1[1]) + rbf(r[1][2]), hi_f(rv1[1]) + rbf(r[1][3]))};
  char* os = smem + C::LDS;
  *(u32x2*)(os + ml * 128 + (((nl >> 3) ^ (ml & 7)) << 4) + (nl & 4) * 2) = o0;
  *(u32x2*)(os + ml * 128 + ((((nl + 16) >> 3) ^ (ml & 7)) << 4) + (nl & 4) * 2) = o1;
  __syncthreads();
  const int row = vtid() >> 3, ch = vtid() & 7;
  const u32x4 ov = *(const u32x4*)(os + row * 128 + ((ch ^ (row & 7)) << 4));
  *(u32x4*)(out + (size_t)(m0 + row) * D_MODEL + n0 + ch * 8) = ov;
}

#define RUNK(i)
extern "C" void kernel_launch(void* const* d_in, const int* in_sizes, int n_in, void* d_out, int out_size,
                              void* d_ws, size_t ws_size, hipStream_t stream) {
  const bf16_t* x = (const bf16_t*)d_in[0];
  const bf16_t* wqkv = (const bf16_t*)d_in[1];
  const bf16_t* wo = (const bf16_t*)d_in[2];
  const bf16_t* w_in = (const bf16_t*)d_in[4];
  const bf16_t* w_sp = (const bf16_t*)d_in[5];
  const bf16_t* b_sp = (const bf16_t*)d_in[6];
  const bf16_t* w_out = (const bf16_t*)d_in[7];
  bf16_t* out = (bf16_t*)d_out;

  char* ws = (char*)d_ws;
  size_t off = 0;
  auto take = [&](size_t bytes) { char* p = ws + off; off += (bytes + 255) & ~(size_t)255; return p; };
  bf16_t* qkf = (bf16_t*)take((size_t)S_LEN * 1024 * 2);
  bf16_t* v2 = (bf16_t*)take((size_t)512 * S_LEN * 2);
  bf16_t* o = (bf16_t*)take((size_t)S_LEN * D_MODEL * 2);
  bf16_t* h = (bf16_t*)take((size_t)S_LEN * D_MODEL * 2);
  f32x2* hstats = (f32x2*)take((size_t)S_LEN * 16 * 8);
  bf16_t* z1c = (bf16_t*)take((size_t)S_LEN * FH * 2);
  bf16_t* z2f = (bf16_t*)take((size_t)S_LEN * FH * 2);
  bf16_t* y = (bf16_t*)take((size_t)S_LEN * FH * 2);
  float* wcs = (float*)take((size_t)4 * 2 * FH * 4);
  u64* ex = (u64*)take((size_t)1024 * 1024 + 256);

  RUNK(1) k1_qkv<<<256, 1024, 0, stream>>>(x, wqkv, qkf, v2, (u32x4*)ex, wo, w_in, wcs);
  RUNK(2) k2_attn<<<256, 512, 0, stream>>>(qkf, v2, (const bf16_t*)(ex + 131072), o, w_sp, w_out);
  RUNK(3) k3_wo<<<256, 768, 0, stream>>>(o, wo, x, h, hstats);
  RUNK(4) k4_in<<<224, 768, 0, stream>>>(h, w_in, hstats, z1c, z2f, ex, wcs);
  RUNK(5) k5_sp<<<256, 768, 0, stream>>>(w_sp, z2f, b_sp, z1c, y);
  RUNK(6) k6_out<<<256, 768, 0, stream>>>(y, w_out, h, out);
}
```

```cpp
#include <hip/hip_runtime.h>
#include <hip/hip_bf16.h>
#include <stdint.h>

typedef __bf16 bf16_t;
typedef __attribute__((ext_vector_type(8))) __bf16 bf16x8;
typedef __attribute__((ext_vector_type(2))) __bf16 bf16x2;
typedef __attribute__((ext_vector_type(4))) short s16x4;
typedef __attribute__((ext_vector_type(8))) short s16x8;
typedef __attribute__((ext_vector_type(4))) float f32x4;
typedef __attribute__((ext_vector_type(2))) float f32x2;
typedef __attribute__((ext_vector_type(16))) float f32x16;
typedef __attribute__((ext_vector_type(4))) unsigned u32x4;
typedef __attribute__((ext_vector_type(2))) unsigned u32x2;
typedef unsigned long long u64;

#define DI __device__ __forceinline__

constexpr int S_LEN = 2048;
constexpr int D_MODEL = 512;
constexpr int FH = 896;

DI float rbf(float x) { return (float)(__bf16)x; }
DI unsigned pack2(float a, float b) {
  bf16x2 v = {(__bf16)a, (__bf16)b};
  return __builtin_bit_cast(unsigned, v);
}
DI float lo_f(unsigned u) { return __uint_as_float(u << 16); }
DI float hi_f(unsigned u) { return __uint_as_float(u & 0xffff0000u); }

#define ST2(p, v) (*(u32x2*)(p) = (v))
DI unsigned vtid() {
  return threadIdx.x < 256u ? threadIdx.x + 512u : (threadIdx.x < 768u ? threadIdx.x - 256u : threadIdx.x);
}
template <int N> DI void wait_vm() { asm volatile("s_waitcnt vmcnt(%0)" ::"n"(N) : "memory"); }

DI void glds16(const void* g, char* l) {
  __builtin_amdgcn_global_load_lds((const __attribute__((address_space(1))) void*)g,
                                   (__attribute__((address_space(3))) void*)l, 16, 0, 0);
}
DI s16x4 tr_read(const char* l) {
  return __builtin_amdgcn_ds_read_tr16_b64_v4i16((__attribute__((address_space(3))) s16x4*)l);
}

template <int V> struct IC { static constexpr int value = V; };
constexpr int cmin3(int a, int b, int c) { return a < b ? (a < c ? a : c) : (b < c ? b : c); }
template <int N, int I = 0, class F> DI void static_for(F& f) {
  if constexpr (I < N) {
    f(IC<I>{});
    static_for<N, I + 1>(f);
  }
}

template <int N, int PER, int I = 0, class F> DI void static_from(F& f, IC<I> = IC<I>{}) {
  if constexpr (I < N) {
    f(IC<I>{});
    static_from<N, PER, I + 1>(f);
  }
}
template <int N, int PER, class F> DI void rolled_for(F& f) {
  constexpr int NL = (PER > 0) ? (N - 4) / PER : 0;
  if constexpr (NL >= 2) {
    f(IC<0>{});
#pragma unroll 1
    for (int it = 0; it < NL; ++it) static_from<1 + PER, PER, 1>(f);
    static_from<N, PER, 1 + NL * PER>(f);
  } else {
    static_for<N>(f);
  }
}

DI int fragaddr(int m, int k, int K32) {
  return ((m >> 4) * K32 + (k >> 5)) * 512 + ((m & 15) + 16 * ((k & 31) >> 3)) * 8 + (k & 7);
}

DI int bimg(int k, int cc) {
  return k * 128 + ((((cc >> 2) & 1) ^ ((k >> 1) & 1)) << 6) + (((cc & 3) ^ (((k >> 3) & 1) << 1)) << 4);
}

#define SPB5 2
#define SPB6 2
#define SPB3 2
template <int BM, int BN, int WM, int WK, int WN, int KT, int NST>
struct GemmCfg {
  static constexpr int NW = WM * WN * WK;
  static constexpr int NL = (KT == 32) ? 8 : 4;
  static constexpr int PA = BM / 8, PB = BN / 8, P = PA + PB;
  static constexpr int PPL = (P + NL - 1) / NL;
  static constexpr int STAGE = (BM + BN) * 128;
  static constexpr int MF = BM / WM / 16;
  static constexpr int NFRAG = BN / 16;
  static constexpr int NFW = (NFRAG + WN - 1) / WN;
  static constexpr int LDS = STAGE * NST;
  static constexpr bool RESIDENT = NST >= KT;
  static constexpr int SPB = (KT == 32) ? SPB5 : (KT == 14) ? SPB6 : (BN == 64) ? SPB3 : 1;
  static constexpr int KS = KT / SPB;
  static constexpr int PRE = RESIDENT ? KS : NST / SPB - 1;
  static_assert(KT % SPB == 0 && NST % SPB == 0, "");
  static constexpr int PER = (NST / SPB == 3) ? 3 : 0;
};

struct NoHook {
  DI void operator()(const char*) const {}
};

template <int BM, int BN, int WM, int WK, int WN, int KT, int NST, bool AFRAG, bool BFRAG, int PW, class Hook>
DI void gemm_loader(char* smem, const bf16_t* __restrict__ A, int lda, const bf16_t* __restrict__ B, int ldb,
                    int pstride, int lw, int rot, Hook& hook) {
  using C = GemmCfg<BM, BN, WM, WK, WN, KT, NST>;
  constexpr int NL = C::NL, PA = C::PA, STAGE = C::STAGE, PRE = C::PRE;
  static_assert(PA % NL == 0, "A pieces must be a multiple of the loader count");
  static_assert(BFRAG || (BN % 64) == 0, "row-major B needs 64-column panels");
  const int lane = vtid() & 63;
  unsigned off[PW];
#pragma unroll
  for (int i = 0; i < PW; ++i) {
    if (i < PA / NL) {
      const int p = lw + i * NL;
      if constexpr (AFRAG) {
        off[i] = (unsigned)(((p >> 1) * lda + (p & 1)) * 1024 + lane * 16);
      } else {
        off[i] = (unsigned)((8 * p + (lane >> 3)) * lda * 2 + (((lane & 7) ^ (lane >> 3)) << 4));
      }
    } else {
      const int p = lw + i * NL - PA;
      if constexpr (BFRAG) {
        off[i] = (unsigned)(((p >> 1) * ldb + (p & 1)) * 1024 + lane * 16);
      } else {
        int k = (p & 7) * 8 + (lane >> 3);
        int cc = (lane & 7) ^ (((lane >> 4) & 1) << 2) ^ ((p & 1) << 1);
        off[i] = (unsigned)(k * ldb * 2 + (p >> 3) * pstride + cc * 16);
      }
    }
  }
  rot = 0;
  int kr = __builtin_amdgcn_readfirstlane(rot), kw = 0;
  auto issue = [&](int slot) {
    char* base = smem + slot * STAGE + lw * 1024;
    const int kk = kw + kr;
    const char* pa = (const char*)A + kk * (AFRAG ? 2048 : 128);
    const char* pb = (const char*)B + (size_t)kk * (BFRAG ? 2048 : 128 * ldb);
#pragma unroll
    for (int i = 0; i < PW; ++i) {
      unsigned o = off[i];
      asm volatile("" : "+v"(o));
      glds16((i < PA / NL ? pa : pb) + o, base + i * NL * 1024);
    }
    if constexpr (KT % 8 == 0) {
      kr = (kr + 1) & 7;
      if (kr == rot) kw += 8;
    } else {
      kr = (kr + 1 == KT) ? 0 : kr + 1;
    }
  };
  constexpr int SPB = C::SPB, KS = C::KS;
  auto body = [&](auto KTC) {
    constexpr int kt = decltype(KTC)::value;
    constexpr int I0 = (kt == 0) ? 0 : cmin3(KS, kt - 1 + PRE, 2 * kt);
    constexpr int I1 = cmin3(KS, kt + PRE, 2 * kt + 2);
#pragma unroll
    for (int u = I0 * SPB; u < I1 * SPB; ++u) issue(u % NST);
    wait_vm<(I1 - kt - 1) * PW * SPB>();
    __builtin_amdgcn_s_barrier();
    asm volatile("" ::: "memory");
#pragma unroll
    for (int u = 0; u < SPB; ++u) hook(smem + ((kt * SPB + u) % NST) * STAGE);
    __builtin_amdgcn_s_waitcnt(0xC07F);
  };
  rolled_for<KS, C::PER>(body);
}

struct NoPre {
  DI void operator()() const {}
};

template <int BM, int BN, int WM, int WK, int WN, int KT, int NST, bool AFRAG, bool BFRAG, bool CS, bool FULLN,
          bool SPLITN, int PLAIN, class Pre = NoPre>
DI void gemm_compute(char* smem, int wave,
                     f32x4 (&acc)[GemmCfg<BM, BN, WM, WK, WN, KT, NST>::MF][GemmCfg<BM, BN, WM, WK, WN, KT, NST>::NFW],
                     f32x4 (&cs)[GemmCfg<BM, BN, WM, WK, WN, KT, NST>::NFW], Pre pre = Pre()) {
  using C = GemmCfg<BM, BN, WM, WK, WN, KT, NST>;
  constexpr int STAGE = C::STAGE, MF = C::MF, NFW = C::NFW;
  constexpr int NF = FULLN ? NFW : NFW - 1;
  const int lane = vtid() & 63;
  const int wr = wave % WM, wk = (wave / WM) % WK, wc = wave / (WM * WK);
  const int g = lane >> 4, q = (lane & 15) >> 2, pp = lane & 3;
  constexpr int NKS = (WK == 2) ? 1 : 2;
  int aoff[NKS];
#pragma unroll
  for (int i = 0; i < NKS; ++i) {
    const int ks = (WK == 2) ? wk : i;
    if constexpr (AFRAG) aoff[i] = (wr * MF * 2 + ks) * 1024 + lane * 16;
    else aoff[i] = (wr * (MF * 16) + (lane & 15)) * 128 + (((4 * ks + g) ^ (lane & 7)) << 4);
  }
  int boff[NF];
#pragma unroll
  for (int nf = 0; nf < NF; ++nf) {
    const int f = SPLITN ? ((nf >> 1) * 4 + 2 * wc + (nf & 1)) : (wc * NFW + nf);
    if constexpr (BFRAG) {
      boff[nf] = BM * 128 + (f * 2 + ((WK == 2) ? wk : 0)) * 1024 + lane * 16;
    } else {
      const int kk = 8 * g + q + ((WK == 2) ? wk * 32 : 0);
      boff[nf] = BM * 128 + (f >> 2) * 8192 + bimg(kk, 2 * (f & 3) + (pp >> 1)) + 8 * (pp & 1);
    }
  }
  bf16x8 ones;
#pragma unroll
  for (int j = 0; j < 8; ++j) ones[j] = (__bf16)1.0f;

  constexpr int G = NKS * C::SPB, NSTEP = KT * NKS;
  static_assert(G <= 2, "");
  bf16x8 af[3][MF], bfr[3][NF];
  auto rd = [&](auto JC) {
    constexpr int j = decltype(JC)::value;
    constexpr int kt = j / NKS, i = j % NKS;
    const char* sb = smem + (kt % NST) * STAGE;
#pragma unroll
    for (int mf = 0; mf < MF; ++mf) af[j % 3][mf] = *(const bf16x8*)(sb + aoff[i] + mf * 2048);
#pragma unroll
    for (int nf = 0; nf < NF; ++nf) {
      if constexpr (BFRAG) {
        bfr[j % 3][nf] = *(const bf16x8*)(sb + boff[nf] + i * 1024);
      } else {
        s16x4 lo = tr_read(sb + boff[nf] + i * 4096);
        s16x4 hi = tr_read(sb + boff[nf] + i * 4096 + 512);
        s16x8 v = __builtin_shufflevector(lo, hi, 0, 1, 2, 3, 4, 5, 6, 7);
        bfr[j % 3][nf] = __builtin_bit_cast(bf16x8, v);
      }
    }
  };
  auto mm = [&](auto JC) {
    constexpr int jp = decltype(JC)::value;
#pragma unroll
    for (int nf = 0; nf < NF; ++nf) {
#pragma unroll
      for (int mf = 0; mf < MF; ++mf) {
        if ((PLAIN >> nf) & 1)
          acc[mf][nf] = __builtin_amdgcn_mfma_f32_16x16x32_bf16(af[jp % 3][mf], bfr[jp % 3][nf], acc[mf][nf], 0, 0, 0);
        else
          acc[mf][nf] = __builtin_amdgcn_mfma_f32_16x16x32_bf16(bfr[jp % 3][nf], af[jp % 3][mf], acc[mf][nf], 0, 0, 0);
      }
    }
  };
  auto csum = [&](auto JC) {
    constexpr int jp = decltype(JC)::value;
    if constexpr (CS) {
      if (wr == (jp % WM)) {
#pragma unroll
        for (int nf = 0; nf < NF; ++nf) {
          if ((PLAIN >> nf) & 1) cs[nf] = __builtin_amdgcn_mfma_f32_16x16x32_bf16(ones, bfr[jp % 3][nf], cs[nf], 0, 0, 0);
          else cs[nf] = __builtin_amdgcn_mfma_f32_16x16x32_bf16(bfr[jp % 3][nf], ones, cs[nf], 0, 0, 0);
        }
      }
    }
  };
  auto group = [&](auto GC) {
    constexpr int j0 = decltype(GC)::value * G;
    constexpr int NRD = G * (MF + (BFRAG ? NF : 2 * NF)), NMM = MF * NF;
    constexpr int RPM = (NRD + NMM - 1) / NMM;
    __builtin_amdgcn_s_barrier();
    asm volatile("" ::: "memory");
    if constexpr (decltype(GC)::value == NSTEP / G - 2) pre();
    rd(IC<j0>{});
    if constexpr (G == 2) rd(IC<j0 + 1>{});
    if constexpr (j0 > 0) {
      mm(IC<j0 - 1>{});
#pragma unroll
      for (int u = 0; u < NMM; ++u) {
        __builtin_amdgcn_sched_group_barrier(0x8, 1, 0);
        __builtin_amdgcn_sched_group_barrier(0x100, RPM, 0);
      }
      __builtin_amdgcn_sched_barrier(0);
      csum(IC<j0 - 1>{});
    }
    if constexpr (G == 2) {
      __builtin_amdgcn_sched_barrier(0);
      mm(IC<j0>{});
      csum(IC<j0>{});
    }
    __builtin_amdgcn_sched_barrier(0);
    __builtin_amdgcn_s_waitcnt(0xC07F);
  };
  rolled_for<NSTEP / G, C::PER>(group);
  mm(IC<NSTEP - 1>{});
  csum(IC<NSTEP - 1>{});
}

template <int BM, int BN, int WM, int WK, int WN, int KT, int NST, bool AFRAG, bool BFRAG, bool CS, bool SPLITN,
          int PLAIN, class Hook, class Pre = NoPre>
DI bool gemm_main(char* smem, const bf16_t* __restrict__ A, int lda, const bf16_t* __restrict__ B, int ldb, int pstride,
                  int rot,
                  f32x4 (&acc)[GemmCfg<BM, BN, WM, WK, WN, KT, NST>::MF][GemmCfg<BM, BN, WM, WK, WN, KT, NST>::NFW],
                  f32x4 (&cs)[GemmCfg<BM, BN, WM, WK, WN, KT, NST>::NFW], Hook& hook, Pre pre = Pre()) {
  using C = GemmCfg<BM, BN, WM, WK, WN, KT, NST>;
  const int wave = __builtin_amdgcn_readfirstlane(vtid() >> 6);
  if (wave >= C::NW) {
    const int lw = wave - C::NW;
    __builtin_amdgcn_s_setprio(3);
    constexpr bool EVEN = (C::P % C::NL) == 0;
    if (EVEN || lw < (C::P % C::NL))
      gemm_loader<BM, BN, WM, WK, WN, KT, NST, AFRAG, BFRAG, C::PPL>(smem, A, lda, B, ldb, pstride, lw, rot, hook);
    else if constexpr (!EVEN)
      gemm_loader<BM, BN, WM, WK, WN, KT, NST, AFRAG, BFRAG, C::PPL - 1>(smem, A, lda, B, ldb, pstride, lw, rot, hook);
    return false;
  }
  const int wc = wave / (WM * WK);
  constexpr bool ALLFULL = (C::NFRAG == WN * C::NFW);
  if (ALLFULL || wc != WN - 1)
    gemm_compute<BM, BN, WM, WK, WN, KT, NST, AFRAG, BFRAG, CS, true, SPLITN, PLAIN>(smem, wave, acc, cs, pre);
  else if constexpr (!ALLFULL)
    gemm_compute<BM, BN, WM, WK, WN, KT, NST, AFRAG, BFRAG, CS, false, SPLITN, PLAIN>(smem, wave, acc, cs, pre);
  return true;
}

template <int NFW>
DI void splitk_combine(char* smem, f32x4 (&acc)[2][NFW], f32x4 (&res)[NFW], int pairid, int wk, int lane, bool fulln) {
  __syncthreads();
  char* mine = smem + (pairid * 2 + wk) * (NFW * 1024) + lane * 16;
  char* theirs = smem + (pairid * 2 + (wk ^ 1)) * (NFW * 1024) + lane * 16;
#pragma unroll
  for (int nf = 0; nf < NFW; ++nf)
    if (nf < NFW - 1 || fulln) *(f32x4*)(mine + nf * 1024) = wk ? acc[0][nf] : acc[1][nf];
  __syncthreads();
#pragma unroll
  for (int nf = 0; nf < NFW; ++nf)
    if (nf < NFW - 1 || fulln) res[nf] = (wk ? acc[1][nf] : acc[0][nf]) + *(const f32x4*)(theirs + nf * 1024);
}

DI void chan_eq(float& mean, float& m2, float mean_b, float m2_b, float n) {
  float d = mean_b - mean;
  m2 = m2 + m2_b + d * d * (0.5f * n);
  mean = 0.5f * (mean + mean_b);
}

DI void ln_consts(float mean, float var, float& mean_b, float& rinv) {
  mean_b = rbf(mean);
  float v = rbf(var);
  float t = rbf(v + 1e-5f);
  float sd = rbf(sqrtf(t));
  rinv = 1.0f / sd;
}

#define CODE_ADDR(dst, sym)                                                                             \
  do {                                                                                                  \
    unsigned lo_, hi_;                                                                                  \
    asm volatile("s_getpc_b64 s[90:91]\n\ts_add_u32 s90, s90, " sym "@rel32@lo+4\n\ts_addc_u32 s91, s91, " sym \
                 "@rel32@hi+12\n\ts_mov_b32 %0, s90\n\ts_mov_b32 %1, s91"                             \
                 : "=s"(lo_), "=s"(hi_) : : "s90", "s91", "scc");                                       \
    dst = (const char*)(((u64)hi_ << 32) | lo_);                                                        \
  } while (0)

DI float row16_sum(float v) {
  v += __builtin_bit_cast(float, __builtin_amdgcn_update_dpp(0, __builtin_bit_cast(int, v), 0x128, 0xf, 0xf, false));
  v += __builtin_bit_cast(float, __builtin_amdgcn_update_dpp(0, __builtin_bit_cast(int, v), 0x124, 0xf, 0xf, false));
  v += __builtin_bit_cast(float, __builtin_amdgcn_update_dpp(0, __builtin_bit_cast(int, v), 0x122, 0xf, 0xf, false));
  v += __builtin_bit_cast(float, __builtin_amdgcn_update_dpp(0, __builtin_bit_cast(int, v), 0x121, 0xf, 0xf, false));
  return v;
}

constexpr int CL1 = 160, CL2 = 64, CL3 = 24, CL4 = 88, CL5 = 72, CL6 = 28;

struct StatHook {
  float s1, s2;
  int o0;
  DI void operator()(const char* sb) {
    bf16x2 one2 = {(__bf16)1.0f, (__bf16)1.0f};
    u32x4 v0 = *(const u32x4*)(sb + o0);
    u32x4 v1 = *(const u32x4*)(sb + o0 + 16);
#pragma unroll
    for (int e = 0; e < 4; ++e) {
      const unsigned ua = v0[e], ub = v1[e];
      bf16x2 a = __builtin_bit_cast(bf16x2, ua), b = __builtin_bit_cast(bf16x2, ub);
      s1 = __builtin_amdgcn_fdot2_f32_bf16(a, one2, s1, false);
      s2 = __builtin_amdgcn_fdot2_f32_bf16(a, a, s2, false);
      s1 = __builtin_amdgcn_fdot2_f32_bf16(b, one2, s1, false);
      s2 = __builtin_amdgcn_fdot2_f32_bf16(b, b, s2, false);
    }
  }
};

extern "C" __global__ __launch_bounds__(1024, 4) void k1_qkv(const bf16_t* __restrict__ x, const bf16_t* __restrict__ w,
                                                 bf16_t* __restrict__ qkf, bf16_t* __restrict__ v2,
                                                 u32x4* __restrict__ exz, const bf16_t* __restrict__ wo,
                                                 const bf16_t* __restrict__ win, float* __restrict__ wcs) {
  constexpr int BM = 64, BN = 192, WM = 2, WK = 1, WN = 4, KT = 8, NST = 4;
  using C = GemmCfg<BM, BN, WM, WK, WN, KT, NST>;
  __shared__ __attribute__((aligned(16))) char smem[C::LDS + 2048];
  if (vtid() < 64) exz[blockIdx.x * 64 + vtid()] = u32x4{0u, 0u, 0u, 0u};
  if (blockIdx.x == 0 && vtid() < 2) {
    const unsigned cv = vtid() ? 0u : 0x3f803f80u;
    exz[65536 + vtid()] = u32x4{cv, cv, cv, cv};
  }

  const int b = blockIdx.x, xcd = b & 7, idx = b >> 3;
  const int tm = xcd * 4 + (idx >> 3), tn = idx & 7;
  const int m0 = tm * BM, n0 = tn * BN;
  f32x4 acc[C::MF][C::NFW], cs[C::NFW];
#pragma unroll
  for (int i = 0; i < C::MF; ++i)
#pragma unroll
    for (int j = 0; j < C::NFW; ++j) acc[i][j] = f32x4{0.f, 0.f, 0.f, 0.f};
#pragma unroll
  for (int j = 0; j < C::NFW; ++j) cs[j] = f32x4{0.f, 0.f, 0.f, 0.f};
  StatHook hook;
  const int srow = (vtid() >> 2) & 63, sq = vtid() & 3;
  hook.s1 = 0.f; hook.s2 = 0.f;
  hook.o0 = srow * 128 + (sq << 5);
  const int lane = vtid() & 63, wave = __builtin_amdgcn_readfirstlane(vtid() >> 6), wr = wave & 1,
            wc = (wave >> 1) & 3;
  const int nw0 = n0 + wc * 48;
  const int plain = (nw0 >= 1024) ? 7 : ((nw0 + 16 >= 1024) ? 6 : 0);
  const bool comp = wave < 8;
  if (wave >= 12) {
    {
      const char *c1, *c2, *c3, *c4, *c5, *c6;
      CODE_ADDR(c1, "k1_qkv"); CODE_ADDR(c2, "k2_attn"); CODE_ADDR(c3, "k3_wo");
      CODE_ADDR(c4, "k4_in"); CODE_ADDR(c5, "k5_sp"); CODE_ADDR(c6, "k6_out");
      const int L = idx * 16 + (lane & 15);
      if (wave == 12 && lane < 16) {
        const char* pc = (L < CL1) ? c1 + L * 128
                       : (L < CL1 + CL2) ? c2 + (L - CL1) * 128
                       : (L < CL1 + CL2 + CL3) ? c3 + (L - CL1 - CL2) * 128
                       : (L < CL1 + CL2 + CL3 + CL4) ? c4 + (L - CL1 - CL2 - CL3) * 128
                       : (L < CL1 + CL2 + CL3 + CL4 + CL5) ? c5 + (L - CL1 - CL2 - CL3 - CL4) * 128
                       : (L < CL1 + CL2 + CL3 + CL4 + CL5 + CL6) ? c6 + (L - CL1 - CL2 - CL3 - CL4 - CL5) * 128
                       : c1;
        const unsigned cvv = *(const unsigned*)pc;
        asm volatile("" ::"v"(cvv));
      }
    }
#pragma unroll
    for (int kt = 0; kt < KT; ++kt) {
      __builtin_amdgcn_s_barrier();
      asm volatile("" ::: "memory");
      hook(smem + (kt % NST) * C::STAGE);
      __builtin_amdgcn_s_waitcnt(0xC07F);
    }
  } else if (wave >= 8) {
    __builtin_amdgcn_s_setprio(3);
    NoHook nh;
    gemm_loader<BM, BN, WM, WK, WN, KT, NST, false, false, C::PPL>(smem, x + (size_t)m0 * D_MODEL, D_MODEL, w + n0,
                                                                   3 * D_MODEL, 128, wave - 8, 0, nh);
    {
      const int lt = vtid() - 512;
      if (lt < 72) {
        const char* p0 = (lt < 16) ? (const char*)wo + (size_t)(b * 16 + lt) * 128
                                   : (const char*)win + (size_t)(b * 56 + lt - 16) * 128;
        unsigned v0 = *(const unsigned*)p0;
        asm volatile("" ::"v"(v0));
      }
      if (b < 28) {
        const bf16_t* wp = win + (size_t)((lt >> 3) * 16) * (2 * FH) + b * 64 + (lt & 7) * 8;
        float a[8];
#pragma unroll
        for (int e = 0; e < 8; ++e) a[e] = 0.f;
#pragma unroll
        for (int i = 0; i < 16; ++i) {
          const u32x4 v = *(const u32x4*)(wp + (size_t)i * (2 * FH));
#pragma unroll
          for (int e = 0; e < 4; ++e) { a[2 * e] += lo_f(v[e]); a[2 * e + 1] += hi_f(v[e]); }
        }
#pragma unroll
        for (int e = 0; e < 8; ++e) {
          a[e] += __shfl_xor(a[e], 8);
          a[e] += __shfl_xor(a[e], 16);
          a[e] += __shfl_xor(a[e], 32);
        }
        if ((lt & 63) < 8) {
          float* dp = wcs + (lt >> 6) * (2 * FH) + b * 64 + (lt & 7) * 8;
          *(f32x4*)dp = f32x4{a[0], a[1], a[2], a[3]};
          *(f32x4*)(dp + 4) = f32x4{a[4], a[5], a[6], a[7]};
        }
      }
    }
    return;
  } else if (plain == 7) {
    gemm_compute<BM, BN, WM, WK, WN, KT, NST, false, false, true, true, false, 7>(smem, wave, acc, cs);
  } else if (plain == 6) {
    gemm_compute<BM, BN, WM, WK, WN, KT, NST, false, false, true, true, false, 6>(smem, wave, acc, cs);
  } else {
    gemm_compute<BM, BN, WM, WK, WN, KT, NST, false, false, true, true, false, 0>(smem, wave, acc, cs);
  }

  float* tmean = (float*)(smem + C::LDS);
  float* trinv = tmean + 64;
  float* tcs = trinv + 64;
  if (!comp) {
    float s1 = hook.s1 + __shfl_xor(hook.s1, 1);
    float s2 = hook.s2 + __shfl_xor(hook.s2, 1);
    s1 += __shfl_xor(s1, 2);
    s2 += __shfl_xor(s2, 2);
    const float mean = s1 * (1.0f / 512.0f), ex2 = s2 * (1.0f / 512.0f);
    float var = ex2 - mean * mean;
    if (var < ex2 * (1.0f / 1024.0f)) {
      const bf16_t* xr = x + (size_t)(m0 + srow) * D_MODEL + sq * 128;
      float a2 = 0.f;
      for (int j = 0; j < 16; ++j) {
        u32x4 v = *(const u32x4*)(xr + j * 8);
#pragma unroll
        for (int e = 0; e < 4; ++e) {
          float d0 = lo_f(v[e]) - mean, d1 = hi_f(v[e]) - mean;
          a2 = fmaf(d0, d0, a2);
          a2 = fmaf(d1, d1, a2);
        }
      }
      a2 += __shfl_xor(a2, 1);
      a2 += __shfl_xor(a2, 2);
      var = a2 * (1.0f / 512.0f);
    }
    float mb, ri;
    ln_consts(mean, var, mb, ri);
    if (sq == 0) { tmean[srow] = mb; trinv[srow] = ri; }
    __syncthreads();
    return;
  }
  const int g = lane >> 4;
#pragma unroll
  for (int nf = 0; nf < C::NFW; ++nf) {
    if ((plain >> nf) & 1) {
      if (lane < 16) tcs[wr * 192 + wc * 48 + nf * 16 + lane] = cs[nf][0];
    } else if ((lane & 15) == 0) {
      *(f32x4*)(tcs + wr * 192 + wc * 48 + nf * 16 + 4 * g) = cs[nf];
    }
  }
  __syncthreads();
#pragma unroll
  for (int nf = 0; nf < C::NFW; ++nf) {
    if ((plain >> nf) & 1) {
      const int nl = wc * 48 + nf * 16 + (lane & 15);
      const float c = tcs[nl] + tcs[192 + nl];
      const int nv = n0 + nl - 1024;
#pragma unroll
      for (int mf = 0; mf < C::MF; ++mf) {
        const int ml = wr * 32 + mf * 16 + 4 * g;
        const f32x4 mb = *(const f32x4*)(tmean + ml), ri = *(const f32x4*)(trinv + ml);
        u32x2 o = {pack2((acc[mf][nf][0] - mb[0] * c) * ri[0], (acc[mf][nf][1] - mb[1] * c) * ri[1]),
                   pack2((acc[mf][nf][2] - mb[2] * c) * ri[2], (acc[mf][nf][3] - mb[3] * c) * ri[3])};
        const int m = m0 + ml;
        ST2(v2 + (((size_t)(nv >> 4) * 256 + (m >> 3)) * 16 + (nv & 15)) * 8 + (m & 7), o);
      }
    } else {
      const int nl = wc * 48 + nf * 16 + 4 * g;
      f32x4 c = *(const f32x4*)(tcs + nl) + *(const f32x4*)(tcs + 192 + nl);
      const int n = n0 + nl;
#pragma unroll
      for (int mf = 0; mf < C::MF; ++mf) {
        const int ml = wr * 32 + mf * 16 + (lane & 15);
        const float mb = tmean[ml], ri = trinv[ml];
        u32x2 o = {pack2((acc[mf][nf][0] - mb * c[0]) * ri, (acc[mf][nf][1] - mb * c[1]) * ri),
                   pack2((acc[mf][nf][2] - mb * c[2]) * ri, (acc[mf][nf][3] - mb * c[3]) * ri)};
        ST2(qkf + ((size_t)(n >> 3) * S_LEN + m0 + ml) * 8 + (n & 7), o);
      }
    }
  }
}

template <bool EDGE>
DI void attn_body(const bf16_t* __restrict__ qkf, const bf16_t* __restrict__ v2, const bf16_t* __restrict__ cst,
                  bf16_t* __restrict__ o, int qt, int h, const char* __restrict__ pf) {
  const int lane = threadIdx.x & 63;
  const int r = lane & 31, hf = lane >> 5;
  const int q0 = qt * 32;
  const int t0 = EDGE ? 8 - qt : 0;

  const bf16x8 qf = *(const bf16x8*)(qkf + ((size_t)(h * 2 + hf) * S_LEN + q0 + r) * 8);
  const int pr = 16 * ((r >> 2) & 1) + 4 * (r >> 3) + (r & 3);
  const bf16_t* kp = qkf + ((size_t)(64 + h * 2 + hf) * S_LEN + pr) * 8;
  bf16x8 kf[9];
#pragma unroll
  for (int t = 0; t < 9; ++t) {
    int kb = q0 - 256 + 32 * t;
    if (EDGE && kb < 0) kb = 0;
    kf[t] = *(const bf16x8*)(kp + kb * 8);
  }
  bf16x8 vf[9][2];
  const bf16_t* vp = (r < 16) ? v2 + ((size_t)h * 256 * 16 + 2 * hf * 16 + r) * 8 : cst + ((r == 16) ? 0 : 8);
  const int vm = (r < 16) ? 16 : 0;
#pragma unroll
  for (int t = 0; t < 9; ++t) {
    int kb = q0 - 256 + 32 * t;
    if (EDGE && kb < 0) kb = 0;
    vf[t][0] = *(const bf16x8*)(vp + kb * vm);
    vf[t][1] = *(const bf16x8*)(vp + (kb + 8) * vm);
  }
  const unsigned touch = *(const unsigned*)pf;
  f32x16 s[9];
  f32x16 zero;
#pragma unroll
  for (int i = 0; i < 16; ++i) zero[i] = 0.f;
#pragma unroll
  for (int t = 0; t < 9; ++t) s[t] = __builtin_amdgcn_mfma_f32_32x32x16_bf16(kf[t], qf, zero, 0, 0, 0);

  const float NEG = -3.0e38f;
#pragma unroll
  for (int i = 0; i < 16; ++i) {
    if (!(16 * hf + i > r)) s[0][i] = NEG;
    if (!(16 * hf + i <= r)) s[8][i] = NEG;
  }
  if constexpr (EDGE) {
#pragma unroll
    for (int t = 0; t < 8; ++t) {
      if (t < t0) {
#pragma unroll
        for (int i = 0; i < 16; ++i) s[t][i] = NEG;
      }
    }
  }
  float mx = NEG;
#pragma unroll
  for (int t = 0; t < 9; ++t)
#pragma unroll
    for (int i = 0; i < 16; ++i) mx = fmaxf(mx, s[t][i]);
  mx = fmaxf(mx, __shfl_xor(mx, 32));
  const float c = 0.25f * 1.4426950408889634f;
  const f32x2 c2 = {c, c}, mc2 = {-mx * c, -mx * c};
  f32x16 oacc = zero;
#pragma unroll
  for (int t = 0; t < 9; ++t) {
#pragma unroll
    for (int st = 0; st < 2; ++st) {
      bf16x8 pf;
#pragma unroll
      for (int j = 0; j < 8; j += 2) {
        f32x2 e = {s[t][8 * st + j], s[t][8 * st + j + 1]};
        e = __builtin_elementwise_fma(e, c2, mc2);
        pf[j] = (__bf16)__builtin_amdgcn_exp2f(e[0]);
        pf[j + 1] = (__bf16)__builtin_amdgcn_exp2f(e[1]);
      }
      oacc = __builtin_amdgcn_mfma_f32_32x32x16_bf16(vf[t][st], pf, oacc, 0, 0, 0);
    }
  }
  const float l = __shfl(oacc[8], r);
  const float il = 1.0f / l;
  bf16_t* op = o + fragaddr(q0 + r, h * 16 + 4 * hf, 16);
  u32x2 o0 = {pack2(oacc[0] * il, oacc[1] * il), pack2(oacc[2] * il, oacc[3] * il)};
  u32x2 o1 = {pack2(oacc[4] * il, oacc[5] * il), pack2(oacc[6] * il, oacc[7] * il)};
  ST2(op, o0);
  ST2(op + 128, o1);
  asm volatile("" ::"v"(touch));
}

extern "C" __global__ __launch_bounds__(512) void k2_attn(const bf16_t* __restrict__ qkf, const bf16_t* __restrict__ v2,
                                               const bf16_t* __restrict__ cst, bf16_t* __restrict__ o,
                                               const bf16_t* __restrict__ wsp, const bf16_t* __restrict__ wout) {
  const int b = blockIdx.x, xcd = b & 7, idx = b >> 3;
  const int qt = xcd * 8 + (idx >> 2), hg = idx & 3;
  const int h = hg * 8 + (threadIdx.x >> 6);
  const int gl = b * 512 + threadIdx.x;
  const char* pf = (gl < 65536) ? (const char*)wsp + (size_t)gl * 128
                                : (const char*)wout + (size_t)((gl - 65536) % 7168) * 128;
  if (qt < 8) attn_body<true>(qkf, v2, cst, o, qt, h, pf);
  else attn_body<false>(qkf, v2, cst, o, qt, h, pf);
}

extern "C" __global__ __launch_bounds__(768, 3) void k3_wo(const bf16_t* __restrict__ o, const bf16_t* __restrict__ w,
                                                const bf16_t* __restrict__ x, bf16_t* __restrict__ h,
                                                f32x2* __restrict__ stats) {
  constexpr int BM = 64, BN = 64, WM = 2, WK = 2, WN = 2, KT = 8, NST = 8;
  using C = GemmCfg<BM, BN, WM, WK, WN, KT, NST>;
  __shared__ __attribute__((aligned(16))) char smem[C::LDS + 8192];
  const int b = blockIdx.x, xcd = b & 7, idx = b >> 3;
  const int tm = xcd * 4 + (idx >> 3), tn = idx & 7;
  const int m0 = tm * BM, n0 = tn * BN;
  const int lane = vtid() & 63, wave = vtid() >> 6, wr = wave & 1, wk = (wave >> 1) & 1, wc = (wave >> 2) & 1;
  char* xs = smem + C::LDS;
  if (wave >= 8) {
    const int lw = __builtin_amdgcn_readfirstlane(wave) - 8;
#pragma unroll
    for (int i = 0; i < 2; ++i) {
      const int p = lw + 4 * i;
      glds16((const char*)(x + (size_t)(m0 + 8 * p + (lane >> 3)) * D_MODEL + n0) + (((lane & 7) ^ (lane >> 3)) << 4),
             xs + p * 1024);
    }
  }
  f32x4 acc[C::MF][C::NFW], cs[C::NFW], r[C::NFW];
#pragma unroll
  for (int i = 0; i < C::MF; ++i)
#pragma unroll
    for (int j = 0; j < C::NFW; ++j) acc[i][j] = f32x4{0.f, 0.f, 0.f, 0.f};
  NoHook hook;
  if (!gemm_main<BM, BN, WM, WK, WN, KT, NST, true, false, false, false, 0>(
          smem, o + (size_t)m0 * D_MODEL, 16, w + n0, D_MODEL, 128, (2 * (idx >> 3) + (idx & 7)) & 7, acc, cs, hook)) {
    return;
  }
  splitk_combine<2>(smem, acc, r, wr + 2 * wc, wk, lane, true);
  const int ml = wr * 32 + wk * 16 + (lane & 15);
  const int nl = wc * 32 + 4 * (lane >> 4);
  const u32x2 rv0 = *(const u32x2*)(xs + ml * 128 + (((nl >> 3) ^ (ml & 7)) << 4) + (nl & 4) * 2);
  const u32x2 rv1 = *(const u32x2*)(xs + ml * 128 + ((((nl + 16) >> 3) ^ (ml & 7)) << 4) + (nl & 4) * 2);
  float hv[8];
  hv[0] = rbf(lo_f(rv0[0]) + rbf(r[0][0])); hv[1] = rbf(hi_f(rv0[0]) + rbf(r[0][1]));
  hv[2] = rbf(lo_f(rv0[1]) + rbf(r[0][2])); hv[3] = rbf(hi_f(rv0[1]) + rbf(r[0][3]));
  hv[4] = rbf(lo_f(rv1[0]) + rbf(r[1][0])); hv[5] = rbf(hi_f(rv1[0]) + rbf(r[1][1]));
  hv[6] = rbf(lo_f(rv1[1]) + rbf(r[1][2])); hv[7] = rbf(hi_f(rv1[1]) + rbf(r[1][3]));
  u32x2 o0 = {pack2(hv[0], hv[1]), pack2(hv[2], hv[3])};
  u32x2 o1 = {pack2(hv[4], hv[5]), pack2(hv[6], hv[7])};
  bf16_t* hp = h + fragaddr(m0 + ml, n0 + nl, 16);
  ST2(hp, o0);
  ST2(hp + 256, o1);
  float mean = 0.f;
#pragma unroll
  for (int j = 0; j < 8; ++j) mean += hv[j];
  mean *= 0.125f;
  float m2 = 0.f;
#pragma unroll
  for (int j = 0; j < 8; ++j) { float d = hv[j] - mean; m2 = fmaf(d, d, m2); }
  chan_eq(mean, m2, __shfl_xor(mean, 16), __shfl_xor(m2, 16), 8.f);
  chan_eq(mean, m2, __shfl_xor(mean, 32), __shfl_xor(m2, 32), 16.f);
  if (lane < 16) stats[(size_t)(tn * 2 + wc) * S_LEN + m0 + ml] = f32x2{mean, m2};
}

DI f32x2 gelu2(f32x2 x) {
  const float k0 = -0.7978845608028654f * 2.0f * 1.4426950408889634f;
  const f32x2 one = {1.0f, 1.0f}, c1 = {0.044715f, 0.044715f}, kk = {k0, k0};
  f32x2 t = __builtin_elementwise_fma(x * x, c1, one);
  f32x2 y = (x * kk) * t;
  f32x2 e = {__builtin_amdgcn_exp2f(y[0]), __builtin_amdgcn_exp2f(y[1])};
  e = e + one;
  f32x2 r = {__builtin_amdgcn_rcpf(e[0]), __builtin_amdgcn_rcpf(e[1])};
  return x * r;
}
DI f32x2 rbf2(f32x2 x) {
  const unsigned u = pack2(x[0], x[1]);
  return f32x2{lo_f(u), hi_f(u)};
}

extern "C" __global__ __launch_bounds__(768, 3) void k4_in(const bf16_t* __restrict__ h, const bf16_t* __restrict__ w,
                                                const f32x2* __restrict__ hstats, bf16_t* __restrict__ z1c,
                                                bf16_t* __restrict__ z2f, u64* __restrict__ ex,
                                                const float* __restrict__ wcs) {
  constexpr int BM = 128, BN = 128, WM = 4, WK = 1, WN = 2, KT = 8, NST = 4;
  using C = GemmCfg<BM, BN, WM, WK, WN, KT, NST>;
  __shared__ __attribute__((aligned(16))) char smem[C::LDS + 8192];
  const int b = blockIdx.x, xcd = b & 7, idx = b >> 3;
  const int tm = xcd * 2 + (idx >= 14 ? 1 : 0), tn = (idx >= 14) ? idx - 14 : idx;
  const int m0 = tm * BM, n0 = tn * 64;
  const int lane = vtid() & 63, wave = vtid() >> 6, wr = wave & 3, wc = (wave >> 2) & 1;
  const int g = lane >> 4;
  float* tmean = (float*)(smem + C::LDS);
  float* trinv = tmean + 128;
  float* tcs = trinv + 128;
  float* tmean2 = tcs + 512;
  float* trinv2 = tmean2 + 128;
  float* tpart = trinv2 + 128;
  f32x2* tpart2 = (f32x2*)(tpart + 512);
  f32x4 acc[C::MF][C::NFW], cs[C::NFW];
#pragma unroll
  for (int i = 0; i < C::MF; ++i)
#pragma unroll
    for (int j = 0; j < C::NFW; ++j) acc[i][j] = f32x4{0.f, 0.f, 0.f, 0.f};
#pragma unroll
  for (int j = 0; j < C::NFW; ++j) cs[j] = f32x4{0.f, 0.f, 0.f, 0.f};
  f32x2 hv[8];
  float cv[4];
  if (vtid() >= 512) {
    const f32x2* hp = hstats + (size_t)((vtid() >> 7) & 1) * 8 * S_LEN + m0 + (vtid() & 127);
#pragma unroll
    for (int j = 0; j < 8; ++j) hv[j] = hp[(size_t)j * S_LEN];
    const float* cp = wcs + n0 + (vtid() & 63) + ((vtid() >> 6) & 1) * FH;
#pragma unroll
    for (int j = 0; j < 4; ++j) cv[j] = cp[j * 2 * FH];
  }
  NoHook hook;
  const bool comp = gemm_main<BM, BN, WM, WK, WN, KT, NST, true, false, false, true, 12>(
      smem, h + (size_t)m0 * D_MODEL, 16, w + n0, 2 * FH, FH * 2, ((idx >= 14 ? 4 : 0) + tn) & 7, acc, cs, hook);
  if (!comp) {
    const int lt = vtid() - 512;
    float mean = 0.f;
#pragma unroll
    for (int j = 0; j < 8; ++j) mean += hv[j][0];
    mean *= 0.125f;
    float m2 = 0.f;
#pragma unroll
    for (int j = 0; j < 8; ++j) { float d = hv[j][0] - mean; m2 += hv[j][1] + 32.f * d * d; }
    if (lt >= 128) tpart2[lt - 128] = f32x2{mean, m2};
    else tcs[lt] = (cv[0] + cv[1]) + (cv[2] + cv[3]);
    __syncthreads();
    if (lt < 128) {
      const f32x2 o = tpart2[lt];
      chan_eq(mean, m2, o[0], o[1], 256.f);
      float mb, ri;
      ln_consts(mean, m2 * (1.0f / 512.0f), mb, ri);
      tmean[lt] = mb;
      trinv[lt] = ri;
    }
    __syncthreads();
    return;
  }
  __syncthreads();
  __syncthreads();
  u64* exp_ = ex + (size_t)tm * (14 * 128);
  {
    float c2[2];
#pragma unroll
    for (int nf = 0; nf < 2; ++nf) {
      const int nl = 64 + wc * 32 + nf * 16 + (lane & 15);
      c2[nf] = tcs[nl];
    }
#pragma unroll
    for (int mf = 0; mf < C::MF; ++mf) {
      const int ml = wr * 32 + mf * 16 + 4 * g;
      const f32x4 mb = *(const f32x4*)(tmean + ml), ri = *(const f32x4*)(trinv + ml);
      float pm = 0.f, pq = 0.f;
#pragma unroll
      for (int r = 0; r < 4; ++r) {
        const f32x2 u2 = (f32x2{acc[mf][2][r], acc[mf][3][r]} - f32x2{c2[0], c2[1]} * mb[r]) * ri[r];
        const f32x2 z2 = rbf2(gelu2(rbf2(u2)));
        const float a = z2[0], bq = z2[1];
        acc[mf][2][r] = a;
        acc[mf][3][r] = bq;
        const float mean = row16_sum(a + bq) * (1.0f / 32.0f);
        const float da = a - mean, db = bq - mean;
        const float m2 = row16_sum(fmaf(da, da, db * db));
        if ((lane & 3) == r) { pm = mean; pq = m2; }
      }
      if ((lane & 15) < 4) ((f32x2*)tpart)[wc * 128 + ml + (lane & 3)] = f32x2{pm, pq};
    }
  }
  __syncthreads();
  if (vtid() < 128) {
    f32x2 a = ((const f32x2*)tpart)[vtid()], bq = ((const f32x2*)tpart)[128 + vtid()];
    float mean = a[0], m2 = a[1];
    chan_eq(mean, m2, bq[0], bq[1], 32.f);
    __hip_atomic_store(exp_ + tn * 128 + vtid(),
                       ((u64)(__float_as_uint(m2) | 1u) << 32) | (u64)__float_as_uint(mean), __ATOMIC_RELAXED,
                       __HIP_MEMORY_SCOPE_AGENT);
  }
#pragma unroll
  for (int nf = 0; nf < 2; ++nf) {
    const int nl = wc * 32 + nf * 16 + 4 * g;
    const f32x4 c = *(const f32x4*)(tcs + nl);
#pragma unroll
    for (int mf = 0; mf < C::MF; ++mf) {
      const int ml = wr * 32 + mf * 16 + (lane & 15);
      const float mb = tmean[ml], ri = trinv[ml];
      const f32x2 ua = (f32x2{acc[mf][nf][0], acc[mf][nf][1]} - f32x2{c[0], c[1]} * mb) * ri;
      const f32x2 ub = (f32x2{acc[mf][nf][2], acc[mf][nf][3]} - f32x2{c[2], c[3]} * mb) * ri;
      const f32x2 va = gelu2(rbf2(ua)), vb = gelu2(rbf2(ub));
      u32x2 ov = {pack2(va[0], va[1]), pack2(vb[0], vb[1])};
      ST2(z1c + ((size_t)((m0 >> 4) + wr * 2 + mf) * 56 + (n0 >> 4) + wc * 2 + nf) * 256 + lane * 4, ov);
    }
  }
  if (vtid() < 256) {
    const int t = vtid(), row = t & 127, half = t >> 7;
    const u64* gp = exp_ + half * (7 * 128) + row;
    float vm[7], vq[7];
    for (unsigned spins = 0;; ++spins) {
      bool ok = true;
#pragma unroll
      for (int j = 0; j < 7; ++j) {
        u64 gg = __hip_atomic_load(gp + j * 128, __ATOMIC_RELAXED, __HIP_MEMORY_SCOPE_AGENT);
        vm[j] = __uint_as_float((unsigned)gg);
        vq[j] = __uint_as_float((unsigned)(gg >> 32));
        ok &= ((unsigned)(gg >> 32) != 0u);
      }
      if (__all(ok)) break;
      if (spins > 4000000u) break;
      __builtin_amdgcn_s_sleep(1);
    }
    float hm = 0.f, hq = 0.f;
#pragma unroll
    for (int j = 0; j < 7; ++j) { hm += vm[j]; hq += vq[j]; }
    hm *= (1.0f / 7.0f);
#pragma unroll
    for (int j = 0; j < 7; ++j) { float d = vm[j] - hm; hq = fmaf(64.f * d, d, hq); }
    tpart2[t] = f32x2{hm, hq};
  }
  __syncthreads();
  if (vtid() < 128) {
    f32x2 a = tpart2[vtid()], bq = tpart2[128 + vtid()];
    float mean = a[0], m2 = a[1];
    chan_eq(mean, m2, bq[0], bq[1], 448.f);
    float mb, ri;
    ln_consts(mean, m2 * (1.0f / 896.0f), mb, ri);
    tmean2[vtid()] = mb;
    trinv2[vtid()] = ri;
  }
  __syncthreads();
#pragma unroll
  for (int mf = 0; mf < C::MF; ++mf) {
    const int ml = wr * 32 + mf * 16 + 4 * g;
    const f32x4 mb = *(const f32x4*)(tmean2 + ml), ri = *(const f32x4*)(trinv2 + ml);
#pragma unroll
    for (int nf = 2; nf < 4; ++nf) {
      const int n = n0 + wc * 32 + (nf & 1) * 16 + (lane & 15);
      float v0 = rbf(acc[mf][nf][0] - mb[0]) * ri[0], v1 = rbf(acc[mf][nf][1] - mb[1]) * ri[1];
      float v2 = rbf(acc[mf][nf][2] - mb[2]) * ri[2], v3 = rbf(acc[mf][nf][3] - mb[3]) * ri[3];
      u32x2 ov = {pack2(v0, v1), pack2(v2, v3)};
      ST2(z2f + fragaddr(n, m0 + ml, 64), ov);
    }
  }
}

extern "C" __global__ __launch_bounds__(1024, 4) void k5_sp(const bf16_t* __restrict__ wsp, const bf16_t* __restrict__ z2f,
                                                const bf16_t* __restrict__ bsp, const bf16_t* __restrict__ z1c,
                                                bf16_t* __restrict__ y) {
  constexpr int BM = 64, BN = 112, WM = 2, WK = 2, WN = 2, KT = 32, NST = (SPB5 == 2) ? 6 : 7;
  using C = GemmCfg<BM, BN, WM, WK, WN, KT, NST>;
  __shared__ __attribute__((aligned(16))) char smem[C::LDS];
  const int b = blockIdx.x, xcd = b & 7, idx = b >> 3;
  const int tm = (xcd >> 2) * 16 + (idx >> 1), tn = (xcd & 3) * 2 + (idx & 1);
  const int m0 = tm * BM, n0 = tn * BN;
  const int lane = vtid() & 63, wave = vtid() >> 6, wr = wave & 1, wk = (wave >> 1) & 1, wc = (wave >> 2) & 1;
  const bool fulln = (wc == 0);
  const int m = m0 + wr * 32 + wk * 16 + (lane & 15);
  const int nb = n0 + wc * 64;
  float bb = 0.f;
  u32x2 zv[4];
  auto pre = [&]() {
    bb = (float)bsp[m];
#pragma unroll
    for (int nf = 0; nf < 4; ++nf)
      if (nf < 3 || fulln) zv[nf] = *(const u32x2*)(z1c + ((size_t)(m >> 4) * 56 + (nb >> 4) + nf) * 256 + lane * 4);
  };
  f32x4 acc[C::MF][C::NFW], cs[C::NFW], r[C::NFW];
#pragma unroll
  for (int i = 0; i < C::MF; ++i)
#pragma unroll
    for (int j = 0; j < C::NFW; ++j) acc[i][j] = f32x4{0.f, 0.f, 0.f, 0.f};
  NoHook hook;
  if (!gemm_main<BM, BN, WM, WK, WN, KT, NST, false, true, false, false, 0>(
          smem, wsp + (size_t)m0 * S_LEN, S_LEN, z2f + (size_t)(n0 >> 4) * 64 * 512, 64, 0,
          (idx >> 1) & 7, acc, cs, hook, pre))
    return;
  splitk_combine<4>(smem, acc, r, wr + 2 * wc, wk, lane, fulln);
#pragma unroll
  for (int nf = 0; nf < C::NFW; ++nf) {
    if (nf < 3 || fulln) {
      float v0 = lo_f(zv[nf][0]) * rbf(rbf(r[nf][0]) + bb);
      float v1 = hi_f(zv[nf][0]) * rbf(rbf(r[nf][1]) + bb);
      float v2 = lo_f(zv[nf][1]) * rbf(rbf(r[nf][2]) + bb);
      float v3 = hi_f(zv[nf][1]) * rbf(rbf(r[nf][3]) + bb);
      u32x2 ov = {pack2(v0, v1), pack2(v2, v3)};
      ST2(y + fragaddr(m, nb + nf * 16 + 4 * (lane >> 4), 28), ov);
    }
  }
}

extern "C" __global__ __launch_bounds__(768, 3) void k6_out(const bf16_t* __restrict__ y, const bf16_t* __restrict__ w,
                                                 const bf16_t* __restrict__ h, bf16_t* __restrict__ out) {
  constexpr int BM = 64, BN = 64, WM = 2, WK = 2, WN = 2, KT = 14, NST = (SPB6 == 2) ? 8 : 9;
  using C = GemmCfg<BM, BN, WM, WK, WN, KT, NST>;
  __shared__ __attribute__((aligned(16))) char smem[C::LDS + 8192];
  const int b = blockIdx.x, xcd = b & 7, idx = b >> 3;
  const int tm = xcd * 4 + (idx >> 3), tn = idx & 7;
  const int m0 = tm * BM, n0 = tn * BN;
  const int lane = vtid() & 63, wave = vtid() >> 6, wr = wave & 1, wk = (wave >> 1) & 1, wc = (wave >> 2) & 1;
  const int ml = wr * 32 + wk * 16 + (lane & 15);
  const int nl = wc * 32 + 4 * (lane >> 4);
  u32x2 rv0 = {0u, 0u}, rv1 = {0u, 0u};
  auto pre = [&]() {
    const bf16_t* hp = h + fragaddr(m0 + ml, n0 + nl, 16);
    rv0 = *(const u32x2*)(hp);
    rv1 = *(const u32x2*)(hp + 256);
  };
  f32x4 acc[C::MF][C::NFW], cs[C::NFW], r[C::NFW];
#pragma unroll
  for (int i = 0; i < C::MF; ++i)
#pragma unroll
    for (int j = 0; j < C::NFW; ++j) acc[i][j] = f32x4{0.f, 0.f, 0.f, 0.f};
  NoHook hook;
  if (!gemm_main<BM, BN, WM, WK, WN, KT, NST, true, false, false, false, 0>(
          smem, y + (size_t)(m0 >> 4) * 28 * 512, 28, w + n0, D_MODEL, 128, ((idx >> 3) * 3 + (idx & 7) * 2) % KT, acc, cs,
          hook, pre))
    return;
  splitk_combine<2>(smem, acc, r, wr + 2 * wc, wk, lane, true);
  u32x2 o0 = {pack2(lo_f(rv0[0]) + rbf(r[0][0]), hi_f(rv0[0]) + rbf(r[0][1])),
              pack2(lo_f(rv0[1]) + rbf(r[0][2]), hi_f(rv0[1]) + rbf(r[0][3]))};
  u32x2 o1 = {pack2(lo_f(rv1[0]) + rbf(r[1][0]), hi_f(rv1[0]) + rbf(r[1][1])),
              pack2(lo_f(rv1[1]) + rbf(r[1][2]), hi_f(rv1[1]) + rbf(r[1][3]))};
  char* os = smem + C::LDS;
  *(u32x2*)(os + ml * 128 + (((nl >> 3) ^ (ml & 7)) << 4) + (nl & 4) * 2) = o0;
  *(u32x2*)(os + ml * 128 + ((((nl + 16) >> 3) ^ (ml & 7)) << 4) + (nl & 4) * 2) = o1;
  __syncthreads();
  const int row = vtid() >> 3, ch = vtid() & 7;
  const u32x4 ov = *(const u32x4*)(os + row * 128 + ((ch ^ (row & 7)) << 4));
  *(u32x4*)(out + (size_t)(m0 + row) * D_MODEL + n0 + ch * 8) = ov;
}

#define RUNK(i)
extern "C" void kernel_launch(void* const* d_in, const int* in_sizes, int n_in, void* d_out, int out_size,
                              void* d_ws, size_t ws_size, hipStream_t stream) {
  const bf16_t* x = (const bf16_t*)d_in[0];
  const bf16_t* wqkv = (const bf16_t*)d_in[1];
  const bf16_t* wo = (const bf16_t*)d_in[2];
  const bf16_t* w_in = (const bf16_t*)d_in[4];
  const bf16_t* w_sp = (const bf16_t*)d_in[5];
  const bf16_t* b_sp = (const bf16_t*)d_in[6];
  const bf16_t* w_out = (const bf16_t*)d_in[7];
  bf16_t* out = (bf16_t*)d_out;

  char* ws = (char*)d_ws;
  size_t off = 0;
  auto take = [&](size_t bytes) { char* p = ws + off; off += (bytes + 255) & ~(size_t)255; return p; };
  bf16_t* qkf = (bf16_t*)take((size_t)S_LEN * 1024 * 2);
  bf16_t* v2 = (bf16_t*)take((size_t)512 * S_LEN * 2);
  bf16_t* o = (bf16_t*)take((size_t)S_LEN * D_MODEL * 2);
  bf16_t* h = (bf16_t*)take((size_t)S_LEN * D_MODEL * 2);
  f32x2* hstats = (f32x2*)take((size_t)S_LEN * 16 * 8);
  bf16_t* z1c = (bf16_t*)take((size_t)S_LEN * FH * 2);
  bf16_t* z2f = (bf16_t*)take((size_t)S_LEN * FH * 2);
  bf16_t* y = (bf16_t*)take((size_t)S_LEN * FH * 2);
  float* wcs = (float*)take((size_t)4 * 2 * FH * 4);
  u64* ex = (u64*)take((size_t)1024 * 1024 + 256);

  RUNK(1) k1_qkv<<<256, 1024, 0, stream>>>(x, wqkv, qkf, v2, (u32x4*)ex, wo, w_in, wcs);
  RUNK(2) k2_attn<<<256, 512, 0, stream>>>(qkf, v2, (const bf16_t*)(ex + 131072), o, w_sp, w_out);
  RUNK(3) k3_wo<<<256, 768, 0, stream>>>(o, wo, x, h, hstats);
  RUNK(4) k4_in<<<224, 768, 0, stream>>>(h, w_in, hstats, z1c, z2f, ex, wcs);
  RUNK(5) k5_sp<<<256, 1024, 0, stream>>>(w_sp, z2f, b_sp, z1c, y);
  RUNK(6) k6_out<<<256, 768, 0, stream>>>(y, w_out, h, out);
}
```

```cpp
#include <hip/hip_runtime.h>
#include <hip/hip_bf16.h>
#include <stdint.h>

typedef __bf16 bf16_t;
typedef __attribute__((ext_vector_type(8))) __bf16 bf16x8;
typedef __attribute__((ext_vector_type(2))) __bf16 bf16x2;
typedef __attribute__((ext_vector_type(4))) short s16x4;
typedef __attribute__((ext_vector_type(8))) short s16x8;
typedef __attribute__((ext_vector_type(4))) float f32x4;
typedef __attribute__((ext_vector_type(2))) float f32x2;
typedef __attribute__((ext_vector_type(16))) float f32x16;
typedef __attribute__((ext_vector_type(4))) unsigned u32x4;
typedef __attribute__((ext_vector_type(2))) unsigned u32x2;
typedef unsigned long long u64;

#define DI __device__ __forceinline__

constexpr int S_LEN = 2048;
constexpr int D_MODEL = 512;
constexpr int FH = 896;

DI float rbf(float x) { return (float)(__bf16)x; }
DI unsigned pack2(float a, float b) {
  bf16x2 v = {(__bf16)a, (__bf16)b};
  return __builtin_bit_cast(unsigned, v);
}
DI float lo_f(unsigned u) { return __uint_as_float(u << 16); }
DI float hi_f(unsigned u) { return __uint_as_float(u & 0xffff0000u); }

#define ST2(p, v) (*(u32x2*)(p) = (v))
DI unsigned vtid() {
  return threadIdx.x < 256u ? threadIdx.x + 512u : (threadIdx.x < 768u ? threadIdx.x - 256u : threadIdx.x);
}
template <int N> DI void wait_vm() { asm volatile("s_waitcnt vmcnt(%0)" ::"n"(N) : "memory"); }

DI void glds16(const void* g, char* l) {
  __builtin_amdgcn_global_load_lds((const __attribute__((address_space(1))) void*)g,
                                   (__attribute__((address_space(3))) void*)l, 16, 0, 0);
}
DI s16x4 tr_read(const char* l) {
  return __builtin_amdgcn_ds_read_tr16_b64_v4i16((__attribute__((address_space(3))) s16x4*)l);
}

template <int V> struct IC { static constexpr int value = V; };
constexpr int cmin3(int a, int b, int c) { return a < b ? (a < c ? a : c) : (b < c ? b : c); }
template <int N, int I = 0, class F> DI void static_for(F& f) {
  if constexpr (I < N) {
    f(IC<I>{});
    static_for<N, I + 1>(f);
  }
}

template <int N, int PER, int I = 0, class F> DI void static_from(F& f, IC<I> = IC<I>{}) {
  if constexpr (I < N) {
    f(IC<I>{});
    static_from<N, PER, I + 1>(f);
  }
}
template <int N, int PER, class F> DI void rolled_for(F& f) {
  constexpr int NL = (PER > 0) ? (N - 4) / PER : 0;
  if constexpr (NL >= 2) {
    f(IC<0>{});
#pragma unroll 1
    for (int it = 0; it < NL; ++it) static_from<1 + PER, PER, 1>(f);
    static_from<N, PER, 1 + NL * PER>(f);
  } else {
    static_for<N>(f);
  }
}

DI int fragaddr(int m, int k, int K32) {
  return ((m >> 4) * K32 + (k >> 5)) * 512 + ((m & 15) + 16 * ((k & 31) >> 3)) * 8 + (k & 7);
}

DI int bimg(int k, int cc) {
  return k * 128 + ((((cc >> 2) & 1) ^ ((k >> 1) & 1)) << 6) + (((cc & 3) ^ (((k >> 3) & 1) << 1)) << 4);
}

#define SPB5 2
#define SPB6 2
#define SPB3 2
template <int BM, int BN, int WM, int WK, int WN, int KT, int NST>
struct GemmCfg {
  static constexpr int NW = WM * WN * WK;
  static constexpr int NL = (KT == 32) ? 8 : 4;
  static constexpr int PA = BM / 8, PB = BN / 8, P = PA + PB;
  static constexpr int PPL = (P + NL - 1) / NL;
  static constexpr int STAGE = (BM + BN) * 128;
  static constexpr int MF = BM / WM / 16;
  static constexpr int NFRAG = BN / 16;
  static constexpr int NFW = (NFRAG + WN - 1) / WN;
  static constexpr int LDS = STAGE * NST;
  static constexpr bool RESIDENT = NST >= KT;
  static constexpr int SPB = (KT == 32) ? SPB5 : (KT == 14) ? SPB6 : (BN == 64) ? SPB3 : 1;
  static constexpr int KS = KT / SPB;
  static constexpr int PRE = RESIDENT ? KS : NST / SPB - 1;
  static_assert(KT % SPB == 0 && NST % SPB == 0, "");
  static constexpr int PER = (NST / SPB == 3) ? 3 : 0;
};

struct NoHook {
  DI void operator()(const char*) const {}
};

template <int BM, int BN, int WM, int WK, int WN, int KT, int NST, bool AFRAG, bool BFRAG, int PW, class Hook>
DI void gemm_loader(char* smem, const bf16_t* __restrict__ A, int lda, const bf16_t* __restrict__ B, int ldb,
                    int pstride, int lw, int rot, Hook& hook) {
  using C = GemmCfg<BM, BN, WM, WK, WN, KT, NST>;
  constexpr int NL = C::NL, PA = C::PA, STAGE = C::STAGE, PRE = C::PRE;
  static_assert(PA % NL == 0, "A pieces must be a multiple of the loader count");
  static_assert(BFRAG || (BN % 64) == 0, "row-major B needs 64-column panels");
  const int lane = vtid() & 63;
  unsigned off[PW];
#pragma unroll
  for (int i = 0; i < PW; ++i) {
    if (i < PA / NL) {
      const int p = lw + i * NL;
      if constexpr (AFRAG) {
        off[i] = (unsigned)(((p >> 1) * lda + (p & 1)) * 1024 + lane * 16);
      } else {
        off[i] = (unsigned)((8 * p + (lane >> 3)) * lda * 2 + (((lane & 7) ^ (lane >> 3)) << 4));
      }
    } else {
      const int p = lw + i * NL - PA;
      if constexpr (BFRAG) {
        off[i] = (unsigned)(((p >> 1) * ldb + (p & 1)) * 1024 + lane * 16);
      } else {
        int k = (p & 7) * 8 + (lane >> 3);
        int cc = (lane & 7) ^ (((lane >> 4) & 1) << 2) ^ ((p & 1) << 1);
        off[i] = (unsigned)(k * ldb * 2 + (p >> 3) * pstride + cc * 16);
      }
    }
  }
  rot = 0;
  int kr = __builtin_amdgcn_readfirstlane(rot), kw = 0;
  auto issue = [&](int slot) {
    char* base = smem + slot * STAGE + lw * 1024;
    const int kk = kw + kr;
    const char* pa = (const char*)A + kk * (AFRAG ? 2048 : 128);
    const char* pb = (const char*)B + (size_t)kk * (BFRAG ? 2048 : 128 * ldb);
#pragma unroll
    for (int i = 0; i < PW; ++i) {
      unsigned o = off[i];
      asm volatile("" : "+v"(o));
      glds16((i < PA / NL ? pa : pb) + o, base + i * NL * 1024);
    }
    if constexpr (KT % 8 == 0) {
      kr = (kr + 1) & 7;
      if (kr == rot) kw += 8;
    } else {
      kr = (kr + 1 == KT) ? 0 : kr + 1;
    }
  };
  constexpr int SPB = C::SPB, KS = C::KS;
  auto body = [&](auto KTC) {
    constexpr int kt = decltype(KTC)::value;
    constexpr int I0 = (kt == 0) ? 0 : cmin3(KS, kt - 1 + PRE, 2 * kt);
    constexpr int I1 = cmin3(KS, kt + PRE, 2 * kt + 2);
#pragma unroll
    for (int u = I0 * SPB; u < I1 * SPB; ++u) issue(u % NST);
    wait_vm<(I1 - kt - 1) * PW * SPB>();
    __builtin_amdgcn_s_barrier();
    asm volatile("" ::: "memory");
#pragma unroll
    for (int u = 0; u < SPB; ++u) hook(smem + ((kt * SPB + u) % NST) * STAGE);
    __builtin_amdgcn_s_waitcnt(0xC07F);
  };
  rolled_for<KS, C::PER>(body);
}

struct NoPre {
  DI void operator()() const {}
};

template <int BM, int BN, int WM, int WK, int WN, int KT, int NST, bool AFRAG, bool BFRAG, bool CS, bool FULLN,
          bool SPLITN, int PLAIN, class Pre = NoPre>
DI void gemm_compute(char* smem, int wave,
                     f32x4 (&acc)[GemmCfg<BM, BN, WM, WK, WN, KT, NST>::MF][GemmCfg<BM, BN, WM, WK, WN, KT, NST>::NFW],
                     f32x4 (&cs)[GemmCfg<BM, BN, WM, WK, WN, KT, NST>::NFW], Pre pre = Pre()) {
  using C = GemmCfg<BM, BN, WM, WK, WN, KT, NST>;
  constexpr int STAGE = C::STAGE, MF = C::MF, NFW = C::NFW;
  constexpr int NF = FULLN ? NFW : NFW - 1;
  const int lane = vtid() & 63;
  const int wr = wave % WM, wk = (wave / WM) % WK, wc = wave / (WM * WK);
  const int g = lane >> 4, q = (lane & 15) >> 2, pp = lane & 3;
  constexpr int NKS = (WK == 2) ? 1 : 2;
  int aoff[NKS];
#pragma unroll
  for (int i = 0; i < NKS; ++i) {
    const int ks = (WK == 2) ? wk : i;
    if constexpr (AFRAG) aoff[i] = (wr * MF * 2 + ks) * 1024 + lane * 16;
    else aoff[i] = (wr * (MF * 16) + (lane & 15)) * 128 + (((4 * ks + g) ^ (lane & 7)) << 4);
  }
  int boff[NF];
#pragma unroll
  for (int nf = 0; nf < NF; ++nf) {
    const int f = SPLITN ? ((nf >> 1) * 4 + 2 * wc + (nf & 1)) : (wc * NFW + nf);
    if constexpr (BFRAG) {
      boff[nf] = BM * 128 + (f * 2 + ((WK == 2) ? wk : 0)) * 1024 + lane * 16;
    } else {
      const int kk = 8 * g + q + ((WK == 2) ? wk * 32 : 0);
      boff[nf] = BM * 128 + (f >> 2) * 8192 + bimg(kk, 2 * (f & 3) + (pp >> 1)) + 8 * (pp & 1);
    }
  }
  bf16x8 ones;
#pragma unroll
  for (int j = 0; j < 8; ++j) ones[j] = (__bf16)1.0f;

  constexpr int G = NKS * C::SPB, NSTEP = KT * NKS;
  static_assert(G <= 2, "");
  bf16x8 af[3][MF], bfr[3][NF];
  auto rd = [&](auto JC) {
    constexpr int j = decltype(JC)::value;
    constexpr int kt = j / NKS, i = j % NKS;
    const char* sb = smem + (kt % NST) * STAGE;
#pragma unroll
    for (int mf = 0; mf < MF; ++mf) af[j % 3][mf] = *(const bf16x8*)(sb + aoff[i] + mf * 2048);
#pragma unroll
    for (int nf = 0; nf < NF; ++nf) {
      if constexpr (BFRAG) {
        bfr[j % 3][nf] = *(const bf16x8*)(sb + boff[nf] + i * 1024);
      } else {
        s16x4 lo = tr_read(sb + boff[nf] + i * 4096);
        s16x4 hi = tr_read(sb + boff[nf] + i * 4096 + 512);
        s16x8 v = __builtin_shufflevector(lo, hi, 0, 1, 2, 3, 4, 5, 6, 7);
        bfr[j % 3][nf] = __builtin_bit_cast(bf16x8, v);
      }
    }
  };
  auto mm = [&](auto JC) {
    constexpr int jp = decltype(JC)::value;
#pragma unroll
    for (int nf = 0; nf < NF; ++nf) {
#pragma unroll
      for (int mf = 0; mf < MF; ++mf) {
        if ((PLAIN >> nf) & 1)
          acc[mf][nf] = __builtin_amdgcn_mfma_f32_16x16x32_bf16(af[jp % 3][mf], bfr[jp % 3][nf], acc[mf][nf], 0, 0, 0);
        else
          acc[mf][nf] = __builtin_amdgcn_mfma_f32_16x16x32_bf16(bfr[jp % 3][nf], af[jp % 3][mf], acc[mf][nf], 0, 0, 0);
      }
    }
  };
  auto csum = [&](auto JC) {
    constexpr int jp = decltype(JC)::value;
    if constexpr (CS) {
      if (wr == (jp % WM)) {
#pragma unroll
        for (int nf = 0; nf < NF; ++nf) {
          if ((PLAIN >> nf) & 1) cs[nf] = __builtin_amdgcn_mfma_f32_16x16x32_bf16(ones, bfr[jp % 3][nf], cs[nf], 0, 0, 0);
          else cs[nf] = __builtin_amdgcn_mfma_f32_16x16x32_bf16(bfr[jp % 3][nf], ones, cs[nf], 0, 0, 0);
        }
      }
    }
  };
  auto group = [&](auto GC) {
    constexpr int j0 = decltype(GC)::value * G;
    constexpr int NRD = G * (MF + (BFRAG ? NF : 2 * NF)), NMM = MF * NF;
    constexpr int RPM = (NRD + NMM - 1) / NMM;
    __builtin_amdgcn_s_barrier();
    asm volatile("" ::: "memory");
    if constexpr (decltype(GC)::value == NSTEP / G - 2) pre();
    rd(IC<j0>{});
    if constexpr (G == 2) rd(IC<j0 + 1>{});
    if constexpr (j0 > 0) {
      mm(IC<j0 - 1>{});
#pragma unroll
      for (int u = 0; u < NMM; ++u) {
        __builtin_amdgcn_sched_group_barrier(0x8, 1, 0);
        __builtin_amdgcn_sched_group_barrier(0x100, RPM, 0);
      }
      __builtin_amdgcn_sched_barrier(0);
      csum(IC<j0 - 1>{});
    }
    if constexpr (G == 2) {
      __builtin_amdgcn_sched_barrier(0);
      mm(IC<j0>{});
      csum(IC<j0>{});
    }
    __builtin_amdgcn_sched_barrier(0);
    __builtin_amdgcn_s_waitcnt(0xC07F);
  };
  rolled_for<NSTEP / G, C::PER>(group);
  mm(IC<NSTEP - 1>{});
  csum(IC<NSTEP - 1>{});
}

template <int BM, int BN, int WM, int WK, int WN, int KT, int NST, bool AFRAG, bool BFRAG, bool CS, bool SPLITN,
          int PLAIN, class Hook, class Pre = NoPre>
DI bool gemm_main(char* smem, const bf16_t* __restrict__ A, int lda, const bf16_t* __restrict__ B, int ldb, int pstride,
                  int rot,
                  f32x4 (&acc)[GemmCfg<BM, BN, WM, WK, WN, KT, NST>::MF][GemmCfg<BM, BN, WM, WK, WN, KT, NST>::NFW],
                  f32x4 (&cs)[GemmCfg<BM, BN, WM, WK, WN, KT, NST>::NFW], Hook& hook, Pre pre = Pre()) {
  using C = GemmCfg<BM, BN, WM, WK, WN, KT, NST>;
  const int wave = __builtin_amdgcn_readfirstlane(vtid() >> 6);
  if (wave >= C::NW) {
    const int lw = wave - C::NW;
    __builtin_amdgcn_s_setprio(3);
    constexpr bool EVEN = (C::P % C::NL) == 0;
    if (EVEN || lw < (C::P % C::NL))
      gemm_loader<BM, BN, WM, WK, WN, KT, NST, AFRAG, BFRAG, C::PPL>(smem, A, lda, B, ldb, pstride, lw, rot, hook);
    else if constexpr (!EVEN)
      gemm_loader<BM, BN, WM, WK, WN, KT, NST, AFRAG, BFRAG, C::PPL - 1>(smem, A, lda, B, ldb, pstride, lw, rot, hook);
    return false;
  }
  const int wc = wave / (WM * WK);
  constexpr bool ALLFULL = (C::NFRAG == WN * C::NFW);
  if (ALLFULL || wc != WN - 1)
    gemm_compute<BM, BN, WM, WK, WN, KT, NST, AFRAG, BFRAG, CS, true, SPLITN, PLAIN>(smem, wave, acc, cs, pre);
  else if constexpr (!ALLFULL)
    gemm_compute<BM, BN, WM, WK, WN, KT, NST, AFRAG, BFRAG, CS, false, SPLITN, PLAIN>(smem, wave, acc, cs, pre);
  return true;
}

template <int NFW>
DI void splitk_combine(char* smem, f32x4 (&acc)[2][NFW], f32x4 (&res)[NFW], int pairid, int wk, int lane, bool fulln) {
  __syncthreads();
  char* mine = smem + (pairid * 2 + wk) * (NFW * 1024) + lane * 16;
  char* theirs = smem + (pairid * 2 + (wk ^ 1)) * (NFW * 1024) + lane * 16;
#pragma unroll
  for (int nf = 0; nf < NFW; ++nf)
    if (nf < NFW - 1 || fulln) *(f32x4*)(mine + nf * 1024) = wk ? acc[0][nf] : acc[1][nf];
  __syncthreads();
#pragma unroll
  for (int nf = 0; nf < NFW; ++nf)
    if (nf < NFW - 1 || fulln) res[nf] = (wk ? acc[1][nf] : acc[0][nf]) + *(const f32x4*)(theirs + nf * 1024);
}

DI void chan_eq(float& mean, float& m2, float mean_b, float m2_b, float n) {
  float d = mean_b - mean;
  m2 = m2 + m2_b + d * d * (0.5f * n);
  mean = 0.5f * (mean + mean_b);
}

DI void ln_consts(float mean, float var, float& mean_b, float& rinv) {
  mean_b = rbf(mean);
  float v = rbf(var);
  float t = rbf(v + 1e-5f);
  float sd = rbf(sqrtf(t));
  rinv = 1.0f / sd;
}

DI float row16_sum(float v) {
  v += __builtin_bit_cast(float, __builtin_amdgcn_update_dpp(0, __builtin_bit_cast(int, v), 0x128, 0xf, 0xf, false));
  v += __builtin_bit_cast(float, __builtin_amdgcn_update_dpp(0, __builtin_bit_cast(int, v), 0x124, 0xf, 0xf, false));
  v += __builtin_bit_cast(float, __builtin_amdgcn_update_dpp(0, __builtin_bit_cast(int, v), 0x122, 0xf, 0xf, false));
  v += __builtin_bit_cast(float, __builtin_amdgcn_update_dpp(0, __builtin_bit_cast(int, v), 0x121, 0xf, 0xf, false));
  return v;
}

struct StatHook {
  float s1, s2;
  int o0;
  DI void operator()(const char* sb) {
    bf16x2 one2 = {(__bf16)1.0f, (__bf16)1.0f};
    u32x4 v0 = *(const u32x4*)(sb + o0);
    u32x4 v1 = *(const u32x4*)(sb + o0 + 16);
#pragma unroll
    for (int e = 0; e < 4; ++e) {
      const unsigned ua = v0[e], ub = v1[e];
      bf16x2 a = __builtin_bit_cast(bf16x2, ua), b = __builtin_bit_cast(bf16x2, ub);
      s1 = __builtin_amdgcn_fdot2_f32_bf16(a, one2, s1, false);
      s2 = __builtin_amdgcn_fdot2_f32_bf16(a, a, s2, false);
      s1 = __builtin_amdgcn_fdot2_f32_bf16(b, one2, s1, false);
      s2 = __builtin_amdgcn_fdot2_f32_bf16(b, b, s2, false);
    }
  }
};

__global__ __launch_bounds__(1024, 4) void k1_qkv(const bf16_t* __restrict__ x, const bf16_t* __restrict__ w,
                                                 bf16_t* __restrict__ qkf, bf16_t* __restrict__ v2,
                                                 u32x4* __restrict__ exz, const bf16_t* __restrict__ wo,
                                                 const bf16_t* __restrict__ win, float* __restrict__ wcs) {
  constexpr int BM = 64, BN = 192, WM = 2, WK = 1, WN = 4, KT = 8, NST = 4;
  using C = GemmCfg<BM, BN, WM, WK, WN, KT, NST>;
  __shared__ __attribute__((aligned(16))) char smem[C::LDS + 2048];
  if (vtid() < 64) exz[blockIdx.x * 64 + vtid()] = u32x4{0u, 0u, 0u, 0u};
  if (blockIdx.x == 0 && vtid() < 2) {
    const unsigned cv = vtid() ? 0u : 0x3f803f80u;
    exz[65536 + vtid()] = u32x4{cv, cv, cv, cv};
  }

  const int b = blockIdx.x, xcd = b & 7, idx = b >> 3;
  const int tm = xcd * 4 + (idx >> 3), tn = idx & 7;
  const int m0 = tm * BM, n0 = tn * BN;
  f32x4 acc[C::MF][C::NFW], cs[C::NFW];
#pragma unroll
  for (int i = 0; i < C::MF; ++i)
#pragma unroll
    for (int j = 0; j < C::NFW; ++j) acc[i][j] = f32x4{0.f, 0.f, 0.f, 0.f};
#pragma unroll
  for (int j = 0; j < C::NFW; ++j) cs[j] = f32x4{0.f, 0.f, 0.f, 0.f};
  StatHook hook;
  const int srow = (vtid() >> 2) & 63, sq = vtid() & 3;
  hook.s1 = 0.f; hook.s2 = 0.f;
  hook.o0 = srow * 128 + (sq << 5);
  const int lane = vtid() & 63, wave = __builtin_amdgcn_readfirstlane(vtid() >> 6), wr = wave & 1,
            wc = (wave >> 1) & 3;
  const int nw0 = n0 + wc * 48;
  const int plain = (nw0 >= 1024) ? 7 : ((nw0 + 16 >= 1024) ? 6 : 0);
  const bool comp = wave < 8;
  if (wave >= 12) {
#pragma unroll
    for (int kt = 0; kt < KT; ++kt) {
      __builtin_amdgcn_s_barrier();
      asm volatile("" ::: "memory");
      hook(smem + (kt % NST) * C::STAGE);
      __builtin_amdgcn_s_waitcnt(0xC07F);
    }
  } else if (wave >= 8) {
    __builtin_amdgcn_s_setprio(3);
    NoHook nh;
    gemm_loader<BM, BN, WM, WK, WN, KT, NST, false, false, C::PPL>(smem, x + (size_t)m0 * D_MODEL, D_MODEL, w + n0,
                                                                   3 * D_MODEL, 128, wave - 8, 0, nh);
    {
      const int lt = vtid() - 512;
      if (lt < 72) {
        const char* p0 = (lt < 16) ? (const char*)wo + (size_t)(b * 16 + lt) * 128
                                   : (const char*)win + (size_t)(b * 56 + lt - 16) * 128;
        unsigned v0 = *(const unsigned*)p0;
        asm volatile("" ::"v"(v0));
      }
      if (b < 28) {
        const bf16_t* wp = win + (size_t)((lt >> 3) * 16) * (2 * FH) + b * 64 + (lt & 7) * 8;
        float a[8];
#pragma unroll
        for (int e = 0; e < 8; ++e) a[e] = 0.f;
#pragma unroll
        for (int i = 0; i < 16; ++i) {
          const u32x4 v = *(const u32x4*)(wp + (size_t)i * (2 * FH));
#pragma unroll
          for (int e = 0; e < 4; ++e) { a[2 * e] += lo_f(v[e]); a[2 * e + 1] += hi_f(v[e]); }
        }
#pragma unroll
        for (int e = 0; e < 8; ++e) {
          a[e] += __shfl_xor(a[e], 8);
          a[e] += __shfl_xor(a[e], 16);
          a[e] += __shfl_xor(a[e], 32);
        }
        if ((lt & 63) < 8) {
          float* dp = wcs + (lt >> 6) * (2 * FH) + b * 64 + (lt & 7) * 8;
          *(f32x4*)dp = f32x4{a[0], a[1], a[2], a[3]};
          *(f32x4*)(dp + 4) = f32x4{a[4], a[5], a[6], a[7]};
        }
      }
    }
    return;
  } else if (plain == 7) {
    gemm_compute<BM, BN, WM, WK, WN, KT, NST, false, false, true, true, false, 7>(smem, wave, acc, cs);
  } else if (plain == 6) {
    gemm_compute<BM, BN, WM, WK, WN, KT, NST, false, false, true, true, false, 6>(smem, wave, acc, cs);
  } else {
    gemm_compute<BM, BN, WM, WK, WN, KT, NST, false, false, true, true, false, 0>(smem, wave, acc, cs);
  }

  float* tmean = (float*)(smem + C::LDS);
  float* trinv = tmean + 64;
  float* tcs = trinv + 64;
  if (!comp) {
    float s1 = hook.s1 + __shfl_xor(hook.s1, 1);
    float s2 = hook.s2 + __shfl_xor(hook.s2, 1);
    s1 += __shfl_xor(s1, 2);
    s2 += __shfl_xor(s2, 2);
    const float mean = s1 * (1.0f / 512.0f), ex2 = s2 * (1.0f / 512.0f);
    float var = ex2 - mean * mean;
    if (var < ex2 * (1.0f / 1024.0f)) {
      const bf16_t* xr = x + (size_t)(m0 + srow) * D_MODEL + sq * 128;
      float a2 = 0.f;
      for (int j = 0; j < 16; ++j) {
        u32x4 v = *(const u32x4*)(xr + j * 8);
#pragma unroll
        for (int e = 0; e < 4; ++e) {
          float d0 = lo_f(v[e]) - mean, d1 = hi_f(v[e]) - mean;
          a2 = fmaf(d0, d0, a2);
          a2 = fmaf(d1, d1, a2);
        }
      }
      a2 += __shfl_xor(a2, 1);
      a2 += __shfl_xor(a2, 2);
      var = a2 * (1.0f / 512.0f);
    }
    float mb, ri;
    ln_consts(mean, var, mb, ri);
    if (sq == 0) { tmean[srow] = mb; trinv[srow] = ri; }
    __syncthreads();
    return;
  }
  const int g = lane >> 4;
#pragma unroll
  for (int nf = 0; nf < C::NFW; ++nf) {
    if ((plain >> nf) & 1) {
      if (lane < 16) tcs[wr * 192 + wc * 48 + nf * 16 + lane] = cs[nf][0];
    } else if ((lane & 15) == 0) {
      *(f32x4*)(tcs + wr * 192 + wc * 48 + nf * 16 + 4 * g) = cs[nf];
    }
  }
  __syncthreads();
#pragma unroll
  for (int nf = 0; nf < C::NFW; ++nf) {
    if ((plain >> nf) & 1) {
      const int nl = wc * 48 + nf * 16 + (lane & 15);
      const float c = tcs[nl] + tcs[192 + nl];
      const int nv = n0 + nl - 1024;
#pragma unroll
      for (int mf = 0; mf < C::MF; ++mf) {
        const int ml = wr * 32 + mf * 16 + 4 * g;
        const f32x4 mb = *(const f32x4*)(tmean + ml), ri = *(const f32x4*)(trinv + ml);
        u32x2 o = {pack2((acc[mf][nf][0] - mb[0] * c) * ri[0], (acc[mf][nf][1] - mb[1] * c) * ri[1]),
                   pack2((acc[mf][nf][2] - mb[2] * c) * ri[2], (acc[mf][nf][3] - mb[3] * c) * ri[3])};
        const int m = m0 + ml;
        ST2(v2 + (((size_t)(nv >> 4) * 256 + (m >> 3)) * 16 + (nv & 15)) * 8 + (m & 7), o);
      }
    } else {
      const int nl = wc * 48 + nf * 16 + 4 * g;
      f32x4 c = *(const f32x4*)(tcs + nl) + *(const f32x4*)(tcs + 192 + nl);
      const int n = n0 + nl;
#pragma unroll
      for (int mf = 0; mf < C::MF; ++mf) {
        const int ml = wr * 32 + mf * 16 + (lane & 15);
        const float mb = tmean[ml], ri = trinv[ml];
        u32x2 o = {pack2((acc[mf][nf][0] - mb * c[0]) * ri, (acc[mf][nf][1] - mb * c[1]) * ri),
                   pack2((acc[mf][nf][2] - mb * c[2]) * ri, (acc[mf][nf][3] - mb * c[3]) * ri)};
        ST2(qkf + ((size_t)(n >> 3) * S_LEN + m0 + ml) * 8 + (n & 7), o);
      }
    }
  }
}

template <bool EDGE>
DI void attn_body(const bf16_t* __restrict__ qkf, const bf16_t* __restrict__ v2, const bf16_t* __restrict__ cst,
                  bf16_t* __restrict__ o, int qt, int h, const char* __restrict__ pf) {
  const int lane = threadIdx.x & 63;
  const int r = lane & 31, hf = lane >> 5;
  const int q0 = qt * 32;
  const int t0 = EDGE ? 8 - qt : 0;

  const bf16x8 qf = *(const bf16x8*)(qkf + ((size_t)(h * 2 + hf) * S_LEN + q0 + r) * 8);
  const int pr = 16 * ((r >> 2) & 1) + 4 * (r >> 3) + (r & 3);
  const bf16_t* kp = qkf + ((size_t)(64 + h * 2 + hf) * S_LEN + pr) * 8;
  bf16x8 kf[9];
#pragma unroll
  for (int t = 0; t < 9; ++t) {
    int kb = q0 - 256 + 32 * t;
    if (EDGE && kb < 0) kb = 0;
    kf[t] = *(const bf16x8*)(kp + kb * 8);
  }
  bf16x8 vf[9][2];
  const bf16_t* vp = (r < 16) ? v2 + ((size_t)h * 256 * 16 + 2 * hf * 16 + r) * 8 : cst + ((r == 16) ? 0 : 8);
  const int vm = (r < 16) ? 16 : 0;
#pragma unroll
  for (int t = 0; t < 9; ++t) {
    int kb = q0 - 256 + 32 * t;
    if (EDGE && kb < 0) kb = 0;
    vf[t][0] = *(const bf16x8*)(vp + kb * vm);
    vf[t][1] = *(const bf16x8*)(vp + (kb + 8) * vm);
  }
  const unsigned touch = *(const unsigned*)pf;
  f32x16 s[9];
  f32x16 zero;
#pragma unroll
  for (int i = 0; i < 16; ++i) zero[i] = 0.f;
#pragma unroll
  for (int t = 0; t < 9; ++t) s[t] = __builtin_amdgcn_mfma_f32_32x32x16_bf16(kf[t], qf, zero, 0, 0, 0);

  const float NEG = -3.0e38f;
#pragma unroll
  for (int i = 0; i < 16; ++i) {
    if (!(16 * hf + i > r)) s[0][i] = NEG;
    if (!(16 * hf + i <= r)) s[8][i] = NEG;
  }
  if constexpr (EDGE) {
#pragma unroll
    for (int t = 0; t < 8; ++t) {
      if (t < t0) {
#pragma unroll
        for (int i = 0; i < 16; ++i) s[t][i] = NEG;
      }
    }
  }
  float mx = NEG;
#pragma unroll
  for (int t = 0; t < 9; ++t)
#pragma unroll
    for (int i = 0; i < 16; ++i) mx = fmaxf(mx, s[t][i]);
  mx = fmaxf(mx, __shfl_xor(mx, 32));
  const float c = 0.25f * 1.4426950408889634f;
  const f32x2 c2 = {c, c}, mc2 = {-mx * c, -mx * c};
  f32x16 oacc = zero;
#pragma unroll
  for (int t = 0; t < 9; ++t) {
#pragma unroll
    for (int st = 0; st < 2; ++st) {
      bf16x8 pf;
#pragma unroll
      for (int j = 0; j < 8; j += 2) {
        f32x2 e = {s[t][8 * st + j], s[t][8 * st + j + 1]};
        e = __builtin_elementwise_fma(e, c2, mc2);
        pf[j] = (__bf16)__builtin_amdgcn_exp2f(e[0]);
        pf[j + 1] = (__bf16)__builtin_amdgcn_exp2f(e[1]);
      }
      oacc = __builtin_amdgcn_mfma_f32_32x32x16_bf16(vf[t][st], pf, oacc, 0, 0, 0);
    }
  }
  const float l = __shfl(oacc[8], r);
  const float il = 1.0f / l;
  bf16_t* op = o + fragaddr(q0 + r, h * 16 + 4 * hf, 16);
  u32x2 o0 = {pack2(oacc[0] * il, oacc[1] * il), pack2(oacc[2] * il, oacc[3] * il)};
  u32x2 o1 = {pack2(oacc[4] * il, oacc[5] * il), pack2(oacc[6] * il, oacc[7] * il)};
  ST2(op, o0);
  ST2(op + 128, o1);
  asm volatile("" ::"v"(touch));
}

__global__ __launch_bounds__(512) void k2_attn(const bf16_t* __restrict__ qkf, const bf16_t* __restrict__ v2,
                                               const bf16_t* __restrict__ cst, bf16_t* __restrict__ o,
                                               const bf16_t* __restrict__ wsp, const bf16_t* __restrict__ wout) {
  const int b = blockIdx.x, xcd = b & 7, idx = b >> 3;
  const int qt = xcd * 8 + (idx >> 2), hg = idx & 3;
  const int h = hg * 8 + (threadIdx.x >> 6);
  const int gl = b * 512 + threadIdx.x;
  const char* pf = (gl < 65536) ? (const char*)wsp + (size_t)gl * 128
                                : (const char*)wout + (size_t)((gl - 65536) % 7168) * 128;
  if (qt < 8) attn_body<true>(qkf, v2, cst, o, qt, h, pf);
  else attn_body<false>(qkf, v2, cst, o, qt, h, pf);
}

__global__ __launch_bounds__(768, 3) void k3_wo(const bf16_t* __restrict__ o, const bf16_t* __restrict__ w,
                                                const bf16_t* __restrict__ x, bf16_t* __restrict__ h,
                                                f32x2* __restrict__ stats) {
  constexpr int BM = 64, BN = 64, WM = 2, WK = 2, WN = 2, KT = 8, NST = 8;
  using C = GemmCfg<BM, BN, WM, WK, WN, KT, NST>;
  __shared__ __attribute__((aligned(16))) char smem[C::LDS + 8192];
  const int b = blockIdx.x, xcd = b & 7, idx = b >> 3;
  const int tm = xcd * 4 + (idx >> 3), tn = idx & 7;
  const int m0 = tm * BM, n0 = tn * BN;
  const int lane = vtid() & 63, wave = vtid() >> 6, wr = wave & 1, wk = (wave >> 1) & 1, wc = (wave >> 2) & 1;
  char* xs = smem + C::LDS;
  if (wave >= 8) {
    const int lw = __builtin_amdgcn_readfirstlane(wave) - 8;
#pragma unroll
    for (int i = 0; i < 2; ++i) {
      const int p = lw + 4 * i;
      glds16((const char*)(x + (size_t)(m0 + 8 * p + (lane >> 3)) * D_MODEL + n0) + (((lane & 7) ^ (lane >> 3)) << 4),
             xs + p * 1024);
    }
  }
  f32x4 acc[C::MF][C::NFW], cs[C::NFW], r[C::NFW];
#pragma unroll
  for (int i = 0; i < C::MF; ++i)
#pragma unroll
    for (int j = 0; j < C::NFW; ++j) acc[i][j] = f32x4{0.f, 0.f, 0.f, 0.f};
  NoHook hook;
  if (!gemm_main<BM, BN, WM, WK, WN, KT, NST, true, false, false, false, 0>(
          smem, o + (size_t)m0 * D_MODEL, 16, w + n0, D_MODEL, 128, (2 * (idx >> 3) + (idx & 7)) & 7, acc, cs, hook)) {
    return;
  }
  splitk_combine<2>(smem, acc, r, wr + 2 * wc, wk, lane, true);
  const int ml = wr * 32 + wk * 16 + (lane & 15);
  const int nl = wc * 32 + 4 * (lane >> 4);
  const u32x2 rv0 = *(const u32x2*)(xs + ml * 128 + (((nl >> 3) ^ (ml & 7)) << 4) + (nl & 4) * 2);
  const u32x2 rv1 = *(const u32x2*)(xs + ml * 128 + ((((nl + 16) >> 3) ^ (ml & 7)) << 4) + (nl & 4) * 2);
  float hv[8];
  hv[0] = rbf(lo_f(rv0[0]) + rbf(r[0][0])); hv[1] = rbf(hi_f(rv0[0]) + rbf(r[0][1]));
  hv[2] = rbf(lo_f(rv0[1]) + rbf(r[0][2])); hv[3] = rbf(hi_f(rv0[1]) + rbf(r[0][3]));
  hv[4] = rbf(lo_f(rv1[0]) + rbf(r[1][0])); hv[5] = rbf(hi_f(rv1[0]) + rbf(r[1][1]));
  hv[6] = rbf(lo_f(rv1[1]) + rbf(r[1][2])); hv[7] = rbf(hi_f(rv1[1]) + rbf(r[1][3]));
  u32x2 o0 = {pack2(hv[0], hv[1]), pack2(hv[2], hv[3])};
  u32x2 o1 = {pack2(hv[4], hv[5]), pack2(hv[6], hv[7])};
  bf16_t* hp = h + fragaddr(m0 + ml, n0 + nl, 16);
  ST2(hp, o0);
  ST2(hp + 256, o1);
  float mean = 0.f;
#pragma unroll
  for (int j = 0; j < 8; ++j) mean += hv[j];
  mean *= 0.125f;
  float m2 = 0.f;
#pragma unroll
  for (int j = 0; j < 8; ++j) { float d = hv[j] - mean; m2 = fmaf(d, d, m2); }
  chan_eq(mean, m2, __shfl_xor(mean, 16), __shfl_xor(m2, 16), 8.f);
  chan_eq(mean, m2, __shfl_xor(mean, 32), __shfl_xor(m2, 32), 16.f);
  if (lane < 16) stats[(size_t)(tn * 2 + wc) * S_LEN + m0 + ml] = f32x2{mean, m2};
}

DI f32x2 gelu2(f32x2 x) {
  const float k0 = -0.7978845608028654f * 2.0f * 1.4426950408889634f;
  const f32x2 one = {1.0f, 1.0f}, c1 = {0.044715f, 0.044715f}, kk = {k0, k0};
  f32x2 t = __builtin_elementwise_fma(x * x, c1, one);
  f32x2 y = (x * kk) * t;
  f32x2 e = {__builtin_amdgcn_exp2f(y[0]), __builtin_amdgcn_exp2f(y[1])};
  e = e + one;
  f32x2 r = {__builtin_amdgcn_rcpf(e[0]), __builtin_amdgcn_rcpf(e[1])};
  return x * r;
}
DI f32x2 rbf2(f32x2 x) {
  const unsigned u = pack2(x[0], x[1]);
  return f32x2{lo_f(u), hi_f(u)};
}

__global__ __launch_bounds__(768, 3) void k4_in(const bf16_t* __restrict__ h, const bf16_t* __restrict__ w,
                                                const f32x2* __restrict__ hstats, bf16_t* __restrict__ z1c,
                                                bf16_t* __restrict__ z2f, u64* __restrict__ ex,
                                                const float* __restrict__ wcs) {
  constexpr int BM = 128, BN = 128, WM = 4, WK = 1, WN = 2, KT = 8, NST = 4;
  using C = GemmCfg<BM, BN, WM, WK, WN, KT, NST>;
  __shared__ __attribute__((aligned(16))) char smem[C::LDS + 8192];
  const int b = blockIdx.x, xcd = b & 7, idx = b >> 3;
  const int tm = xcd * 2 + (idx >= 14 ? 1 : 0), tn = (idx >= 14) ? idx - 14 : idx;
  const int m0 = tm * BM, n0 = tn * 64;
  const int lane = vtid() & 63, wave = vtid() >> 6, wr = wave & 3, wc = (wave >> 2) & 1;
  const int g = lane >> 4;
  float* tmean = (float*)(smem + C::LDS);
  float* trinv = tmean + 128;
  float* tcs = trinv + 128;
  float* tmean2 = tcs + 512;
  float* trinv2 = tmean2 + 128;
  float* tpart = trinv2 + 128;
  f32x2* tpart2 = (f32x2*)(tpart + 512);
  f32x4 acc[C::MF][C::NFW], cs[C::NFW];
#pragma unroll
  for (int i = 0; i < C::MF; ++i)
#pragma unroll
    for (int j = 0; j < C::NFW; ++j) acc[i][j] = f32x4{0.f, 0.f, 0.f, 0.f};
#pragma unroll
  for (int j = 0; j < C::NFW; ++j) cs[j] = f32x4{0.f, 0.f, 0.f, 0.f};
  f32x2 hv[8];
  float cv[4];
  if (vtid() >= 512) {
    const f32x2* hp = hstats + (size_t)((vtid() >> 7) & 1) * 8 * S_LEN + m0 + (vtid() & 127);
#pragma unroll
    for (int j = 0; j < 8; ++j) hv[j] = hp[(size_t)j * S_LEN];
    const float* cp = wcs + n0 + (vtid() & 63) + ((vtid() >> 6) & 1) * FH;
#pragma unroll
    for (int j = 0; j < 4; ++j) cv[j] = cp[j * 2 * FH];
  }
  NoHook hook;
  const bool comp = gemm_main<BM, BN, WM, WK, WN, KT, NST, true, false, false, true, 12>(
      smem, h + (size_t)m0 * D_MODEL, 16, w + n0, 2 * FH, FH * 2, ((idx >= 14 ? 4 : 0) + tn) & 7, acc, cs, hook);
  if (!comp) {
    const int lt = vtid() - 512;
    float mean = 0.f;
#pragma unroll
    for (int j = 0; j < 8; ++j) mean += hv[j][0];
    mean *= 0.125f;
    float m2 = 0.f;
#pragma unroll
    for (int j = 0; j < 8; ++j) { float d = hv[j][0] - mean; m2 += hv[j][1] + 32.f * d * d; }
    if (lt >= 128) tpart2[lt - 128] = f32x2{mean, m2};
    else tcs[lt] = (cv[0] + cv[1]) + (cv[2] + cv[3]);
    __syncthreads();
    if (lt < 128) {
      const f32x2 o = tpart2[lt];
      chan_eq(mean, m2, o[0], o[1], 256.f);
      float mb, ri;
      ln_consts(mean, m2 * (1.0f / 512.0f), mb, ri);
      tmean[lt] = mb;
      trinv[lt] = ri;
    }
    __syncthreads();
    return;
  }
  __syncthreads();
  __syncthreads();
  u64* exp_ = ex + (size_t)tm * (14 * 128);
  {
    float c2[2];
#pragma unroll
    for (int nf = 0; nf < 2; ++nf) {
      const int nl = 64 + wc * 32 + nf * 16 + (lane & 15);
      c2[nf] = tcs[nl];
    }
#pragma unroll
    for (int mf = 0; mf < C::MF; ++mf) {
      const int ml = wr * 32 + mf * 16 + 4 * g;
      const f32x4 mb = *(const f32x4*)(tmean + ml), ri = *(const f32x4*)(trinv + ml);
      float pm = 0.f, pq = 0.f;
#pragma unroll
      for (int r = 0; r < 4; ++r) {
        const f32x2 u2 = (f32x2{acc[mf][2][r], acc[mf][3][r]} - f32x2{c2[0], c2[1]} * mb[r]) * ri[r];
        const f32x2 z2 = rbf2(gelu2(rbf2(u2)));
        const float a = z2[0], bq = z2[1];
        acc[mf][2][r] = a;
        acc[mf][3][r] = bq;
        const float mean = row16_sum(a + bq) * (1.0f / 32.0f);
        const float da = a - mean, db = bq - mean;
        const float m2 = row16_sum(fmaf(da, da, db * db));
        if ((lane & 3) == r) { pm = mean; pq = m2; }
      }
      if ((lane & 15) < 4) ((f32x2*)tpart)[wc * 128 + ml + (lane & 3)] = f32x2{pm, pq};
    }
  }
  __syncthreads();
  if (vtid() < 128) {
    f32x2 a = ((const f32x2*)tpart)[vtid()], bq = ((const f32x2*)tpart)[128 + vtid()];
    float mean = a[0], m2 = a[1];
    chan_eq(mean, m2, bq[0], bq[1], 32.f);
    __hip_atomic_store(exp_ + tn * 128 + vtid(),
                       ((u64)(__float_as_uint(m2) | 1u) << 32) | (u64)__float_as_uint(mean), __ATOMIC_RELAXED,
                       __HIP_MEMORY_SCOPE_AGENT);
  }
#pragma unroll
  for (int nf = 0; nf < 2; ++nf) {
    const int nl = wc * 32 + nf * 16 + 4 * g;
    const f32x4 c = *(const f32x4*)(tcs + nl);
#pragma unroll
    for (int mf = 0; mf < C::MF; ++mf) {
      const int ml = wr * 32 + mf * 16 + (lane & 15);
      const float mb = tmean[ml], ri = trinv[ml];
      const f32x2 ua = (f32x2{acc[mf][nf][0], acc[mf][nf][1]} - f32x2{c[0], c[1]} * mb) * ri;
      const f32x2 ub = (f32x2{acc[mf][nf][2], acc[mf][nf][3]} - f32x2{c[2], c[3]} * mb) * ri;
      const f32x2 va = gelu2(rbf2(ua)), vb = gelu2(rbf2(ub));
      u32x2 ov = {pack2(va[0], va[1]), pack2(vb[0], vb[1])};
      ST2(z1c + ((size_t)((m0 >> 4) + wr * 2 + mf) * 56 + (n0 >> 4) + wc * 2 + nf) * 256 + lane * 4, ov);
    }
  }
  if (vtid() < 256) {
    const int t = vtid(), row = t & 127, half = t >> 7;
    const u64* gp = exp_ + half * (7 * 128) + row;
    float vm[7], vq[7];
    for (unsigned spins = 0;; ++spins) {
      bool ok = true;
#pragma unroll
      for (int j = 0; j < 7; ++j) {
        u64 gg = __hip_atomic_load(gp + j * 128, __ATOMIC_RELAXED, __HIP_MEMORY_SCOPE_AGENT);
        vm[j] = __uint_as_float((unsigned)gg);
        vq[j] = __uint_as_float((unsigned)(gg >> 32));
        ok &= ((unsigned)(gg >> 32) != 0u);
      }
      if (__all(ok)) break;
      if (spins > 4000000u) break;
      __builtin_amdgcn_s_sleep(1);
    }
    float hm = 0.f, hq = 0.f;
#pragma unroll
    for (int j = 0; j < 7; ++j) { hm += vm[j]; hq += vq[j]; }
    hm *= (1.0f / 7.0f);
#pragma unroll
    for (int j = 0; j < 7; ++j) { float d = vm[j] - hm; hq = fmaf(64.f * d, d, hq); }
    tpart2[t] = f32x2{hm, hq};
  }
  __syncthreads();
  if (vtid() < 128) {
    f32x2 a = tpart2[vtid()], bq = tpart2[128 + vtid()];
    float mean = a[0], m2 = a[1];
    chan_eq(mean, m2, bq[0], bq[1], 448.f);
    float mb, ri;
    ln_consts(mean, m2 * (1.0f / 896.0f), mb, ri);
    tmean2[vtid()] = mb;
    trinv2[vtid()] = ri;
  }
  __syncthreads();
#pragma unroll
  for (int mf = 0; mf < C::MF; ++mf) {
    const int ml = wr * 32 + mf * 16 + 4 * g;
    const f32x4 mb = *(const f32x4*)(tmean2 + ml), ri = *(const f32x4*)(trinv2 + ml);
#pragma unroll
    for (int nf = 2; nf < 4; ++nf) {
      const int n = n0 + wc * 32 + (nf & 1) * 16 + (lane & 15);
      float v0 = rbf(acc[mf][nf][0] - mb[0]) * ri[0], v1 = rbf(acc[mf][nf][1] - mb[1]) * ri[1];
      float v2 = rbf(acc[mf][nf][2] - mb[2]) * ri[2], v3 = rbf(acc[mf][nf][3] - mb[3]) * ri[3];
      u32x2 ov = {pack2(v0, v1), pack2(v2, v3)};
      ST2(z2f + fragaddr(n, m0 + ml, 64), ov);
    }
  }
}

__global__ __launch_bounds__(1024, 4) void k5_sp(const bf16_t* __restrict__ wsp, const bf16_t* __restrict__ z2f,
                                                const bf16_t* __restrict__ bsp, const bf16_t* __restrict__ z1c,
                                                bf16_t* __restrict__ y) {
  constexpr int BM = 64, BN = 112, WM = 2, WK = 2, WN = 2, KT = 32, NST = (SPB5 == 2) ? 6 : 7;
  using C = GemmCfg<BM, BN, WM, WK, WN, KT, NST>;
  __shared__ __attribute__((aligned(16))) char smem[C::LDS];
  const int b = blockIdx.x, xcd = b & 7, idx = b >> 3;
  const int tm = (xcd >> 2) * 16 + (idx >> 1), tn = (xcd & 3) * 2 + (idx & 1);
  const int m0 = tm * BM, n0 = tn * BN;
  const int lane = vtid() & 63, wave = vtid() >> 6, wr = wave & 1, wk = (wave >> 1) & 1, wc = (wave >> 2) & 1;
  const bool fulln = (wc == 0);
  const int m = m0 + wr * 32 + wk * 16 + (lane & 15);
  const int nb = n0 + wc * 64;
  float bb = 0.f;
  u32x2 zv[4];
  auto pre = [&]() {
    bb = (float)bsp[m];
#pragma unroll
    for (int nf = 0; nf < 4; ++nf)
      if (nf < 3 || fulln) zv[nf] = *(const u32x2*)(z1c + ((size_t)(m >> 4) * 56 + (nb >> 4) + nf) * 256 + lane * 4);
  };
  f32x4 acc[C::MF][C::NFW], cs[C::NFW], r[C::NFW];
#pragma unroll
  for (int i = 0; i < C::MF; ++i)
#pragma unroll
    for (int j = 0; j < C::NFW; ++j) acc[i][j] = f32x4{0.f, 0.f, 0.f, 0.f};
  NoHook hook;
  if (!gemm_main<BM, BN, WM, WK, WN, KT, NST, false, true, false, false, 0>(
          smem, wsp + (size_t)m0 * S_LEN, S_LEN, z2f + (size_t)(n0 >> 4) * 64 * 512, 64, 0,
          (idx >> 1) & 7, acc, cs, hook, pre))
    return;
  splitk_combine<4>(smem, acc, r, wr + 2 * wc, wk, lane, fulln);
#pragma unroll
  for (int nf = 0; nf < C::NFW; ++nf) {
    if (nf < 3 || fulln) {
      float v0 = lo_f(zv[nf][0]) * rbf(rbf(r[nf][0]) + bb);
      float v1 = hi_f(zv[nf][0]) * rbf(rbf(r[nf][1]) + bb);
      float v2 = lo_f(zv[nf][1]) * rbf(rbf(r[nf][2]) + bb);
      float v3 = hi_f(zv[nf][1]) * rbf(rbf(r[nf][3]) + bb);
      u32x2 ov = {pack2(v0, v1), pack2(v2, v3)};
      ST2(y + fragaddr(m, nb + nf * 16 + 4 * (lane >> 4), 28), ov);
    }
  }
}

__global__ __launch_bounds__(768, 3) void k6_out(const bf16_t* __restrict__ y, const bf16_t* __restrict__ w,
                                                 const bf16_t* __restrict__ h, bf16_t* __restrict__ out) {
  constexpr int BM = 64, BN = 64, WM = 2, WK = 2, WN = 2, KT = 14, NST = (SPB6 == 2) ? 8 : 9;
  using C = GemmCfg<BM, BN, WM, WK, WN, KT, NST>;
  __shared__ __attribute__((aligned(16))) char smem[C::LDS + 8192];
  const int b = blockIdx.x, xcd = b & 7, idx = b >> 3;
  const int tm = xcd * 4 + (idx >> 3), tn = idx & 7;
  const int m0 = tm * BM, n0 = tn * BN;
  const int lane = vtid() & 63, wave = vtid() >> 6, wr = wave & 1, wk = (wave >> 1) & 1, wc = (wave >> 2) & 1;
  const int ml = wr * 32 + wk * 16 + (lane & 15);
  const int nl = wc * 32 + 4 * (lane >> 4);
  u32x2 rv0 = {0u, 0u}, rv1 = {0u, 0u};
  auto pre = [&]() {
    const bf16_t* hp = h + fragaddr(m0 + ml, n0 + nl, 16);
    rv0 = *(const u32x2*)(hp);
    rv1 = *(const u32x2*)(hp + 256);
  };
  f32x4 acc[C::MF][C::NFW], cs[C::NFW], r[C::NFW];
#pragma unroll
  for (int i = 0; i < C::MF; ++i)
#pragma unroll
    for (int j = 0; j < C::NFW; ++j) acc[i][j] = f32x4{0.f, 0.f, 0.f, 0.f};
  NoHook hook;
  if (!gemm_main<BM, BN, WM, WK, WN, KT, NST, true, false, false, false, 0>(
          smem, y + (size_t)(m0 >> 4) * 28 * 512, 28, w + n0, D_MODEL, 128, ((idx >> 3) * 3 + (idx & 7) * 2) % KT, acc, cs,
          hook, pre))
    return;
  splitk_combine<2>(smem, acc, r, wr + 2 * wc, wk, lane, true);
  u32x2 o0 = {pack2(lo_f(rv0[0]) + rbf(r[0][0]), hi_f(rv0[0]) + rbf(r[0][1])),
              pack2(lo_f(rv0[1]) + rbf(r[0][2]), hi_f(rv0[1]) + rbf(r[0][3]))};
  u32x2 o1 = {pack2(lo_f(rv1[0]) + rbf(r[1][0]), hi_f(rv1[0]) + rbf(r[1][1])),
              pack2(lo_f(rv1[1]) + rbf(r[1][2]), hi_f(rv1[1]) + rbf(r[1][3]))};
  char* os = smem + C::LDS;
  *(u32x2*)(os + ml * 128 + (((nl >> 3) ^ (ml & 7)) << 4) + (nl & 4) * 2) = o0;
  *(u32x2*)(os + ml * 128 + ((((nl + 16) >> 3) ^ (ml & 7)) << 4) + (nl & 4) * 2) = o1;
  __syncthreads();
  const int row = vtid() >> 3, ch = vtid() & 7;
  const u32x4 ov = *(const u32x4*)(os + row * 128 + ((ch ^ (row & 7)) << 4));
  *(u32x4*)(out + (size_t)(m0 + row) * D_MODEL + n0 + ch * 8) = ov;
}

#define RUNK(i)
extern "C" void kernel_launch(void* const* d_in, const int* in_sizes, int n_in, void* d_out, int out_size,
                              void* d_ws, size_t ws_size, hipStream_t stream) {
  const bf16_t* x = (const bf16_t*)d_in[0];
  const bf16_t* wqkv = (const bf16_t*)d_in[1];
  const bf16_t* wo = (const bf16_t*)d_in[2];
  const bf16_t* w_in = (const bf16_t*)d_in[4];
  const bf16_t* w_sp = (const bf16_t*)d_in[5];
  const bf16_t* b_sp = (const bf16_t*)d_in[6];
  const bf16_t* w_out = (const bf16_t*)d_in[7];
  bf16_t* out = (bf16_t*)d_out;

  char* ws = (char*)d_ws;
  size_t off = 0;
  auto take = [&](size_t bytes) { char* p = ws + off; off += (bytes + 255) & ~(size_t)255; return p; };
  bf16_t* qkf = (bf16_t*)take((size_t)S_LEN * 1024 * 2);
  bf16_t* v2 = (bf16_t*)take((size_t)512 * S_LEN * 2);
  bf16_t* o = (bf16_t*)take((size_t)S_LEN * D_MODEL * 2);
  bf16_t* h = (bf16_t*)take((size_t)S_LEN * D_MODEL * 2);
  f32x2* hstats = (f32x2*)take((size_t)S_LEN * 16 * 8);
  bf16_t* z1c = (bf16_t*)take((size_t)S_LEN * FH * 2);
  bf16_t* z2f = (bf16_t*)take((size_t)S_LEN * FH * 2);
  bf16_t* y = (bf16_t*)take((size_t)S_LEN * FH * 2);
  float* wcs = (float*)take((size_t)4 * 2 * FH * 4);
  u64* ex = (u64*)take((size_t)1024 * 1024 + 256);

  RUNK(1) k1_qkv<<<256, 1024, 0, stream>>>(x, wqkv, qkf, v2, (u32x4*)ex, wo, w_in, wcs);
  RUNK(2) k2_attn<<<256, 512, 0, stream>>>(qkf, v2, (const bf16_t*)(ex + 131072), o, w_sp, w_out);
  RUNK(3) k3_wo<<<256, 768, 0, stream>>>(o, wo, x, h, hstats);
  RUNK(4) k4_in<<<224, 768, 0, stream>>>(h, w_in, hstats, z1c, z2f, ex, wcs);
  RUNK(5) k5_sp<<<256, 1024, 0, stream>>>(w_sp, z2f, b_sp, z1c, y);
  RUNK(6) k6_out<<<256, 768, 0, stream>>>(y, w_out, h, out);
}
```
